```python
import torch

def _softmax(x, axis=-1):
    return torch.softmax(x, dim=axis)
def _gelu(x):
    return torch.nn.functional.gelu(x, approximate="tanh")
def _silu(x):
    return torch.nn.functional.silu(x)
def _sigmoid(x):
    return torch.sigmoid(x)
def _layernorm(x, axis=-1, eps=1e-5):
    m = x.mean(dim=axis, keepdim=True)
    v = x.var(dim=axis, keepdim=True, unbiased=False)
    return (x - m) / torch.sqrt(v + eps)
def _scatter_add(tgt, idx, src, axis=0):
    return tgt.clone().index_add_(axis, idx, src)
def _segment_sum(src, seg, n):
    out = torch.zeros((n,) + src.shape[1:], dtype=src.dtype)
    return out.index_add_(0, seg, src)
def _pad(x, pw):
    flat = tuple(v for pair in reversed(pw) for v in pair)
    return torch.nn.functional.pad(x, flat)

kernel_name = 'gated_mix__linear_attn_block__spectral_gfnet__prompt'

def setup_inputs():

    _w = (lambda a: (lambda t: t.to(torch.bfloat16) if t.is_floating_point() else t)(torch.as_tensor(a)))
    def _A_setup():
        B, S, D = 1, 2048, 640
        return {
            "x": _w((lambda *s: torch.randn(*s, dtype=torch.bfloat16) * 0.05)(B, S, D)),
            "wqkv": _w((lambda *s: torch.randn(*s, dtype=torch.bfloat16) * 0.4)(D, 3 * D)),
            "wo": _w((lambda *s: torch.randn(*s, dtype=torch.bfloat16) * 0.05)(D, D)),
        }
    _A_in = _A_setup()

    def _B_setup():
        B, S, D = 1, 2048, 640
        Df = D // 2 + 1
        return {
            "x": _w((lambda *s: torch.randn(*s, dtype=torch.bfloat16) * 0.05)(B, S, D)),
            "w_r": _w((lambda *s: torch.randn(*s, dtype=torch.bfloat16) * 1.6)(S, Df) * 0.02),
            "w_i": _w((lambda *s: torch.randn(*s, dtype=torch.bfloat16) * 1.6)(S, Df) * 0.02),
        }
    _B_in = _B_setup()

    return {
        "A_x": _A_in["x"],
        "A_wqkv": _A_in["wqkv"],
        "A_wo": _A_in["wo"],
        "B_x": _B_in["x"],
        "B_w_r": _B_in["w_r"],
        "B_w_i": _B_in["w_i"],
        "gate_w": _w((lambda *s: torch.randn(*s, dtype=torch.bfloat16) * 0.05)(640) * 16.0),
    }

def reference(A_x, A_wqkv, A_wo, B_x, B_w_r, B_w_i, gate_w):
    def _A_ref(x, wqkv, wo):
        B, S, D = x.shape
        H = 32; Dh = D // H
        qkv = (lambda x, *d: x.permute(*d))((x @ wqkv).reshape(B, S, 3, H, Dh), 2, 0, 3, 1, 4)
        q, k, v = qkv[0], qkv[1], qkv[2]
        phi = lambda t: torch.where(t > 0, t + 1, torch.exp(t)) + 1e-6
        pq, pk = phi(q), phi(k)
        kv = torch.einsum("bhsd,bhse->bhde", pk, v)
        z = torch.einsum("bhsd->bhd", pk)
        num = torch.einsum("bhqd,bhde->bhqe", pq, kv)
        den = torch.einsum("bhqd,bhd->bhq", pq, z)[..., None] + 1e-6
        o = num / den
        return (lambda x, *d: x.permute(*d))(o, 0, 2, 1, 3).reshape(B, S, D) @ wo

    def _B_ref(x, w_r, w_i):
        B, S, D = x.shape
        X = (lambda x, axes=(-2,-1): torch.fft.rfft2(x.float(), dim=axes))(x, axes=(-2, -1))
        W = w_r + 1j * w_i
        X = X * W
        y = (lambda x, s, axes=(-2,-1): torch.fft.irfft2(x, s=s, dim=axes).to(torch.bfloat16))(X, (S, D), axes=(-2, -1))
        return y

    x = A_x
    g = _sigmoid((x * gate_w).sum(-1, keepdims=True))
    a = _A_ref(x, A_wqkv, A_wo)
    b = _B_ref(x, B_w_r, B_w_i)
    return g * a + (1 - g) * b
```

```cpp
#include <hip/hip_runtime.h>
#include <stdint.h>

typedef unsigned short u16;
typedef unsigned int u32;
typedef short s16x4 __attribute__((ext_vector_type(4)));
typedef short s16x8 __attribute__((ext_vector_type(8)));
typedef __bf16 bf16x8 __attribute__((ext_vector_type(8)));
typedef __bf16 bf16x2 __attribute__((ext_vector_type(2)));
typedef float f32x4 __attribute__((ext_vector_type(4)));
typedef float f32x2 __attribute__((ext_vector_type(2)));
typedef u32 u32x4 __attribute__((ext_vector_type(4)));
typedef u32 u32x2 __attribute__((ext_vector_type(2)));

#define LDS_AS __attribute__((address_space(3)))
#define GLB_AS __attribute__((address_space(1)))
#define DI __device__ __forceinline__
#define GLDS16(g, l) __builtin_amdgcn_global_load_lds((const GLB_AS void*)(g), (LDS_AS void*)(l), 16, 0, 0)
#define WAITV(n) asm volatile("s_waitcnt vmcnt(" #n ")" ::: "memory")
#define RAWBAR() do { asm volatile("s_waitcnt lgkmcnt(0)" ::: "memory"); __builtin_amdgcn_s_barrier(); asm volatile("" ::: "memory"); } while (0)

constexpr size_t OFF_PQ  = 0;
constexpr size_t OFF_O2  = OFF_PQ + 2621440;
constexpr size_t OFF_KVP = OFF_O2 + 2621440;
constexpr size_t OFF_VB  = OFF_KVP + 860160;
constexpr int    VB_TS   = 10368;
constexpr size_t OFF_AP  = OFF_VB + (size_t)256 * VB_TS;
constexpr int    AP_TS   = 10240;
constexpr size_t OFF_WP  = OFF_AP + (size_t)256 * AP_TS;
constexpr size_t OFF_WOF = OFF_WP + (size_t)161 * 16384;
constexpr size_t OFF_G   = OFF_WOF + 819200;

constexpr double kPi = 3.14159265358979323846;
constexpr double tcos(double x) {
  double x2 = x * x, term = 1.0, s = 1.0;
  for (int i = 1; i < 14; ++i) { term *= -x2 / ((2 * i - 1) * (2 * i)); s += term; }
  return s;
}
constexpr double cos64(int k) {
  k &= 63; if (k > 32) k = 64 - k;
  if (k == 16) return 0.0;
  return (k < 16) ? tcos(2 * kPi * k / 64) : -tcos(2 * kPi * (32 - k) / 64);
}
constexpr u16 tobf(double v) {
  u32 u = __builtin_bit_cast(u32, (float)v);
  return (u16)((u + 0x7fffu + ((u >> 16) & 1u)) >> 16);
}
struct C64 { u16 c[64]; };
constexpr C64 mkC64() { C64 t{}; for (int i = 0; i < 64; ++i) t.c[i] = tobf(cos64(i)); return t; }
constexpr C64 kC64 = mkC64();
constexpr u16 gsv(int qp, int n1) {
  int i = qp >> 1;
  if (qp == 1) return kC64.c[(32 * n1) & 63];
  return (qp & 1) ? kC64.c[(i * n1 + 48) & 63] : kC64.c[(i * n1) & 63];
}
constexpr int kperm(int s2, int g, int j) { return 32 * s2 + (j < 4 ? 4 * g + j : 12 + 4 * g + j); }
struct T16 { u16 v[4096]; };
struct T32 { u32 v[4096]; };
constexpr T16 mkGsA() { T16 t{};
  for (int f = 0; f < 8; ++f) for (int l = 0; l < 64; ++l) for (int j = 0; j < 8; ++j)
    t.v[(f * 64 + l) * 8 + j] = gsv(16 * (f >> 1) + (l & 15), kperm(f & 1, l >> 4, j));
  return t; }
constexpr T16 mkGsB() { T16 t{};
  for (int f = 0; f < 8; ++f) for (int l = 0; l < 64; ++l) for (int j = 0; j < 8; ++j)
    t.v[(f * 64 + l) * 8 + j] = gsv(kperm(f & 1, l >> 4, j), 16 * (f >> 1) + (l & 15));
  return t; }
constexpr T32 mkF64() { T32 t{};
  for (int f = 0; f < 16; ++f) for (int l = 0; l < 64; ++l) for (int j = 0; j < 4; ++j) {
    int u = 16 * (f >> 2) + (l & 15), v = 4 * (4 * (f & 3) + (l >> 4)) + j;
    t.v[(f * 64 + l) * 4 + j] = (u32)kC64.c[(u * v) & 63] | ((u32)kC64.c[(u * v + 48) & 63] << 16);
  }
  return t; }
__device__ __attribute__((aligned(128))) const T16 dGsA = mkGsA();
__device__ __attribute__((aligned(128))) const T16 dGsB = mkGsB();
__device__ __attribute__((aligned(128))) const T32 dF64 = mkF64();

DI f32x4 mfma(u32x4 a, u32x4 b, f32x4 c) {
  return __builtin_amdgcn_mfma_f32_16x16x32_bf16(__builtin_bit_cast(bf16x8, a), __builtin_bit_cast(bf16x8, b), c, 0, 0, 0);
}
DI u32 pk2(float lo, float hi) { f32x2 v = {lo, hi}; return __builtin_bit_cast(u32, __builtin_convertvector(v, bf16x2)); }
DI float lo2f(u32 p) { return __uint_as_float(p << 16); }
DI float hi2f(u32 p) { return __uint_as_float(p & 0xffff0000u); }
DI float bfr(float x) { return lo2f(pk2(x, x)); }
DI u32x4 pkfrag(f32x4 a, f32x4 b) { u32x4 r = {pk2(a[0], a[1]), pk2(a[2], a[3]), pk2(b[0], b[1]), pk2(b[2], b[3])}; return r; }
DI float phi(float t) {
  float u = bfr(t > 0.f ? t + 1.f : __expf(t));
  return u + 1e-6f;
}
DI float ror8(float v) { return __int_as_float(__builtin_amdgcn_update_dpp(0, __float_as_int(v), 0x128, 0xf, 0xf, true)); }
DI u32x4 ldsr128(const char* smem, u32 off) { return *(const LDS_AS u32x4*)((const LDS_AS char*)smem + off); }
DI u32x2 ldstr(const char* smem, u32 off) {
  return __builtin_bit_cast(u32x2, __builtin_amdgcn_ds_read_tr16_b64_v4i16((LDS_AS s16x4*)((LDS_AS char*)smem + off)));
}
DI u32x4 onehot(u32 v, int pos) {
  u32 s = v << ((pos & 1) * 16); int d = pos >> 1;
  u32x4 r = {d == 0 ? s : 0u, d == 1 ? s : 0u, d == 2 ? s : 0u, d == 3 ? s : 0u};
  return r;
}

constexpr int K1_NS = 4, K1_SLOT = 32768;
constexpr int K1_GATE = K1_NS * K1_SLOT, K1_WST = K1_GATE + 2048, K1_LDS = K1_WST + 21504;

template <bool QT>
DI void k1_gemm(char* smem, const u16* __restrict__ x, const u16* __restrict__ wqkv, char* ws,
                int m, int nt, int rot, int w, int l, const u16* const (&ap)[4], const u16* const (&bp)[4]) {
  const int g = l >> 4, l15 = l & 15;
  auto issue = [&](int c) {
    int cc = c + rot; if (cc >= 10) cc -= 10;
    char* s = smem + (c & (K1_NS - 1)) * K1_SLOT + w * 4096;
#pragma unroll
    for (int i = 0; i < 4; ++i) GLDS16(ap[i] + cc * 64, s + i * 1024);
#pragma unroll
    for (int i = 0; i < 4; ++i) GLDS16(bp[i] + cc * (64 * 1920), s + 16384 + i * 1024);
  };
  u32 xa[2][2], wa[8][2];
#pragma unroll
  for (int rt = 0; rt < 2; ++rt)
#pragma unroll
    for (int ks = 0; ks < 2; ++ks) { int row = 32 * w + 16 * rt + l15; xa[rt][ks] = row * 128 + (((4 * ks + g) ^ ((row >> 1) & 7)) << 4); }
  const int ofs4 = QT ? 0 : (nt & 1);
#pragma unroll
  for (int ct = 0; ct < 8; ++ct)
#pragma unroll
    for (int t2 = 0; t2 < 2; ++t2) {
      int q4 = l15 >> 2, p = l & 3, krow = 8 * g + 4 * t2 + q4;
      int u = 4 * ct + p + ofs4, ch = (u >> 1) & 15, swz = (q4 << 2) | ((2 * g + t2) & 3);
      wa[ct][t2] = 16384 + krow * 256 + ((ch ^ swz) << 4) + 8 * (u & 1);
    }
  const int grow = 8 * nt + (l >> 3);
  const u32 ga = grow * 128 + (((l & 7) ^ ((grow >> 1) & 7)) << 4);
  float gsum = 0.f;
  f32x4 acc[2][8];
#pragma unroll
  for (int rt = 0; rt < 2; ++rt)
#pragma unroll
    for (int ct = 0; ct < 8; ++ct) acc[rt][ct] = f32x4{0.f, 0.f, 0.f, 0.f};

#pragma unroll 1
  for (int c = 0; c < 10; ++c) {
    if (c < 8) WAITV(16); else if (c == 8) WAITV(8); else WAITV(0);
    RAWBAR();
    if (c < 7) issue(c + 3);
    const char* s = smem + (c & (K1_NS - 1)) * K1_SLOT;
#pragma unroll
    for (int ks = 0; ks < 2; ++ks) {
      u32x4 xf[2], wf[8];
#pragma unroll
      for (int rt = 0; rt < 2; ++rt) xf[rt] = ldsr128(s, xa[rt][ks]);
#pragma unroll
      for (int ct = 0; ct < 8; ++ct) {
        u32x2 a = ldstr(s, wa[ct][0] + ks * 8192), b = ldstr(s, wa[ct][1] + ks * 8192);
        wf[ct] = u32x4{a[0], a[1], b[0], b[1]};
      }
#pragma unroll
      for (int ct = 0; ct < 8; ++ct)
#pragma unroll
        for (int rt = 0; rt < 2; ++rt)
          acc[rt][ct] = QT ? mfma(wf[ct], xf[rt], acc[rt][ct]) : mfma(xf[rt], wf[ct], acc[rt][ct]);
    }
    if (w == 3) {
      int cc = c + rot; if (cc >= 10) cc -= 10;
      u32x4 xv = ldsr128(s, ga), gv = ldsr128(smem, K1_GATE + cc * 128 + (l & 7) * 16);
#pragma unroll
      for (int i = 0; i < 4; ++i) {
        u32 pr = pk2(lo2f(xv[i]) * lo2f(gv[i]), hi2f(xv[i]) * hi2f(gv[i]));
        gsum += lo2f(pr); gsum += hi2f(pr);
      }
    }
  }
  if (w == 3) {
    gsum += __shfl_xor(gsum, 1); gsum += __shfl_xor(gsum, 2); gsum += __shfl_xor(gsum, 4);
    float z = bfr(gsum);
    float gt = bfr(1.f / (1.f + __expf(-z)));
    if ((l & 7) == 0) ((float*)(ws + OFF_G))[128 * m + grow] = gt;
  }
  if (QT) {
    u16* pq = (u16*)(ws + OFF_PQ);
#pragma unroll
    for (int ct = 0; ct < 8; ++ct) {
      int col = 128 * (nt - 11) + 16 * ct + 4 * g, hp = col / 40, cw = col - 40 * hp;
#pragma unroll
      for (int rt = 0; rt < 2; ++rt) {
        f32x4 a = acc[rt][ct];
        u32x2 o = {pk2(phi(bfr(a[0])), phi(bfr(a[1]))), pk2(phi(bfr(a[2])), phi(bfr(a[3])))};
        int sp = 128 * m + 32 * w + 16 * rt + l15;
        *(u32x2*)(pq + ((size_t)hp * 2048 + sp) * 40 + cw) = o;
      }
    }
  } else {
    u32x4 kf[4], vf[4];
#pragma unroll
    for (int ct = 0; ct < 4; ++ct) {
      f32x4 a = acc[0][ct], b = acc[1][ct];
#pragma unroll
      for (int i = 0; i < 4; ++i) { a[i] = phi(bfr(a[i])); b[i] = phi(bfr(b[i])); }
      kf[ct] = pkfrag(a, b);
      vf[ct] = pkfrag(acc[0][4 + ct], acc[1][4 + ct]);
    }
    if (l15 == 12) vf[3] = u32x4{0x3f803f80u, 0x3f803f80u, 0x3f803f80u, 0x3f803f80u};
    constexpr int PI[12] = {0, 0, 1, 1, 1, 2, 2, 2, 3, 3, 0, 1};
    constexpr int PJ[12] = {0, 1, 0, 1, 2, 1, 2, 3, 2, 3, 3, 3};
    RAWBAR();
#pragma unroll
    for (int P = 0; P < 12; ++P) {
      f32x4 r = mfma(kf[PI[P]], vf[PJ[P]], f32x4{0.f, 0.f, 0.f, 0.f});
      *(LDS_AS f32x4*)((LDS_AS char*)smem + ((w * 12 + P) * 64 + l) * 16) = r;
    }
    RAWBAR();
    float* kvp = (float*)(ws + OFF_KVP);
#pragma unroll
    for (int k = 0; k < 3; ++k) {
      int P = 3 * w + k;
      int pi = (0x103322211100ull >> (4 * P)) & 15, pj = (0x333232121010ull >> (4 * P)) & 15;
      f32x4 r = f32x4{0.f, 0.f, 0.f, 0.f};
#pragma unroll
      for (int ww = 0; ww < 4; ++ww) r += *(const LDS_AS f32x4*)((const LDS_AS char*)smem + ((ww * 12 + P) * 64 + l) * 16);
      int d = 16 * pi + 4 * g, e = 16 * pj + l15, hd = d / 20, he = e / 20;
      int h = 3 * nt + hd;
      bool ok = d < 60 && h < 32 && (e == 60 || (e < 60 && he == hd));
      int ee = e == 60 ? 20 : e - 20 * he;
      if (ok) *(f32x4*)(kvp + (((size_t)m * 32 + h) * 21 + ee) * 20 + (d - 20 * hd)) = r;
    }
  }
}

__global__ __launch_bounds__(256, 1) void k1(const u16* __restrict__ x, const u16* __restrict__ wqkv, const u16* __restrict__ wo,
                                             const u16* __restrict__ wr, const u16* __restrict__ wi, const u16* __restrict__ gate,
                                             char* __restrict__ ws) {
  extern __shared__ __attribute__((aligned(16))) char smem[];
  const int tid = threadIdx.x, l = tid & 63, w = __builtin_amdgcn_readfirstlane(tid >> 6);
  const int g = l >> 4, l15 = l & 15;
  const int b = blockIdx.x, xcd = b & 7, bi = b >> 3;
  const int m = (xcd >> 1) * 4 + (bi & 3), nt = (xcd & 1) * 8 + (bi >> 2);
  const int rot = (((nt & 7) * 5 >> 2) + ((m & 3) * 5 >> 1)) % 10;

  const int wq = b >> 2, wPP = b & 3;
  for (int ii = w; ii < 21; ii += 4) {
    int P = 64 * ii + l, row = P / 42, pc = P - 42 * row;
    int ks = wq + 64 * (8 * wPP + (row & 7)); if (row & 8) ks = (2048 - ks) & 2047;
    int a = ((ks * 642) & ~15) + pc * 16; a = a > 1314800 ? 1314800 : a;
    GLDS16((const char*)((row & 16) ? wi : wr) + a, smem + K1_WST + ii * 1024);
  }
  if (w < 2) { int e = w * 512 + l * 8; e = e > 632 ? 632 : e; GLDS16(gate + e, smem + K1_GATE + w * 1024); }

  const u16* ap[4]; const u16* bp[4];
#pragma unroll
  for (int i = 0; i < 4; ++i) {
    int r = 8 * (4 * w + i) + (l >> 3), ch = (l & 7) ^ ((r >> 1) & 7);
    ap[i] = x + (32 * (r & 63) + 2 * m + (r >> 6)) * 640 + ch * 8;
    int kr = 4 * (4 * w + i) + g, cb = l15 ^ (((kr & 3) << 2) | ((kr >> 2) & 3)), col;
    if (nt >= 11) col = 128 * (nt - 11) + 8 * cb;
    else { int c8 = cb & 7; if (nt == 10 && c8 > 4) c8 = 4; col = 640 * (1 + (cb >> 3)) + ((60 * nt) & ~7) + 8 * c8; }
    bp[i] = wqkv + kr * 1920 + col;
  }
#pragma unroll
  for (int c = 0; c < 3; ++c) {
    int cc = c + rot; if (cc >= 10) cc -= 10;
    char* s = smem + c * K1_SLOT + w * 4096;
#pragma unroll
    for (int i = 0; i < 4; ++i) GLDS16(ap[i] + cc * 64, s + i * 1024);
#pragma unroll
    for (int i = 0; i < 4; ++i) GLDS16(bp[i] + cc * (64 * 1920), s + 16384 + i * 1024);
  }

  if (w == 3 && l < 25) {
    int id = 25 * b + l, ko = id / 80, cb = id - 80 * ko;
    u32x4 in[8];
#pragma unroll
    for (int i = 0; i < 8; ++i) in[i] = *(const u32x4*)(wo + (8 * ko + i) * 640 + 8 * cb);
    int a = cb >> 3, bg = cb & 7, hp = ko / 5, wv = ko - 5 * hp;
    u32x4* dst = (u32x4*)(ws + OFF_WOF) + (((bg * 5 + (a >> 1)) * 20 + (hp >> 2) * 5 + wv) * 64 + (hp & 3) * 16 + (a & 1) * 8);
#pragma unroll
    for (int jj = 0; jj < 8; ++jj) {
      u32x4 o;
#pragma unroll
      for (int d = 0; d < 4; ++d) o[d] = __builtin_amdgcn_perm(in[2 * d + 1][jj >> 1], in[2 * d][jj >> 1], (jj & 1) ? 0x07060302u : 0x05040100u);
      dst[jj] = o;
    }
  }

  if (nt >= 10) {
    int grp = nt - 10, n2 = 2 * m + (grp >= 3), g3 = grp >= 3 ? grp - 3 : grp, bg = 3 * g3 + w;
    if (w < 3 && bg < 8) {
      u32x4 xf[4][3], gs[8];
#pragma unroll
      for (int rt = 0; rt < 4; ++rt)
#pragma unroll
        for (int s = 0; s < 3; ++s) {
          int a = 4 * s + g; a = a > 9 ? 9 : a;
          xf[rt][s] = *(const u32x4*)(x + (32 * (16 * rt + l15) + n2) * 640 + 64 * a + 8 * bg);
        }
#pragma unroll
      for (int f = 0; f < 8; ++f) gs[f] = *(const u32x4*)(dGsA.v + (f * 64 + l) * 8);
      const int h = (l >> 3) & 1, j = l & 7, bb = 8 * bg + j;
      char* vb = ws + OFF_VB + (size_t)(n2 * 8 + bg) * VB_TS;
      const float tc = h ? __builtin_amdgcn_cosf(bb * (1.f / 128.f)) : 1.f, ts = h ? __builtin_amdgcn_sinf(bb * (1.f / 128.f)) : 0.f;
#pragma unroll 1
      for (int c = 0; c < 5; ++c) {
        u32x4 kd[3];
#pragma unroll
        for (int s = 0; s < 3; ++s) {
          int a = 4 * s + g;
          float rev = c < 4 ? (float)((c + 1) * (64 * a + bb)) * (1.f / 640.f) - (h ? 0.25f : 0.f) : (h ? 0.5f * a : 0.f);
          float v = a < 10 ? __builtin_amdgcn_cosf(rev) : 0.f;
          kd[s] = onehot(pk2(v, v) & 0xffffu, j);
        }
        f32x4 t[4];
#pragma unroll
        for (int rt = 0; rt < 4; ++rt) {
          t[rt] = f32x4{0.f, 0.f, 0.f, 0.f};
#pragma unroll
          for (int s = 0; s < 3; ++s) t[rt] = mfma(xf[rt][s], kd[s], t[rt]);
        }
        u32x4 tb0 = pkfrag(t[0], t[1]), tb1 = pkfrag(t[2], t[3]);
#pragma unroll
        for (int mt = 0; mt < 4; ++mt) {
          f32x4 a2 = mfma(gs[2 * mt], tb0, f32x4{0.f, 0.f, 0.f, 0.f});
          a2 = mfma(gs[2 * mt + 1], tb1, a2);
#pragma unroll
          for (int rp = 0; rp < 2; ++rp) {
            float o0 = a2[2 * rp], o1 = a2[2 * rp + 1], re, im;
            const bool first = (mt == 0 && rp == 0) && g == 0;
            if (c < 4) {
              float p0 = ror8(o0), p1 = ror8(o1);
              re = h ? p0 + o1 : o0 - p1; im = h ? p1 - o0 : -(o1 + p0);
              if (mt == 0 && rp == 0) { if (first) { re = h ? p1 : o0; im = h ? -o1 : -p0; } }
            } else {
              float s1 = first ? 0.f : o1;
              re = o0 * tc - s1 * ts; im = -(o0 * ts + s1 * tc);
              if (mt == 0 && rp == 0) { if (first) *(u32*)(vb + 10240 + l * 4) = pk2(o1 * tc, -(o1 * ts)); }
            }
            *(u32*)(vb + ((c * 4 + mt) * 2 + rp) * 256 + l * 4) = pk2(re, im);
          }
        }
      }
    }
  }

  WAITV(0);
  RAWBAR();
  {
    const int t = l, ncomb = wq <= 32 ? 12 : 8;
    const int dd = (2 * wq) & 15, dm = (-2 * wq) & 15;
    for (int cb = w; cb < ncomb; cb += 4) {
      int rI = cb >> 1, P2 = cb & 1, r = rI < 4 ? rI + 1 : (rI == 4 ? 0 : 5);
      int kd = r + 10 * t; bool mir = kd > 320;
      int base = K1_WST + (mir ? 8 * 672 + dm + 2 * (640 - kd) : dd + 2 * kd) + P2 * 4 * 672;
      u32x4 o;
#pragma unroll
      for (int i = 0; i < 4; ++i) {
        u32 re = *(const LDS_AS u16*)((const LDS_AS char*)smem + base + i * 672);
        u32 im = *(const LDS_AS u16*)((const LDS_AS char*)smem + base + i * 672 + 16 * 672);
        o[i] = re | ((im ^ (mir ? 0x8000u : 0u)) << 16);
      }
      if (r == 0 && (t & 31) == 0) {
        int b2 = K1_WST + 8 * 672 + dm + 2 * kd + P2 * 4 * 672;
#pragma unroll
        for (int i = 0; i < 4; ++i) {
          u32 re = *(const LDS_AS u16*)((const LDS_AS char*)smem + b2 + i * 672);
          u32 im = *(const LDS_AS u16*)((const LDS_AS char*)smem + b2 + i * 672 + 16 * 672);
          o[i] = pk2(0.5f * (lo2f(o[i]) + lo2f(re)), 0.5f * (hi2f(o[i]) - lo2f(im)));
        }
      }
      int ci, half, c;
      if (rI < 4) { c = rI; half = wq >= 32; ci = wq < 32 ? wq : (64 - wq) & 31; }
      else { c = 4; half = rI - 4; ci = wq; }
      int sidx = ci == 32 ? 160 : ((c * 4 + (ci >> 3)) * 2 + (ci & 1)) * 4 + ((ci >> 1) & 3);
      int P = 2 * wPP + P2;
      *(u32x4*)(ws + OFF_WP + (size_t)sidx * 16384 + half * 8192 + (P >> 2) * 4096 + (t >> 4) * 1024 + ((P & 3) * 16 + (t & 15)) * 16) = o;
    }
  }

  if (nt >= 11) k1_gemm<true>(smem, x, wqkv, ws, m, nt, rot, w, l, ap, bp);
  else k1_gemm<false>(smem, x, wqkv, ws, m, nt, rot, w, l, ap, bp);
}

constexpr int K2_V = 0, K2_A = 16384, K2_U = 32768, K2_US = 272, K2_LDS = K2_U + 2 * 32 * K2_US;
constexpr int K2_NF = 168;

__global__ __launch_bounds__(512, 2) void k2(char* __restrict__ ws) {
  extern __shared__ __attribute__((aligned(16))) char smem[];
  const int tid = threadIdx.x, l = tid & 63, w8 = __builtin_amdgcn_readfirstlane(tid >> 6);
  const int g = l >> 4, l15 = l & 15;
  const int b = blockIdx.x;
  if (b < K2_NF) {
    const int sidx = (b & 7) * 21 + (b >> 3);
    if (sidx > 160) return;
    int c, ci;
    if (sidx == 160) { c = 4; ci = 32; }
    else { c = sidx >> 5; ci = ((sidx >> 3) & 3) * 8 + (sidx & 3) * 2 + ((sidx >> 2) & 1); }
    const int half = w8 >> 2, w4 = w8 & 3;
    const int q = c == 4 ? ci : (half == 0 ? ci : (ci == 0 ? 32 : 64 - ci));
#pragma unroll
    for (int i = 0; i < 2; ++i) {
      int row = 4 * (2 * w4 + i) + g, kc = l15 ^ (row & 15);
      GLDS16(ws + OFF_VB + (size_t)(row * 8 + (kc >> 1)) * VB_TS + sidx * 64 + half * 32 + (kc & 1) * 16,
             smem + K2_V + half * 8192 + (2 * w4 + i) * 1024);
    }
    u32x4 tr[4], ti[4], wf[2];
#pragma unroll
    for (int ks = 0; ks < 4; ++ks) tr[ks] = *(const u32x4*)(dF64.v + ((w4 * 4 + ks) * 64 + l) * 4);
#pragma unroll
    for (int mt = 0; mt < 2; ++mt) wf[mt] = *(const u32x4*)(ws + OFF_WP + (size_t)sidx * 16384 + half * 8192 + mt * 4096 + w4 * 1024 + l * 16);
    {
      float cs[8], sn[8];
#pragma unroll
      for (int j = 0; j < 8; ++j) {
        int ri = 16 * (w4 & 1) + l15, ki = j < 4 ? 4 * g + j : 12 + 4 * g + j;
        int n2 = w4 < 2 ? ki : ri, p = w4 < 2 ? ri : ki;
        float rev = (float)((n2 * (q + 64 * p)) & 2047) * (1.f / 2048.f);
        cs[j] = __builtin_amdgcn_cosf(rev); sn[j] = __builtin_amdgcn_sinf(rev);
      }
      u32x4 cf = {pk2(cs[0], cs[1]), pk2(cs[2], cs[3]), pk2(cs[4], cs[5]), pk2(cs[6], cs[7])};
      u32x4 sf = {pk2(sn[0], sn[1]), pk2(sn[2], sn[3]), pk2(sn[4], sn[5]), pk2(sn[6], sn[7])};
      *(LDS_AS u32x4*)((LDS_AS char*)smem + K2_A + half * 8192 + w4 * 2048 + l * 16) = cf;
      *(LDS_AS u32x4*)((LDS_AS char*)smem + K2_A + half * 8192 + w4 * 2048 + 1024 + l * 16) = sf;
    }
#pragma unroll
    for (int ks = 0; ks < 4; ++ks)
#pragma unroll
      for (int i = 0; i < 4; ++i) ti[ks][i] = __builtin_amdgcn_alignbit(tr[ks][i], tr[ks][i], 16) ^ 0x8000u;
    WAITV(0);
    RAWBAR();
    const f32x4 z4 = {0.f, 0.f, 0.f, 0.f};
    f32x4 Tr[2], Ti[2];
#pragma unroll
    for (int mt = 0; mt < 2; ++mt) {
      Tr[mt] = z4; Ti[mt] = z4;
#pragma unroll
      for (int ks = 0; ks < 4; ++ks) {
        u32x4 vf = ldsr128(smem, K2_V + half * 8192 + (16 * mt + l15) * 256 + (((4 * ks + g) ^ l15) << 4));
        Tr[mt] = mfma(vf, tr[ks], Tr[mt]); Ti[mt] = mfma(vf, ti[ks], Ti[mt]);
      }
    }
    u32x4 br = pkfrag(Tr[0], Tr[1]), bi = pkfrag(Ti[0], Ti[1]);
    f32x4 Yr[2], Yi[2];
#pragma unroll
    for (int mt = 0; mt < 2; ++mt) {
      u32x4 cf = ldsr128(smem, K2_A + half * 8192 + mt * 2048 + l * 16), sf = ldsr128(smem, K2_A + half * 8192 + mt * 2048 + 1024 + l * 16);
      u32x4 nf = sf ^ 0x80008000u;
      f32x4 zr = mfma(cf, br, z4); zr = mfma(sf, bi, zr);
      f32x4 zi = mfma(cf, bi, z4); zi = mfma(nf, br, zi);
#pragma unroll
      for (int i = 0; i < 4; ++i) {
        float a = lo2f(wf[mt][i]), bq = hi2f(wf[mt][i]);
        Yr[mt][i] = zr[i] * a - zi[i] * bq; Yi[mt][i] = zr[i] * bq + zi[i] * a;
      }
    }
    br = pkfrag(Yr[0], Yr[1]); bi = pkfrag(Yi[0], Yi[1]);
#pragma unroll
    for (int mt = 0; mt < 2; ++mt) {
      u32x4 cf = ldsr128(smem, K2_A + half * 8192 + (2 + mt) * 2048 + l * 16), sf = ldsr128(smem, K2_A + half * 8192 + (2 + mt) * 2048 + 1024 + l * 16);
      u32x4 nf = sf ^ 0x80008000u;
      f32x4 ur = mfma(cf, br, z4); ur = mfma(nf, bi, ur);
      f32x4 ui = mfma(cf, bi, z4); ui = mfma(sf, br, ui);
#pragma unroll
      for (int i = 0; i < 4; ++i)
        *(LDS_AS u32*)((LDS_AS char*)smem + K2_U + half * (32 * K2_US) + (16 * mt + 4 * g + i) * K2_US + (16 * w4 + l15) * 4) = pk2(ur[i], -ui[i]);
    }
    RAWBAR();
    f32x4 Rr[2], Ri[2];
#pragma unroll
    for (int cl = 0; cl < 2; ++cl) {
      Rr[cl] = z4; Ri[cl] = z4;
#pragma unroll
      for (int ks = 0; ks < 4; ++ks) {
        u32x4 uf = ldsr128(smem, K2_U + cl * (32 * K2_US) + (16 * half + l15) * K2_US + (4 * ks + g) * 16);
        Rr[cl] = mfma(tr[ks], uf, Rr[cl]); Ri[cl] = mfma(ti[ks], uf, Ri[cl]);
      }
    }
    const float sc = 2.f / (2048.f * 640.f);
    float e00[4], e01[4], e10[4], e11[4];
#pragma unroll
    for (int i = 0; i < 4; ++i) {
      float ar = Rr[0][i] * sc, ai = -Ri[0][i] * sc, brr = Rr[1][i] * sc, bii = -Ri[1][i] * sc;
      if (c < 4) {
        if (ci > 0) { e00[i] = ar + brr; e01[i] = -(ai + bii); e10[i] = bii - ai; e11[i] = brr - ar; }
        else { e00[i] = ar; e01[i] = -ai; e10[i] = brr; e11[i] = -bii; }
      } else {
        float rev = (float)(16 * w4 + 4 * g + i) * (1.f / 128.f);
        float tc = __builtin_amdgcn_cosf(rev), ts = __builtin_amdgcn_sinf(rev);
        float xr = brr * tc - bii * ts, xi = brr * ts + bii * tc;
        if (ci & 31) { e00[i] = ar; e01[i] = xr; e10[i] = -ai; e11[i] = -xi; }
        else { e00[i] = e10[i] = 0.5f * ar; e01[i] = e11[i] = 0.5f * xr; }
      }
    }
    u32x4 r0 = {pk2(e00[0], e00[1]), pk2(e00[2], e00[3]), pk2(e01[0], e01[1]), pk2(e01[2], e01[3])};
    u32x4 r1 = {pk2(e10[0], e10[1]), pk2(e10[2], e10[3]), pk2(e11[0], e11[1]), pk2(e11[2], e11[3])};
    const int so = sidx == 160 ? 128 : sidx;
    char* dst = ws + OFF_AP + (size_t)((16 * half + l15) * 8 + 2 * w4 + (g >> 1)) * AP_TS + so * 64 + (g & 1) * 32;
    if (sidx != 160) *(u32x4*)dst = r0;
    if (sidx != 128) *(u32x4*)(dst + 16) = r1;
  } else {
    const int ob = b - K2_NF, hp = ob & 15, rb = ob >> 4;
    const u16* pq = (const u16*)(ws + OFF_PQ) + (size_t)hp * 2048 * 40;
    u32x4 pf[2][2];
#pragma unroll
    for (int rt = 0; rt < 2; ++rt)
#pragma unroll
      for (int u = 0; u < 2; ++u) {
        int sp = 256 * rb + 32 * w8 + 16 * rt + l15;
        const u32* p = (const u32*)(pq + sp * 40 + 20 * u + 8 * (g > 2 ? 2 : g));
        u32x2 a = *(const u32x2*)p, bq = *(const u32x2*)(p + (g < 2 ? 2 : 0));
        pf[rt][u] = u32x4{g < 3 ? a[0] : 0u, g < 3 ? a[1] : 0u, g < 2 ? bq[0] : 0u, g < 2 ? bq[1] : 0u};
      }
    if (tid < 210) {
      const f32x4* kvp = (const f32x4*)(ws + OFF_KVP) + hp * 210 + tid;
      f32x4 s = kvp[0];
#pragma unroll
      for (int mm = 1; mm < 16; ++mm) s += kvp[mm * 3360];
      int u = tid >= 105, in = tid - 105 * u, e = in / 5, dq = in - 5 * e;
      u32x2 o = {pk2(s[0], s[1]), pk2(s[2], s[3])};
      *(LDS_AS u32x2*)((LDS_AS char*)smem + u * 1024 + e * 48 + dq * 8) = o;
    }
    __syncthreads();
    u16* o2 = (u16*)(ws + OFF_O2) + (size_t)hp * 2048 * 40;
    const f32x4 z4 = {0.f, 0.f, 0.f, 0.f};
#pragma unroll
    for (int u = 0; u < 2; ++u) {
      u32x4 af[3];
#pragma unroll
      for (int tI = 0; tI < 3; ++tI) {
        int e = tI == 0 ? l15 : (tI == 1 ? 16 + (l15 & 3) : 20);
        u32x4 v = ldsr128(smem, u * 1024 + e * 48 + (g > 2 ? 2 : g) * 16);
        af[tI] = u32x4{g < 3 ? v[0] : 0u, g < 3 ? v[1] : 0u, g < 2 ? v[2] : 0u, g < 2 ? v[3] : 0u};
      }
#pragma unroll
      for (int rt = 0; rt < 2; ++rt) {
        f32x4 n0 = mfma(af[0], pf[rt][u], z4), n1 = mfma(af[1], pf[rt][u], z4), dn = mfma(af[2], pf[rt][u], z4);
        float rd = 1.f / bfr(bfr(dn[0]) + 1e-6f);
        int sp = 256 * rb + 32 * w8 + 16 * rt + l15;
        u32x2 a = {pk2(bfr(n0[0]) * rd, bfr(n0[1]) * rd), pk2(bfr(n0[2]) * rd, bfr(n0[3]) * rd)};
        u32x2 bq = {pk2(bfr(n1[0]) * rd, bfr(n1[1]) * rd), pk2(bfr(n1[2]) * rd, bfr(n1[3]) * rd)};
        *(u32x2*)(o2 + sp * 40 + 20 * u + 4 * g) = a;
        if (g == 0) *(u32x2*)(o2 + sp * 40 + 20 * u + 16) = bq;
      }
    }
  }
}

constexpr int K3_LDS = 81920;

__global__ __launch_bounds__(320, 1) void k3(const char* __restrict__ ws, u16* __restrict__ out) {
  extern __shared__ __attribute__((aligned(16))) char smem[];
  const int tid = threadIdx.x, l = tid & 63, ct = __builtin_amdgcn_readfirstlane(tid >> 6);
  const int g = l >> 4, l15 = l & 15;
  const int b = blockIdx.x, n2 = 4 * (b & 7) + ((b >> 3) & 3), bg = b >> 5;
#pragma unroll
  for (int i = 0; i < 16; ++i)
    GLDS16(ws + OFF_O2 + ((size_t)i * 2048 + 64 * n2) * 80 + ct * 1024 + l * 16, smem + i * 5120 + ct * 1024);
  u32x4 wf[20], af[4][3], gs[8];
  float gt[4];
  const char* ap = ws + OFF_AP + (size_t)(n2 * 8 + bg) * AP_TS;
#pragma unroll
  for (int rt = 0; rt < 4; ++rt)
#pragma unroll
    for (int s = 0; s < 3; ++s) {
      int kc = 4 * s + g; kc = kc > 9 ? 9 : kc;
      int i = 8 * rt + (l15 >> 1);
      af[rt][s] = *(const u32x4*)(ap + ((((kc >> 1) * 4 + rt) * 2 + (i & 1)) * 4 + ((i >> 1) & 3)) * 64 + (kc & 1) * 32 + (l15 & 1) * 16);
    }
#pragma unroll
  for (int f = 0; f < 8; ++f) gs[f] = *(const u32x4*)(dGsB.v + (f * 64 + l) * 8);
#pragma unroll
  for (int rt = 0; rt < 4; ++rt) gt[rt] = ((const float*)(ws + OFF_G))[64 * n2 + 16 * rt + l15];
#pragma unroll
  for (int ks = 0; ks < 20; ++ks) wf[ks] = *(const u32x4*)(ws + OFF_WOF + ((size_t)((bg * 5 + ct) * 20 + ks) * 64 + l) * 16);
  u32x4 kd[3];
  {
    const int a = 2 * ct + (l15 >> 3), j = l15 & 7, d = 64 * a + 8 * bg + j;
#pragma unroll
    for (int s = 0; s < 3; ++s) {
      int kc = 4 * s + g, c = kc >> 1;
      float rev = (float)((c + 1) * d) * (1.f / 640.f);
      float v0 = c < 4 ? __builtin_amdgcn_cosf(rev) : 1.f, v1 = c < 4 ? __builtin_amdgcn_sinf(rev) : ((a & 1) ? -1.f : 1.f);
      bool on = kc < 10 && (kc & 1) == (j >> 2);
      u32 p0 = on ? (pk2(v0, v0) & 0xffffu) << ((j & 1) * 16) : 0u, p1 = on ? (pk2(v1, v1) & 0xffffu) << ((j & 1) * 16) : 0u;
      kd[s] = (j & 2) ? u32x4{0u, p0, 0u, p1} : u32x4{p0, 0u, p1, 0u};
    }
  }
  const f32x4 z4 = {0.f, 0.f, 0.f, 0.f};
  f32x4 yb[4];
  {
    f32x4 P[4];
#pragma unroll
    for (int rt = 0; rt < 4; ++rt) {
      P[rt] = z4;
#pragma unroll
      for (int s = 0; s < 3; ++s) P[rt] = mfma(af[rt][s], kd[s], P[rt]);
    }
    u32x4 p0 = pkfrag(P[0], P[1]), p1 = pkfrag(P[2], P[3]);
#pragma unroll
    for (int nt = 0; nt < 4; ++nt) { yb[nt] = mfma(p0, gs[2 * nt], z4); yb[nt] = mfma(p1, gs[2 * nt + 1], yb[nt]); }
  }
  WAITV(0);
  RAWBAR();
  f32x4 acc[4] = {z4, z4, z4, z4};
#pragma unroll
  for (int ks = 0; ks < 20; ++ks) {
    const int Q = ks / 5, wv = ks % 5;
#pragma unroll
    for (int rt = 0; rt < 4; ++rt) {
      u32x4 of = ldsr128(smem, (4 * Q + g) * 5120 + (16 * rt + l15) * 80 + wv * 16);
      acc[rt] = mfma(wf[ks], of, acc[rt]);
    }
    if (ks & 1) __builtin_amdgcn_sched_barrier(0);
  }
#pragma unroll
  for (int rt = 0; rt < 4; ++rt) {
    float gg = gt[rt], hh = bfr(1.f - gg), o[4];
#pragma unroll
    for (int i = 0; i < 4; ++i) o[i] = bfr(gg * bfr(acc[rt][i])) + bfr(hh * bfr(yb[rt][i]));
    u32x2 v = {pk2(o[0], o[1]), pk2(o[2], o[3])};
    *(u32x2*)(out + (32 * (16 * rt + l15) + n2) * 640 + 64 * (2 * ct + (g >> 1)) + 8 * bg + 4 * (g & 1)) = v;
  }
}

extern "C" void kernel_launch(void* const* d_in, const int* in_sizes, int n_in, void* d_out, int out_size, void* d_ws,
                              size_t ws_size, hipStream_t stream) {
  const u16* x = (const u16*)d_in[0];
  const u16* wqkv = (const u16*)d_in[1];
  const u16* wo = (const u16*)d_in[2];
  const u16* wr = (const u16*)d_in[4];
  const u16* wi = (const u16*)d_in[5];
  const u16* gate = (const u16*)d_in[6];
  char* ws = (char*)d_ws;
  (void)hipFuncSetAttribute((const void*)k1, hipFuncAttributeMaxDynamicSharedMemorySize, K1_LDS);
  (void)hipFuncSetAttribute((const void*)k3, hipFuncAttributeMaxDynamicSharedMemorySize, K3_LDS);
  k1<<<256, 256, K1_LDS, stream>>>(x, wqkv, wo, wr, wi, gate, ws);
  k2<<<K2_NF + 128, 512, K2_LDS, stream>>>(ws);
  k3<<<256, 320, K3_LDS, stream>>>(ws, (u16*)d_out);
}
```

```cpp
#include <hip/hip_runtime.h>
#include <stdint.h>

typedef unsigned short u16;
typedef unsigned int u32;
typedef short s16x4 __attribute__((ext_vector_type(4)));
typedef short s16x8 __attribute__((ext_vector_type(8)));
typedef __bf16 bf16x8 __attribute__((ext_vector_type(8)));
typedef __bf16 bf16x2 __attribute__((ext_vector_type(2)));
typedef float f32x4 __attribute__((ext_vector_type(4)));
typedef float f32x2 __attribute__((ext_vector_type(2)));
typedef u32 u32x4 __attribute__((ext_vector_type(4)));
typedef u32 u32x2 __attribute__((ext_vector_type(2)));

#define LDS_AS __attribute__((address_space(3)))
#define GLB_AS __attribute__((address_space(1)))
#define DI __device__ __forceinline__
#define STAMP_DECL
#define STAMP()
#define STAMP_FLUSH(K)
__device__ __forceinline__ void dma1(u32 lds, const void* base, u32 v0) {
  u32 keep;
  asm volatile("s_mov_b32 %0, m0\n\ts_mov_b32 m0, %1\n\ts_nop 4\n\tglobal_load_lds_dwordx4 %2, %3\n\ts_mov_b32 m0, %0"
               : "=&s"(keep) : "s"(lds), "v"(v0), "s"(base) : "memory");
}
__device__ __forceinline__ void dma4(u32 lds, const void* base, u32 v0, u32 v1, u32 v2, u32 v3) {
  u32 keep;
  asm volatile("s_mov_b32 %0, m0\n\ts_mov_b32 m0, %1\n\ts_nop 4\n\tglobal_load_lds_dwordx4 %2, %6\n\t"
               "global_load_lds_dwordx4 %3, %6 offset:1024\n\tglobal_load_lds_dwordx4 %4, %6 offset:2048\n\t"
               "global_load_lds_dwordx4 %5, %6 offset:3072\n\ts_mov_b32 m0, %0"
               : "=&s"(keep) : "s"(lds), "v"(v0), "v"(v1), "v"(v2), "v"(v3), "s"(base) : "memory");
}
__device__ __forceinline__ void dma1v(u32 lds, const void* src) {
  u32 keep;
  asm volatile("s_mov_b32 %0, m0\n\ts_mov_b32 m0, %1\n\ts_nop 0\n\tglobal_load_lds_dwordx4 %2, off\n\ts_mov_b32 m0, %0"
               : "=&s"(keep) : "s"(lds), "v"(src) : "memory");
}
#define LDSADDR(p) ((u32)(uintptr_t)(LDS_AS char*)(p))
#define WAITV(n) asm volatile("s_waitcnt vmcnt(" #n ")" ::: "memory")
#define RAWBAR() do { asm volatile("s_waitcnt lgkmcnt(0)" ::: "memory"); __builtin_amdgcn_s_barrier(); asm volatile("" ::: "memory"); } while (0)

constexpr size_t OFF_PQ  = 0;
constexpr size_t OFF_O2  = OFF_PQ + 2621440;
constexpr size_t OFF_KVP = OFF_O2 + 2621440;
constexpr size_t OFF_VB  = OFF_KVP + 860160;
constexpr int    VB_TS   = 10368;
constexpr size_t OFF_AP  = OFF_VB + (size_t)256 * VB_TS;
constexpr int    AP_TS   = 10240;
constexpr size_t OFF_WP  = OFF_AP + (size_t)256 * AP_TS;
constexpr size_t OFF_WOF = OFF_WP + (size_t)161 * 16384;
constexpr size_t OFF_G   = OFF_WOF + 819200;

constexpr double kPi = 3.14159265358979323846;
constexpr double tcos(double x) {
  double x2 = x * x, term = 1.0, s = 1.0;
  for (int i = 1; i < 14; ++i) { term *= -x2 / ((2 * i - 1) * (2 * i)); s += term; }
  return s;
}
constexpr double cos64(int k) {
  k &= 63; if (k > 32) k = 64 - k;
  if (k == 16) return 0.0;
  return (k < 16) ? tcos(2 * kPi * k / 64) : -tcos(2 * kPi * (32 - k) / 64);
}
constexpr u16 tobf(double v) {
  u32 u = __builtin_bit_cast(u32, (float)v);
  return (u16)((u + 0x7fffu + ((u >> 16) & 1u)) >> 16);
}
struct C64 { u16 c[64]; };
constexpr C64 mkC64() { C64 t{}; for (int i = 0; i < 64; ++i) t.c[i] = tobf(cos64(i)); return t; }
constexpr C64 kC64 = mkC64();
constexpr u16 gsv(int qp, int n1) {
  int i = qp >> 1;
  if (qp == 1) return kC64.c[(32 * n1) & 63];
  return (qp & 1) ? kC64.c[(i * n1 + 48) & 63] : kC64.c[(i * n1) & 63];
}
constexpr int kperm(int s2, int g, int j) { return 32 * s2 + (j < 4 ? 4 * g + j : 12 + 4 * g + j); }
struct T16 { u16 v[4096]; };
struct T32 { u32 v[4096]; };
constexpr T16 mkGsA() { T16 t{};
  for (int f = 0; f < 8; ++f) for (int l = 0; l < 64; ++l) for (int j = 0; j < 8; ++j)
    t.v[(f * 64 + l) * 8 + j] = gsv(16 * (f >> 1) + (l & 15), kperm(f & 1, l >> 4, j));
  return t; }
constexpr T16 mkGsB() { T16 t{};
  for (int f = 0; f < 8; ++f) for (int l = 0; l < 64; ++l) for (int j = 0; j < 8; ++j)
    t.v[(f * 64 + l) * 8 + j] = gsv(kperm(f & 1, l >> 4, j), 16 * (f >> 1) + (l & 15));
  return t; }
constexpr T32 mkF64() { T32 t{};
  for (int f = 0; f < 16; ++f) for (int l = 0; l < 64; ++l) for (int j = 0; j < 4; ++j) {
    int u = 16 * (f >> 2) + (l & 15), v = 4 * (4 * (f & 3) + (l >> 4)) + j;
    t.v[(f * 64 + l) * 4 + j] = (u32)kC64.c[(u * v) & 63] | ((u32)kC64.c[(u * v + 48) & 63] << 16);
  }
  return t; }
__device__ __attribute__((aligned(128))) const T16 dGsA = mkGsA();
__device__ __attribute__((aligned(128))) const T16 dGsB = mkGsB();
__device__ __attribute__((aligned(128))) const T32 dF64 = mkF64();

DI f32x4 mfma(u32x4 a, u32x4 b, f32x4 c) {
  return __builtin_amdgcn_mfma_f32_16x16x32_bf16(__builtin_bit_cast(bf16x8, a), __builtin_bit_cast(bf16x8, b), c, 0, 0, 0);
}
DI u32 pk2(float lo, float hi) { f32x2 v = {lo, hi}; return __builtin_bit_cast(u32, __builtin_convertvector(v, bf16x2)); }
DI float lo2f(u32 p) { return __uint_as_float(p << 16); }
DI float hi2f(u32 p) { return __uint_as_float(p & 0xffff0000u); }
DI float bfr(float x) { return lo2f(pk2(x, x)); }
DI u32x4 pkfrag(f32x4 a, f32x4 b) { u32x4 r = {pk2(a[0], a[1]), pk2(a[2], a[3]), pk2(b[0], b[1]), pk2(b[2], b[3])}; return r; }
DI float phi(float t) {
  float u = bfr(t > 0.f ? t + 1.f : __expf(t));
  return u + 1e-6f;
}
DI float ror8(float v) { return __int_as_float(__builtin_amdgcn_update_dpp(0, __float_as_int(v), 0x128, 0xf, 0xf, true)); }
DI u32x4 ldsr128(const char* smem, u32 off) { return *(const LDS_AS u32x4*)((const LDS_AS char*)smem + off); }
DI u32x2 ldstr(const char* smem, u32 off) {
  return __builtin_bit_cast(u32x2, __builtin_amdgcn_ds_read_tr16_b64_v4i16((LDS_AS s16x4*)((LDS_AS char*)smem + off)));
}
DI u32x4 onehot(u32 v, int pos) {
  u32 s = v << ((pos & 1) * 16); int d = pos >> 1;
  u32x4 r = {d == 0 ? s : 0u, d == 1 ? s : 0u, d == 2 ? s : 0u, d == 3 ? s : 0u};
  return r;
}

constexpr int K1_NS = 4, K1_SLOT = 32768;
constexpr int K1_GATE = K1_NS * K1_SLOT, K1_WST = K1_GATE + 2048, K1_LDS = K1_WST + 21504;

template <bool QT>
DI void k1_gemm(char* smem, const u16* __restrict__ x, const u16* __restrict__ wqkv, char* ws,
                int m, int nt, int rot, int w, int l, const u32 (&ap)[4], const u32 (&bp)[4]
                ) {
  const int g = l >> 4, l15 = l & 15;
  auto issue = [&](int c) {
    int cc = c + rot; if (cc >= 10) cc -= 10;
    u32 s = LDSADDR(smem) + (c & (K1_NS - 1)) * K1_SLOT + w * 4096;
    dma4(s, x + cc * 64, ap[0], ap[1], ap[2], ap[3]);
    dma4(s + 16384, wqkv + cc * (64 * 1920), bp[0], bp[1], bp[2], bp[3]);
  };
  u32 xa[2][2], wa[8][2];
#pragma unroll
  for (int rt = 0; rt < 2; ++rt)
#pragma unroll
    for (int ks = 0; ks < 2; ++ks) { int row = 32 * w + 16 * rt + l15; xa[rt][ks] = row * 128 + (((4 * ks + g) ^ ((row >> 1) & 7)) << 4); }
  const int ofs4 = QT ? 0 : (nt & 1);
#pragma unroll
  for (int ct = 0; ct < 8; ++ct)
#pragma unroll
    for (int t2 = 0; t2 < 2; ++t2) {
      int q4 = l15 >> 2, p = l & 3, krow = 8 * g + 4 * t2 + q4;
      int u = 4 * ct + p + ofs4, ch = (u >> 1) & 15, swz = (q4 << 2) | ((2 * g + t2) & 3);
      wa[ct][t2] = 16384 + krow * 256 + ((ch ^ swz) << 4) + 8 * (u & 1);
    }
  const int grow = 8 * nt + (l >> 3);
  const u32 ga = grow * 128 + (((l & 7) ^ ((grow >> 1) & 7)) << 4);
  float gsum = 0.f;
  f32x4 acc[2][8];
#pragma unroll
  for (int rt = 0; rt < 2; ++rt)
#pragma unroll
    for (int ct = 0; ct < 8; ++ct) acc[rt][ct] = f32x4{0.f, 0.f, 0.f, 0.f};

#pragma unroll 1
  for (int c = 0; c < 10; ++c) {
    if (c < 8) WAITV(16); else if (c == 8) WAITV(8); else WAITV(0);
    RAWBAR();
    if (c < 7) issue(c + 3);
    const char* s = smem + (c & (K1_NS - 1)) * K1_SLOT;
#pragma unroll
    for (int ks = 0; ks < 2; ++ks) {
      u32x4 xf[2], wf[8];
#pragma unroll
      for (int rt = 0; rt < 2; ++rt) xf[rt] = ldsr128(s, xa[rt][ks]);
#pragma unroll
      for (int ct = 0; ct < 8; ++ct) {
        u32x2 a = ldstr(s, wa[ct][0] + ks * 8192), b = ldstr(s, wa[ct][1] + ks * 8192);
        wf[ct] = u32x4{a[0], a[1], b[0], b[1]};
      }
#pragma unroll
      for (int ct = 0; ct < 8; ++ct)
#pragma unroll
        for (int rt = 0; rt < 2; ++rt)
          acc[rt][ct] = QT ? mfma(wf[ct], xf[rt], acc[rt][ct]) : mfma(xf[rt], wf[ct], acc[rt][ct]);
    }
    if (w == 3) {
      int cc = c + rot; if (cc >= 10) cc -= 10;
      u32x4 xv = ldsr128(s, ga), gv = ldsr128(smem, K1_GATE + cc * 128 + (l & 7) * 16);
#pragma unroll
      for (int i = 0; i < 4; ++i) {
        u32 pr = pk2(lo2f(xv[i]) * lo2f(gv[i]), hi2f(xv[i]) * hi2f(gv[i]));
        gsum += lo2f(pr); gsum += hi2f(pr);
      }
    }
  }
  STAMP();
  if (w == 3) {
    gsum += __shfl_xor(gsum, 1); gsum += __shfl_xor(gsum, 2); gsum += __shfl_xor(gsum, 4);
    float z = bfr(gsum);
    float gt = bfr(1.f / (1.f + __expf(-z)));
    if ((l & 7) == 0) ((float*)(ws + OFF_G))[128 * m + grow] = gt;
  }
  if (QT) {
    u16* pq = (u16*)(ws + OFF_PQ);
#pragma unroll
    for (int ct = 0; ct < 8; ++ct) {
      int col = 128 * (nt - 11) + 16 * ct + 4 * g, hp = col / 40, cw = col - 40 * hp;
#pragma unroll
      for (int rt = 0; rt < 2; ++rt) {
        f32x4 a = acc[rt][ct];
        u32x2 o = {pk2(phi(bfr(a[0])), phi(bfr(a[1]))), pk2(phi(bfr(a[2])), phi(bfr(a[3])))};
        int sp = 128 * m + 32 * w + 16 * rt + l15;
        *(u32x2*)(pq + ((size_t)hp * 2048 + sp) * 40 + cw) = o;
      }
    }
  } else {
    u32x4 kf[4], vf[4];
#pragma unroll
    for (int ct = 0; ct < 4; ++ct) {
      f32x4 a = acc[0][ct], b = acc[1][ct];
#pragma unroll
      for (int i = 0; i < 4; ++i) { a[i] = phi(bfr(a[i])); b[i] = phi(bfr(b[i])); }
      kf[ct] = pkfrag(a, b);
      vf[ct] = pkfrag(acc[0][4 + ct], acc[1][4 + ct]);
    }
    if (l15 == 12) vf[3] = u32x4{0x3f803f80u, 0x3f803f80u, 0x3f803f80u, 0x3f803f80u};
    constexpr int PI[12] = {0, 0, 1, 1, 1, 2, 2, 2, 3, 3, 0, 1};
    constexpr int PJ[12] = {0, 1, 0, 1, 2, 1, 2, 3, 2, 3, 3, 3};
    RAWBAR();
#pragma unroll
    for (int P = 0; P < 12; ++P) {
      f32x4 r = mfma(kf[PI[P]], vf[PJ[P]], f32x4{0.f, 0.f, 0.f, 0.f});
      *(LDS_AS f32x4*)((LDS_AS char*)smem + ((w * 12 + P) * 64 + l) * 16) = r;
    }
    RAWBAR();
    float* kvp = (float*)(ws + OFF_KVP);
#pragma unroll
    for (int k = 0; k < 3; ++k) {
      int P = 3 * w + k;
      int pi = (0x103322211100ull >> (4 * P)) & 15, pj = (0x333232121010ull >> (4 * P)) & 15;
      f32x4 r = f32x4{0.f, 0.f, 0.f, 0.f};
#pragma unroll
      for (int ww = 0; ww < 4; ++ww) r += *(const LDS_AS f32x4*)((const LDS_AS char*)smem + ((ww * 12 + P) * 64 + l) * 16);
      int d = 16 * pi + 4 * g, e = 16 * pj + l15, hd = d / 20, he = e / 20;
      int h = 3 * nt + hd;
      bool ok = d < 60 && h < 32 && (e == 60 || (e < 60 && he == hd));
      int ee = e == 60 ? 20 : e - 20 * he;
      if (ok) *(f32x4*)(kvp + (((size_t)m * 32 + h) * 21 + ee) * 20 + (d - 20 * hd)) = r;
    }
  }
}

__global__ __launch_bounds__(256, 1) void k1(const u16* __restrict__ x, const u16* __restrict__ wqkv, const u16* __restrict__ wo,
                                             const u16* __restrict__ wr, const u16* __restrict__ wi, const u16* __restrict__ gate,
                                             char* __restrict__ ws) {
  extern __shared__ __attribute__((aligned(16))) char smem[];
  const int tid = threadIdx.x, l = tid & 63, w = __builtin_amdgcn_readfirstlane(tid >> 6);
  const int g = l >> 4, l15 = l & 15;
  const int b = blockIdx.x, xcd = b & 7, bi = b >> 3;
  const int m = (xcd >> 1) * 4 + (bi & 3), nt = (xcd & 1) * 8 + (bi >> 2);
  const int rot = (((nt & 7) * 5 >> 2) + ((m & 3) * 5 >> 1)) % 10;

  STAMP_DECL; STAMP();
  const int wq = b >> 2, wPP = b & 3;
  for (int ii = w; ii < 21; ii += 4) {
    int P = 64 * ii + l, row = P / 42, pc = P - 42 * row;
    int ks = wq + 64 * (8 * wPP + (row & 7)); if (row & 8) ks = (2048 - ks) & 2047;
    int a = ((ks * 642) & ~15) + pc * 16; a = a > 1314800 ? 1314800 : a;
    dma1v(LDSADDR(smem) + K1_WST + ii * 1024, (const char*)((row & 16) ? wi : wr) + a);
  }
  if (w < 2) { int e = w * 512 + l * 8; e = e > 632 ? 632 : e; dma1(LDSADDR(smem) + K1_GATE + w * 1024, gate, e * 2); }

  u32 ap[4], bp[4];
#pragma unroll
  for (int i = 0; i < 4; ++i) {
    int r = 8 * (4 * w + i) + (l >> 3), ch = (l & 7) ^ ((r >> 1) & 7);
    ap[i] = ((32 * (r & 63) + 2 * m + (r >> 6)) * 640 + ch * 8) * 2 - 1024 * i;
    int kr = 4 * (4 * w + i) + g, cb = l15 ^ (((kr & 3) << 2) | ((kr >> 2) & 3)), col;
    if (nt >= 11) col = 128 * (nt - 11) + 8 * cb;
    else { int c8 = cb & 7; if (nt == 10 && c8 > 4) c8 = 4; col = 640 * (1 + (cb >> 3)) + ((60 * nt) & ~7) + 8 * c8; }
    bp[i] = (kr * 1920 + col) * 2 - 1024 * i;
  }
#pragma unroll
  for (int c = 0; c < 3; ++c) {
    int cc = c + rot; if (cc >= 10) cc -= 10;
    u32 s = LDSADDR(smem) + c * K1_SLOT + w * 4096;
    dma4(s, x + cc * 64, ap[0], ap[1], ap[2], ap[3]);
    dma4(s + 16384, wqkv + cc * (64 * 1920), bp[0], bp[1], bp[2], bp[3]);
  }
  STAMP();

  if (w == 3 && l < 25) {
    int id = 25 * b + l, ko = id / 80, cb = id - 80 * ko;
    u32x4 in[8];
#pragma unroll
    for (int i = 0; i < 8; ++i) in[i] = *(const u32x4*)(wo + (8 * ko + i) * 640 + 8 * cb);
    int a = cb >> 3, bg = cb & 7, hp = ko / 5, wv = ko - 5 * hp;
    u32x4* dst = (u32x4*)(ws + OFF_WOF) + (((bg * 5 + (a >> 1)) * 20 + (hp >> 2) * 5 + wv) * 64 + (hp & 3) * 16 + (a & 1) * 8);
#pragma unroll
    for (int jj = 0; jj < 8; ++jj) {
      u32x4 o;
#pragma unroll
      for (int d = 0; d < 4; ++d) o[d] = __builtin_amdgcn_perm(in[2 * d + 1][jj >> 1], in[2 * d][jj >> 1], (jj & 1) ? 0x07060302u : 0x05040100u);
      dst[jj] = o;
    }
  }

  if (nt >= 10) {
    int grp = nt - 10, n2 = 2 * m + (grp >= 3), g3 = grp >= 3 ? grp - 3 : grp, bg = 3 * g3 + w;
    if (w < 3 && bg < 8) {
      u32x4 xf[4][3], gs[8];
#pragma unroll
      for (int rt = 0; rt < 4; ++rt)
#pragma unroll
        for (int s = 0; s < 3; ++s) {
          int a = 4 * s + g; a = a > 9 ? 9 : a;
          xf[rt][s] = *(const u32x4*)(x + (32 * (16 * rt + l15) + n2) * 640 + 64 * a + 8 * bg);
        }
#pragma unroll
      for (int f = 0; f < 8; ++f) gs[f] = *(const u32x4*)(dGsA.v + (f * 64 + l) * 8);
      const int h = (l >> 3) & 1, j = l & 7, bb = 8 * bg + j;
      char* vb = ws + OFF_VB + (size_t)(n2 * 8 + bg) * VB_TS;
      const float tc = h ? __builtin_amdgcn_cosf(bb * (1.f / 128.f)) : 1.f, ts = h ? __builtin_amdgcn_sinf(bb * (1.f / 128.f)) : 0.f;
#pragma unroll 1
      for (int c = 0; c < 5; ++c) {
        u32x4 kd[3];
#pragma unroll
        for (int s = 0; s < 3; ++s) {
          int a = 4 * s + g;
          float rev = c < 4 ? (float)((c + 1) * (64 * a + bb)) * (1.f / 640.f) - (h ? 0.25f : 0.f) : (h ? 0.5f * a : 0.f);
          float v = a < 10 ? __builtin_amdgcn_cosf(rev) : 0.f;
          kd[s] = onehot(pk2(v, v) & 0xffffu, j);
        }
        f32x4 t[4];
#pragma unroll
        for (int rt = 0; rt < 4; ++rt) {
          t[rt] = f32x4{0.f, 0.f, 0.f, 0.f};
#pragma unroll
          for (int s = 0; s < 3; ++s) t[rt] = mfma(xf[rt][s], kd[s], t[rt]);
        }
        u32x4 tb0 = pkfrag(t[0], t[1]), tb1 = pkfrag(t[2], t[3]);
#pragma unroll
        for (int mt = 0; mt < 4; ++mt) {
          f32x4 a2 = mfma(gs[2 * mt], tb0, f32x4{0.f, 0.f, 0.f, 0.f});
          a2 = mfma(gs[2 * mt + 1], tb1, a2);
#pragma unroll
          for (int rp = 0; rp < 2; ++rp) {
            float o0 = a2[2 * rp], o1 = a2[2 * rp + 1], re, im;
            const bool first = (mt == 0 && rp == 0) && g == 0;
            if (c < 4) {
              float p0 = ror8(o0), p1 = ror8(o1);
              re = h ? p0 + o1 : o0 - p1; im = h ? p1 - o0 : -(o1 + p0);
              if (mt == 0 && rp == 0) { if (first) { re = h ? p1 : o0; im = h ? -o1 : -p0; } }
            } else {
              float s1 = first ? 0.f : o1;
              re = o0 * tc - s1 * ts; im = -(o0 * ts + s1 * tc);
              if (mt == 0 && rp == 0) { if (first) *(u32*)(vb + 10240 + l * 4) = pk2(o1 * tc, -(o1 * ts)); }
            }
            *(u32*)(vb + ((c * 4 + mt) * 2 + rp) * 256 + l * 4) = pk2(re, im);
          }
        }
      }
    }
  }

  STAMP();
  WAITV(0);
  RAWBAR();
  STAMP();
  {
    const int t = l, ncomb = wq <= 32 ? 12 : 8;
    const int dd = (2 * wq) & 15, dm = (-2 * wq) & 15;
    for (int cb = w; cb < ncomb; cb += 4) {
      int rI = cb >> 1, P2 = cb & 1, r = rI < 4 ? rI + 1 : (rI == 4 ? 0 : 5);
      int kd = r + 10 * t; bool mir = kd > 320;
      int base = K1_WST + (mir ? 8 * 672 + dm + 2 * (640 - kd) : dd + 2 * kd) + P2 * 4 * 672;
      u32x4 o;
#pragma unroll
      for (int i = 0; i < 4; ++i) {
        u32 re = *(const LDS_AS u16*)((const LDS_AS char*)smem + base + i * 672);
        u32 im = *(const LDS_AS u16*)((const LDS_AS char*)smem + base + i * 672 + 16 * 672);
        o[i] = re | ((im ^ (mir ? 0x8000u : 0u)) << 16);
      }
      if (r == 0 && (t & 31) == 0) {
        int b2 = K1_WST + 8 * 672 + dm + 2 * kd + P2 * 4 * 672;
#pragma unroll
        for (int i = 0; i < 4; ++i) {
          u32 re = *(const LDS_AS u16*)((const LDS_AS char*)smem + b2 + i * 672);
          u32 im = *(const LDS_AS u16*)((const LDS_AS char*)smem + b2 + i * 672 + 16 * 672);
          o[i] = pk2(0.5f * (lo2f(o[i]) + lo2f(re)), 0.5f * (hi2f(o[i]) - lo2f(im)));
        }
      }
      int ci, half, c;
      if (rI < 4) { c = rI; half = wq >= 32; ci = wq < 32 ? wq : (64 - wq) & 31; }
      else { c = 4; half = rI - 4; ci = wq; }
      int sidx = ci == 32 ? 160 : ((c * 4 + (ci >> 3)) * 2 + (ci & 1)) * 4 + ((ci >> 1) & 3);
      int P = 2 * wPP + P2;
      *(u32x4*)(ws + OFF_WP + (size_t)sidx * 16384 + half * 8192 + (P >> 2) * 4096 + (t >> 4) * 1024 + ((P & 3) * 16 + (t & 15)) * 16) = o;
    }
  }

  STAMP();
#define TSARG
  if (nt >= 11) k1_gemm<true>(smem, x, wqkv, ws, m, nt, rot, w, l, ap, bp TSARG);
  else k1_gemm<false>(smem, x, wqkv, ws, m, nt, rot, w, l, ap, bp TSARG);
  STAMP();
  STAMP_FLUSH(0);
}

constexpr int K2_V = 0, K2_A = 16384, K2_U = 32768, K2_US = 272, K2_LDS = K2_U + 2 * 32 * K2_US;
constexpr int K2_NF = 168;

__global__ __launch_bounds__(512, 2) void k2(char* __restrict__ ws) {
  extern __shared__ __attribute__((aligned(16))) char smem[];
  const int tid = threadIdx.x, l = tid & 63, w8 = __builtin_amdgcn_readfirstlane(tid >> 6);
  const int g = l >> 4, l15 = l & 15;
  const int b = blockIdx.x;
  STAMP_DECL; STAMP();
  if (b < K2_NF) {
    const int sidx = (b & 7) * 21 + (b >> 3);
    if (sidx > 160) return;
    int c, ci;
    if (sidx == 160) { c = 4; ci = 32; }
    else { c = sidx >> 5; ci = ((sidx >> 3) & 3) * 8 + (sidx & 3) * 2 + ((sidx >> 2) & 1); }
    const int half = w8 >> 2, w4 = w8 & 3;
    const int q = c == 4 ? ci : (half == 0 ? ci : (ci == 0 ? 32 : 64 - ci));
#pragma unroll
    for (int i = 0; i < 2; ++i) {
      int row = 4 * (2 * w4 + i) + g, kc = l15 ^ (row & 15);
      dma1(LDSADDR(smem) + K2_V + half * 8192 + (2 * w4 + i) * 1024, ws + OFF_VB + sidx * 64 + half * 32,
           (u32)((row * 8 + (kc >> 1)) * VB_TS + (kc & 1) * 16));
    }
    u32x4 tr[4], ti[4], wf[2];
#pragma unroll
    for (int ks = 0; ks < 4; ++ks) tr[ks] = *(const u32x4*)(dF64.v + ((w4 * 4 + ks) * 64 + l) * 4);
#pragma unroll
    for (int mt = 0; mt < 2; ++mt) wf[mt] = *(const u32x4*)(ws + OFF_WP + (size_t)sidx * 16384 + half * 8192 + mt * 4096 + w4 * 1024 + l * 16);
    {
      float cs[8], sn[8];
#pragma unroll
      for (int j = 0; j < 8; ++j) {
        int ri = 16 * (w4 & 1) + l15, ki = j < 4 ? 4 * g + j : 12 + 4 * g + j;
        int n2 = w4 < 2 ? ki : ri, p = w4 < 2 ? ri : ki;
        float rev = (float)((n2 * (q + 64 * p)) & 2047) * (1.f / 2048.f);
        cs[j] = __builtin_amdgcn_cosf(rev); sn[j] = __builtin_amdgcn_sinf(rev);
      }
      u32x4 cf = {pk2(cs[0], cs[1]), pk2(cs[2], cs[3]), pk2(cs[4], cs[5]), pk2(cs[6], cs[7])};
      u32x4 sf = {pk2(sn[0], sn[1]), pk2(sn[2], sn[3]), pk2(sn[4], sn[5]), pk2(sn[6], sn[7])};
      *(LDS_AS u32x4*)((LDS_AS char*)smem + K2_A + half * 8192 + w4 * 2048 + l * 16) = cf;
      *(LDS_AS u32x4*)((LDS_AS char*)smem + K2_A + half * 8192 + w4 * 2048 + 1024 + l * 16) = sf;
    }
#pragma unroll
    for (int ks = 0; ks < 4; ++ks)
#pragma unroll
      for (int i = 0; i < 4; ++i) ti[ks][i] = __builtin_amdgcn_alignbit(tr[ks][i], tr[ks][i], 16) ^ 0x8000u;
    STAMP();
    WAITV(0);
    RAWBAR();
    STAMP();
    const f32x4 z4 = {0.f, 0.f, 0.f, 0.f};
    f32x4 Tr[2], Ti[2];
#pragma unroll
    for (int mt = 0; mt < 2; ++mt) {
      Tr[mt] = z4; Ti[mt] = z4;
#pragma unroll
      for (int ks = 0; ks < 4; ++ks) {
        u32x4 vf = ldsr128(smem, K2_V + half * 8192 + (16 * mt + l15) * 256 + (((4 * ks + g) ^ l15) << 4));
        Tr[mt] = mfma(vf, tr[ks], Tr[mt]); Ti[mt] = mfma(vf, ti[ks], Ti[mt]);
      }
    }
    u32x4 br = pkfrag(Tr[0], Tr[1]), bi = pkfrag(Ti[0], Ti[1]);
    f32x4 Yr[2], Yi[2];
#pragma unroll
    for (int mt = 0; mt < 2; ++mt) {
      u32x4 cf = ldsr128(smem, K2_A + half * 8192 + mt * 2048 + l * 16), sf = ldsr128(smem, K2_A + half * 8192 + mt * 2048 + 1024 + l * 16);
      u32x4 nf = sf ^ 0x80008000u;
      f32x4 zr = mfma(cf, br, z4); zr = mfma(sf, bi, zr);
      f32x4 zi = mfma(cf, bi, z4); zi = mfma(nf, br, zi);
#pragma unroll
      for (int i = 0; i < 4; ++i) {
        float a = lo2f(wf[mt][i]), bq = hi2f(wf[mt][i]);
        Yr[mt][i] = zr[i] * a - zi[i] * bq; Yi[mt][i] = zr[i] * bq + zi[i] * a;
      }
    }
    br = pkfrag(Yr[0], Yr[1]); bi = pkfrag(Yi[0], Yi[1]);
#pragma unroll
    for (int mt = 0; mt < 2; ++mt) {
      u32x4 cf = ldsr128(smem, K2_A + half * 8192 + (2 + mt) * 2048 + l * 16), sf = ldsr128(smem, K2_A + half * 8192 + (2 + mt) * 2048 + 1024 + l * 16);
      u32x4 nf = sf ^ 0x80008000u;
      f32x4 ur = mfma(cf, br, z4); ur = mfma(nf, bi, ur);
      f32x4 ui = mfma(cf, bi, z4); ui = mfma(sf, br, ui);
#pragma unroll
      for (int i = 0; i < 4; ++i)
        *(LDS_AS u32*)((LDS_AS char*)smem + K2_U + half * (32 * K2_US) + (16 * mt + 4 * g + i) * K2_US + (16 * w4 + l15) * 4) = pk2(ur[i], -ui[i]);
    }
    STAMP();
    RAWBAR();
    STAMP();
    f32x4 Rr[2], Ri[2];
#pragma unroll
    for (int cl = 0; cl < 2; ++cl) {
      Rr[cl] = z4; Ri[cl] = z4;
#pragma unroll
      for (int ks = 0; ks < 4; ++ks) {
        u32x4 uf = ldsr128(smem, K2_U + cl * (32 * K2_US) + (16 * half + l15) * K2_US + (4 * ks + g) * 16);
        Rr[cl] = mfma(tr[ks], uf, Rr[cl]); Ri[cl] = mfma(ti[ks], uf, Ri[cl]);
      }
    }
    const float sc = 2.f / (2048.f * 640.f);
    float e00[4], e01[4], e10[4], e11[4];
#pragma unroll
    for (int i = 0; i < 4; ++i) {
      float ar = Rr[0][i] * sc, ai = -Ri[0][i] * sc, brr = Rr[1][i] * sc, bii = -Ri[1][i] * sc;
      if (c < 4) {
        if (ci > 0) { e00[i] = ar + brr; e01[i] = -(ai + bii); e10[i] = bii - ai; e11[i] = brr - ar; }
        else { e00[i] = ar; e01[i] = -ai; e10[i] = brr; e11[i] = -bii; }
      } else {
        float rev = (float)(16 * w4 + 4 * g + i) * (1.f / 128.f);
        float tc = __builtin_amdgcn_cosf(rev), ts = __builtin_amdgcn_sinf(rev);
        float xr = brr * tc - bii * ts, xi = brr * ts + bii * tc;
        if (ci & 31) { e00[i] = ar; e01[i] = xr; e10[i] = -ai; e11[i] = -xi; }
        else { e00[i] = e10[i] = 0.5f * ar; e01[i] = e11[i] = 0.5f * xr; }
      }
    }
    u32x4 r0 = {pk2(e00[0], e00[1]), pk2(e00[2], e00[3]), pk2(e01[0], e01[1]), pk2(e01[2], e01[3])};
    u32x4 r1 = {pk2(e10[0], e10[1]), pk2(e10[2], e10[3]), pk2(e11[0], e11[1]), pk2(e11[2], e11[3])};
    const int so = sidx == 160 ? 128 : sidx;
    char* dst = ws + OFF_AP + (size_t)((16 * half + l15) * 8 + 2 * w4 + (g >> 1)) * AP_TS + so * 64 + (g & 1) * 32;
    if (sidx != 160) *(u32x4*)dst = r0;
    if (sidx != 128) *(u32x4*)(dst + 16) = r1;
    STAMP();
  } else {
    const int ob = b - K2_NF, hp = ob & 15, rb = ob >> 4;
    const u16* pq = (const u16*)(ws + OFF_PQ) + (size_t)hp * 2048 * 40;
    u32x4 pf[2][2];
#pragma unroll
    for (int rt = 0; rt < 2; ++rt)
#pragma unroll
      for (int u = 0; u < 2; ++u) {
        int sp = 256 * rb + 32 * w8 + 16 * rt + l15;
        const u32* p = (const u32*)(pq + sp * 40 + 20 * u + 8 * (g > 2 ? 2 : g));
        u32x2 a = *(const u32x2*)p, bq = *(const u32x2*)(p + (g < 2 ? 2 : 0));
        pf[rt][u] = u32x4{g < 3 ? a[0] : 0u, g < 3 ? a[1] : 0u, g < 2 ? bq[0] : 0u, g < 2 ? bq[1] : 0u};
      }
    if (tid < 210) {
      const f32x4* kvp = (const f32x4*)(ws + OFF_KVP) + hp * 210 + tid;
      f32x4 s = kvp[0];
#pragma unroll
      for (int mm = 1; mm < 16; ++mm) s += kvp[mm * 3360];
      int u = tid >= 105, in = tid - 105 * u, e = in / 5, dq = in - 5 * e;
      u32x2 o = {pk2(s[0], s[1]), pk2(s[2], s[3])};
      *(LDS_AS u32x2*)((LDS_AS char*)smem + u * 1024 + e * 48 + dq * 8) = o;
    }
    STAMP();
    __syncthreads();
    STAMP();
    u16* o2 = (u16*)(ws + OFF_O2) + (size_t)hp * 2048 * 40;
    const f32x4 z4 = {0.f, 0.f, 0.f, 0.f};
#pragma unroll
    for (int u = 0; u < 2; ++u) {
      u32x4 af[3];
#pragma unroll
      for (int tI = 0; tI < 3; ++tI) {
        int e = tI == 0 ? l15 : (tI == 1 ? 16 + (l15 & 3) : 20);
        u32x4 v = ldsr128(smem, u * 1024 + e * 48 + (g > 2 ? 2 : g) * 16);
        af[tI] = u32x4{g < 3 ? v[0] : 0u, g < 3 ? v[1] : 0u, g < 2 ? v[2] : 0u, g < 2 ? v[3] : 0u};
      }
#pragma unroll
      for (int rt = 0; rt < 2; ++rt) {
        f32x4 n0 = mfma(af[0], pf[rt][u], z4), n1 = mfma(af[1], pf[rt][u], z4), dn = mfma(af[2], pf[rt][u], z4);
        float rd = 1.f / bfr(bfr(dn[0]) + 1e-6f);
        int sp = 256 * rb + 32 * w8 + 16 * rt + l15;
        u32x2 a = {pk2(bfr(n0[0]) * rd, bfr(n0[1]) * rd), pk2(bfr(n0[2]) * rd, bfr(n0[3]) * rd)};
        u32x2 bq = {pk2(bfr(n1[0]) * rd, bfr(n1[1]) * rd), pk2(bfr(n1[2]) * rd, bfr(n1[3]) * rd)};
        *(u32x2*)(o2 + sp * 40 + 20 * u + 4 * g) = a;
        if (g == 0) *(u32x2*)(o2 + sp * 40 + 20 * u + 16) = bq;
      }
    }
    STAMP();
  }
  STAMP_FLUSH(1);
}

constexpr int K3_LDS = 81920;

__global__ __launch_bounds__(320, 1) void k3(const char* __restrict__ ws, u16* __restrict__ out) {
  extern __shared__ __attribute__((aligned(16))) char smem[];
  const int tid = threadIdx.x, l = tid & 63, ct = __builtin_amdgcn_readfirstlane(tid >> 6);
  const int g = l >> 4, l15 = l & 15;
  const int b = blockIdx.x, n2 = 4 * (b & 7) + ((b >> 3) & 3), bg = b >> 5;
  STAMP_DECL; STAMP();
#pragma unroll
  for (int i = 0; i < 16; ++i)
    dma1(LDSADDR(smem) + i * 5120 + ct * 1024, ws + OFF_O2 + ((size_t)i * 2048 + 64 * n2) * 80 + ct * 1024, l * 16);
  u32x4 wf[20], af[4][3], gs[8];
  float gt[4];
  const char* ap = ws + OFF_AP + (size_t)(n2 * 8 + bg) * AP_TS;
#pragma unroll
  for (int rt = 0; rt < 4; ++rt)
#pragma unroll
    for (int s = 0; s < 3; ++s) {
      int kc = 4 * s + g; kc = kc > 9 ? 9 : kc;
      int i = 8 * rt + (l15 >> 1);
      af[rt][s] = *(const u32x4*)(ap + ((((kc >> 1) * 4 + rt) * 2 + (i & 1)) * 4 + ((i >> 1) & 3)) * 64 + (kc & 1) * 32 + (l15 & 1) * 16);
    }
#pragma unroll
  for (int f = 0; f < 8; ++f) gs[f] = *(const u32x4*)(dGsB.v + (f * 64 + l) * 8);
#pragma unroll
  for (int rt = 0; rt < 4; ++rt) gt[rt] = ((const float*)(ws + OFF_G))[64 * n2 + 16 * rt + l15];
#pragma unroll
  for (int ks = 0; ks < 20; ++ks) wf[ks] = *(const u32x4*)(ws + OFF_WOF + ((size_t)((bg * 5 + ct) * 20 + ks) * 64 + l) * 16);
  u32x4 kd[3];
  {
    const int a = 2 * ct + (l15 >> 3), j = l15 & 7, d = 64 * a + 8 * bg + j;
#pragma unroll
    for (int s = 0; s < 3; ++s) {
      int kc = 4 * s + g, c = kc >> 1;
      float rev = (float)((c + 1) * d) * (1.f / 640.f);
      float v0 = c < 4 ? __builtin_amdgcn_cosf(rev) : 1.f, v1 = c < 4 ? __builtin_amdgcn_sinf(rev) : ((a & 1) ? -1.f : 1.f);
      bool on = kc < 10 && (kc & 1) == (j >> 2);
      u32 p0 = on ? (pk2(v0, v0) & 0xffffu) << ((j & 1) * 16) : 0u, p1 = on ? (pk2(v1, v1) & 0xffffu) << ((j & 1) * 16) : 0u;
      kd[s] = (j & 2) ? u32x4{0u, p0, 0u, p1} : u32x4{p0, 0u, p1, 0u};
    }
  }
  const f32x4 z4 = {0.f, 0.f, 0.f, 0.f};
  f32x4 yb[4];
  {
    f32x4 P[4];
#pragma unroll
    for (int rt = 0; rt < 4; ++rt) {
      P[rt] = z4;
#pragma unroll
      for (int s = 0; s < 3; ++s) P[rt] = mfma(af[rt][s], kd[s], P[rt]);
    }
    u32x4 p0 = pkfrag(P[0], P[1]), p1 = pkfrag(P[2], P[3]);
#pragma unroll
    for (int nt = 0; nt < 4; ++nt) { yb[nt] = mfma(p0, gs[2 * nt], z4); yb[nt] = mfma(p1, gs[2 * nt + 1], yb[nt]); }
  }
  STAMP();
  WAITV(0);
  RAWBAR();
  STAMP();
  f32x4 acc[4] = {z4, z4, z4, z4};
#pragma unroll
  for (int ks = 0; ks < 20; ++ks) {
    const int Q = ks / 5, wv = ks % 5;
#pragma unroll
    for (int rt = 0; rt < 4; ++rt) {
      u32x4 of = ldsr128(smem, (4 * Q + g) * 5120 + (16 * rt + l15) * 80 + wv * 16);
      acc[rt] = mfma(wf[ks], of, acc[rt]);
    }
    if (ks & 1) __builtin_amdgcn_sched_barrier(0);
  }
  STAMP();
#pragma unroll
  for (int rt = 0; rt < 4; ++rt) {
    float gg = gt[rt], hh = bfr(1.f - gg), o[4];
#pragma unroll
    for (int i = 0; i < 4; ++i) o[i] = bfr(gg * bfr(acc[rt][i])) + bfr(hh * bfr(yb[rt][i]));
    u32x2 v = {pk2(o[0], o[1]), pk2(o[2], o[3])};
    *(u32x2*)(out + (32 * (16 * rt + l15) + n2) * 640 + 64 * (2 * ct + (g >> 1)) + 8 * bg + 4 * (g & 1)) = v;
  }
  STAMP();
  STAMP_FLUSH(2);
}

extern "C" void kernel_launch(void* const* d_in, const int* in_sizes, int n_in, void* d_out, int out_size, void* d_ws,
                              size_t ws_size, hipStream_t stream) {
  const u16* x = (const u16*)d_in[0];
  const u16* wqkv = (const u16*)d_in[1];
  const u16* wo = (const u16*)d_in[2];
  const u16* wr = (const u16*)d_in[4];
  const u16* wi = (const u16*)d_in[5];
  const u16* gate = (const u16*)d_in[6];
  char* ws = (char*)d_ws;
  (void)hipFuncSetAttribute((const void*)k1, hipFuncAttributeMaxDynamicSharedMemorySize, K1_LDS);
  (void)hipFuncSetAttribute((const void*)k3, hipFuncAttributeMaxDynamicSharedMemorySize, K3_LDS);
  k1<<<256, 256, K1_LDS, stream>>>(x, wqkv, wo, wr, wi, gate, ws);
  k2<<<K2_NF + 128, 512, K2_LDS, stream>>>(ws);
  k3<<<256, 320, K3_LDS, stream>>>(ws, (u16*)d_out);
}
```

```cpp
#include <hip/hip_runtime.h>
#include <stdint.h>

typedef unsigned short u16;
typedef unsigned int u32;
typedef short s16x4 __attribute__((ext_vector_type(4)));
typedef short s16x8 __attribute__((ext_vector_type(8)));
typedef __bf16 bf16x8 __attribute__((ext_vector_type(8)));
typedef __bf16 bf16x2 __attribute__((ext_vector_type(2)));
typedef float f32x4 __attribute__((ext_vector_type(4)));
typedef float f32x2 __attribute__((ext_vector_type(2)));
typedef u32 u32x4 __attribute__((ext_vector_type(4)));
typedef u32 u32x2 __attribute__((ext_vector_type(2)));

#define LDS_AS __attribute__((address_space(3)))
#define GLB_AS __attribute__((address_space(1)))
#define DI __device__ __forceinline__
#define STAMP_DECL
#define STAMP()
#define STAMP_FLUSH(K)
__device__ __forceinline__ void dma1(u32 lds, const void* base, u32 v0) {
  u32 keep;
  asm volatile("s_mov_b32 %0, m0\n\ts_mov_b32 m0, %1\n\ts_nop 4\n\tglobal_load_lds_dwordx4 %2, %3\n\ts_mov_b32 m0, %0"
               : "=&s"(keep) : "s"(lds), "v"(v0), "s"(base) : "memory");
}
__device__ __forceinline__ void dma4(u32 lds, const void* base, u32 v0, u32 v1, u32 v2, u32 v3) {
  u32 keep;
  asm volatile("s_mov_b32 %0, m0\n\ts_mov_b32 m0, %1\n\ts_nop 4\n\tglobal_load_lds_dwordx4 %2, %6\n\t"
               "global_load_lds_dwordx4 %3, %6 offset:1024\n\tglobal_load_lds_dwordx4 %4, %6 offset:2048\n\t"
               "global_load_lds_dwordx4 %5, %6 offset:3072\n\ts_mov_b32 m0, %0"
               : "=&s"(keep) : "s"(lds), "v"(v0), "v"(v1), "v"(v2), "v"(v3), "s"(base) : "memory");
}
__device__ __forceinline__ void dma2(u32 lds, const void* base, u32 v0, u32 v1) {
  u32 keep;
  asm volatile("s_mov_b32 %0, m0\n\ts_mov_b32 m0, %1\n\ts_nop 4\n\tglobal_load_lds_dwordx4 %2, %4\n\t"
               "global_load_lds_dwordx4 %3, %4 offset:1024\n\ts_mov_b32 m0, %0"
               : "=&s"(keep) : "s"(lds), "v"(v0), "v"(v1), "s"(base) : "memory");
}
__device__ __forceinline__ void dma1v(u32 lds, const void* src) {
  u32 keep;
  asm volatile("s_mov_b32 %0, m0\n\ts_mov_b32 m0, %1\n\ts_nop 0\n\tglobal_load_lds_dwordx4 %2, off\n\ts_mov_b32 m0, %0"
               : "=&s"(keep) : "s"(lds), "v"(src) : "memory");
}
#define GLD(dst, p, imm) asm volatile("global_load_dwordx4 %0, %1, off offset:" #imm : "=v"(dst) : "v"(p) : "memory")
#define GLD1(dst, p, imm) asm volatile("global_load_dword %0, %1, off offset:" #imm : "=v"(dst) : "v"(p) : "memory")
#define GLDH(dst, p) asm volatile("global_load_ushort %0, %1, off" : "=v"(dst) : "v"(p) : "memory")
#define LDSADDR(p) ((u32)(uintptr_t)(LDS_AS char*)(p))
#define SB() __builtin_amdgcn_sched_barrier(0)
#define WAITV(n) asm volatile("s_waitcnt vmcnt(" #n ")" ::: "memory")
#define RAWBAR() do { asm volatile("" ::: "memory"); __builtin_amdgcn_s_waitcnt(0xC07F); __builtin_amdgcn_s_barrier(); asm volatile("" ::: "memory"); } while (0)

constexpr size_t OFF_PQ  = 0;
constexpr size_t OFF_O2  = OFF_PQ + 2621440;
constexpr size_t OFF_KVP = OFF_O2 + 2621440;
constexpr size_t OFF_VB  = OFF_KVP + 860160;
constexpr int    VB_TS   = 10368;
constexpr size_t OFF_AP  = OFF_VB + (size_t)256 * VB_TS;
constexpr int    AP_TS   = 10240;
constexpr size_t OFF_WP  = OFF_AP + (size_t)256 * AP_TS;
constexpr size_t OFF_WOF = OFF_WP + (size_t)161 * 16384;
constexpr size_t OFF_G   = OFF_WOF + 819200;

constexpr double kPi = 3.14159265358979323846;
constexpr double tcos(double x) {
  double x2 = x * x, term = 1.0, s = 1.0;
  for (int i = 1; i < 14; ++i) { term *= -x2 / ((2 * i - 1) * (2 * i)); s += term; }
  return s;
}
constexpr double cos64(int k) {
  k &= 63; if (k > 32) k = 64 - k;
  if (k == 16) return 0.0;
  return (k < 16) ? tcos(2 * kPi * k / 64) : -tcos(2 * kPi * (32 - k) / 64);
}
constexpr u16 tobf(double v) {
  u32 u = __builtin_bit_cast(u32, (float)v);
  return (u16)((u + 0x7fffu + ((u >> 16) & 1u)) >> 16);
}
struct C64 { u16 c[64]; };
constexpr C64 mkC64() { C64 t{}; for (int i = 0; i < 64; ++i) t.c[i] = tobf(cos64(i)); return t; }
constexpr C64 kC64 = mkC64();
constexpr u16 gsv(int qp, int n1) {
  int i = qp >> 1;
  if (qp == 1) return kC64.c[(32 * n1) & 63];
  return (qp & 1) ? kC64.c[(i * n1 + 48) & 63] : kC64.c[(i * n1) & 63];
}
constexpr int kperm(int s2, int g, int j) { return 32 * s2 + (j < 4 ? 4 * g + j : 12 + 4 * g + j); }
struct T16 { u16 v[4096]; };
struct T32 { u32 v[4096]; };
constexpr T16 mkGsA() { T16 t{};
  for (int f = 0; f < 8; ++f) for (int l = 0; l < 64; ++l) for (int j = 0; j < 8; ++j)
    t.v[(f * 64 + l) * 8 + j] = gsv(16 * (f >> 1) + (l & 15), kperm(f & 1, l >> 4, j));
  return t; }
constexpr T16 mkGsB() { T16 t{};
  for (int f = 0; f < 8; ++f) for (int l = 0; l < 64; ++l) for (int j = 0; j < 8; ++j)
    t.v[(f * 64 + l) * 8 + j] = gsv(kperm(f & 1, l >> 4, j), 16 * (f >> 1) + (l & 15));
  return t; }
constexpr T32 mkF64() { T32 t{};
  for (int f = 0; f < 16; ++f) for (int l = 0; l < 64; ++l) for (int j = 0; j < 4; ++j) {
    int u = 16 * (f >> 2) + (l & 15), v = 4 * (4 * (f & 3) + (l >> 4)) + j;
    t.v[(f * 64 + l) * 4 + j] = (u32)kC64.c[(u * v) & 63] | ((u32)kC64.c[(u * v + 16) & 63] << 16);
  }
  return t; }
__device__ __attribute__((aligned(128))) const T16 dGsA = mkGsA();
__device__ __attribute__((aligned(128))) const T16 dGsB = mkGsB();
__device__ __attribute__((aligned(128))) const T32 dF64 = mkF64();

DI f32x4 mfma(u32x4 a, u32x4 b, f32x4 c) {
  return __builtin_amdgcn_mfma_f32_16x16x32_bf16(__builtin_bit_cast(bf16x8, a), __builtin_bit_cast(bf16x8, b), c, 0, 0, 0);
}
DI u32 pk2(float lo, float hi) { f32x2 v = {lo, hi}; return __builtin_bit_cast(u32, __builtin_convertvector(v, bf16x2)); }
DI float lo2f(u32 p) { return __uint_as_float(p << 16); }
DI float hi2f(u32 p) { return __uint_as_float(p & 0xffff0000u); }
DI float bfr(float x) { return lo2f(pk2(x, x)); }
DI u32x4 pkfrag(f32x4 a, f32x4 b) { u32x4 r = {pk2(a[0], a[1]), pk2(a[2], a[3]), pk2(b[0], b[1]), pk2(b[2], b[3])}; return r; }
DI float ror8(float v) { return __int_as_float(__builtin_amdgcn_update_dpp(0, __float_as_int(v), 0x128, 0xf, 0xf, true)); }
DI u32x4 ldsr128(const char* smem, u32 off) { return *(const LDS_AS u32x4*)((const LDS_AS char*)smem + off); }
DI u32x2 ldstr(const char* smem, u32 off) {
  return __builtin_bit_cast(u32x2, __builtin_amdgcn_ds_read_tr16_b64_v4i16((LDS_AS s16x4*)((LDS_AS char*)smem + off)));
}
DI void st16wt(void* p, u32x4 v) { asm volatile("global_store_dwordx4 %0, %1, off sc1\n\ts_nop 1" :: "v"(p), "v"(v) : "memory"); }
DI void st4wt(void* p, u32 v) { asm volatile("global_store_dword %0, %1, off sc1" :: "v"(p), "v"(v) : "memory"); }
DI void st16s(const void* b, u32 o, u32x4 v) { asm volatile("global_store_dwordx4 %0, %1, %2 sc1\n\ts_nop 1" :: "v"(o), "v"(v), "s"(b) : "memory"); }
#define ST16SO(b, o, imm, v) asm volatile("global_store_dwordx4 %0, %1, %2 offset:" #imm " sc1\n\ts_nop 1" :: "v"(o), "v"(v), "s"(b) : "memory")
#define ST4SO(b, o, imm, v) asm volatile("global_store_dword %0, %1, %2 offset:%3 sc1" :: "v"(o), "v"(v), "s"(b), "i"(imm) : "memory")
#define ST4O(p, imm, v) asm volatile("global_store_dword %0, %1, off offset:%2 sc1" :: "v"(p), "v"(v), "i"(imm) : "memory")
DI void ldsw128(char* smem, u32 off, u32x4 v) { *(LDS_AS u32x4*)((LDS_AS char*)smem + off) = v; }
DI void ldsw64(char* smem, u32 off, u32x2 v) { *(LDS_AS u32x2*)((LDS_AS char*)smem + off) = v; }
DI u32x2 ldsr64(const char* smem, u32 off) { return *(const LDS_AS u32x2*)((const LDS_AS char*)smem + off); }
DI u32 ldsr16(const char* smem, u32 off) { return *(const LDS_AS u16*)((const LDS_AS char*)smem + off); }
template <int N> DI void phi4(u32x4 (&p)[N]) {
  u32 mn = 0xffffffffu;
#pragma unroll
  for (int n = 0; n < N; ++n)
#pragma unroll
    for (int i = 0; i < 4; ++i) {
      float t0 = lo2f(p[n][i]), t1 = hi2f(p[n][i]);
      float u0 = t0 > 0.f ? t0 + 1.f : __builtin_amdgcn_exp2f(t0 * 1.44269504088896f);
      float u1 = t1 > 0.f ? t1 + 1.f : __builtin_amdgcn_exp2f(t1 * 1.44269504088896f);
      mn = min(mn, min(__float_as_uint(u0), __float_as_uint(u1)));
      p[n][i] = pk2(u0, u1);
    }
  if (__builtin_amdgcn_ballot_w64(mn < 0x3a800000u)) {
#pragma unroll
    for (int n = 0; n < N; ++n)
#pragma unroll
      for (int i = 0; i < 4; ++i) p[n][i] = pk2(lo2f(p[n][i]) + 1e-6f, hi2f(p[n][i]) + 1e-6f);
  }
}
template <int CTRL> DI float dppf(float v) { return __int_as_float(__builtin_amdgcn_update_dpp(0, __float_as_int(v), CTRL, 0xf, 0xf, true)); }
DI u32x4 onehot(u32 s, int d) { u32x4 r = {d == 0 ? s : 0u, d == 1 ? s : 0u, d == 2 ? s : 0u, d == 3 ? s : 0u}; return r; }

constexpr int K1_SLOT = 16384, K1_B = 65536, K1_AUX = 131072, K1_LDS = K1_AUX + 10240;

struct Frags { u32x4 x[2][2], w[2][4]; };
#define PIN(v) asm volatile("" : "+v"(v))

DI int wslot(int q) { int ci = q <= 32 ? q : 64 - q; return ci == 32 ? 32 : ((ci >> 3) * 2 + (ci & 1)) * 4 + ((ci >> 1) & 3); }

struct WRegs { u32x4 R4[4], I4[4]; u32 R1[4], I1[4], sp[4]; };
DI void wperm_load(WRegs& W, const u16* wr, const u16* wi, int tb, int l, int pass) {
  const int q = tb >> 2, P = 2 * (tb & 3) + (l >> 5);
#pragma unroll
  for (int i = 0; i < 4; ++i) {
    const u32 off = (q + 64 * ((4 * P + i + pass) & 31)) * 642 + 20 * (l & 31);
    asm volatile("global_load_dwordx4 %0, %4, %5\n\tglobal_load_dword %1, %4, %5 offset:16\n\t"
                 "global_load_dwordx4 %2, %4, %6\n\tglobal_load_dword %3, %4, %6 offset:16"
                 : "=&v"(W.R4[i]), "=&v"(W.R1[i]), "=&v"(W.I4[i]), "=&v"(W.I1[i]) : "v"(off), "s"(wr), "s"(wi) : "memory");
  }
}
#define WPIN(W) "+v"(W.R4[0]), "+v"(W.R4[1]), "+v"(W.R4[2]), "+v"(W.R4[3]), "+v"(W.I4[0]), "+v"(W.I4[1]), "+v"(W.I4[2]), "+v"(W.I4[3]), \
                "+v"(W.R1[0]), "+v"(W.R1[1]), "+v"(W.R1[2]), "+v"(W.R1[3]), "+v"(W.I1[0]), "+v"(W.I1[1]), "+v"(W.I1[2]), "+v"(W.I1[3])
DI void wperm_store(WRegs& W, const u16* wr, const u16* wi, char* ws, int tb, int l) {
  const int q = tb >> 2, t = l & 31, P = 2 * (tb & 3) + (l >> 5), qm = (64 - q) & 63;
  char* wp = ws + OFF_WP;
  const int d14 = (wslot(q) & 31) * 16384 + (q >= 32) * 8192, m14 = (wslot(qm) & 31) * 16384 + (qm >= 32) * 8192;
  const int d05 = 4 * 524288 + wslot(q) * 16384, m05 = 4 * 524288 + wslot(qm) * 16384;
  const u32 pd = (P >> 2) * 4096 + (P & 3) * 256 + (t >> 4) * 1024 + (t & 15) * 16;
  const u32 pm1 = 8176 - pd, t0 = (64 - t) & 63, pm0 = ((7 - P) >> 2) * 4096 + ((7 - P) & 3) * 256 + (t0 >> 4) * 1024 + (t0 & 15) * 16;
  const int p = 8 * (tb & 3) + (l & 7);
  const u32 po = (p >> 4) * 4096 + ((p >> 2) & 3) * 256 + ((l >> 3) & 1) * 2048 + (p & 3) * 4;
  if (q <= 32 && l < 16)
    ST4SO(wp, d05 + po, 0, pk2(0.5f * (lo2f(W.sp[0]) + lo2f(W.sp[1])), 0.5f * (lo2f(W.sp[2]) - lo2f(W.sp[3]))));
#pragma unroll 1
  for (int pass = 0;; ++pass) {
    const bool dir = pass == 0, mir = q != 0 || pass == 1;
#pragma unroll
    for (int r = 0; r < 10; ++r) {
      u32x4 v;
#pragma unroll
      for (int i = 0; i < 4; ++i)
        v[i] = __builtin_amdgcn_perm(r < 8 ? W.I4[i][(r >> 1) & 3] : W.I1[i], r < 8 ? W.R4[i][(r >> 1) & 3] : W.R1[i], (r & 1) ? 0x07060302u : 0x05040100u);
      u32x4 vm = u32x4{v[3], v[2], v[1], v[0]} ^ 0x80000000u;
      if (r >= 1 && r <= 4) { if (dir) st16s(wp, (r - 1) * 524288 + d14 + pd, v); }
      else if (r >= 6) { if (mir) st16s(wp, (9 - r) * 524288 + m14 + pm1, vm); }
      else if (r == 5) {
        if (dir && q <= 32) st16s(wp, d05 + 8192 + pd, v);
        if (mir && qm <= 32) st16s(wp, m05 + 8192 + pm1, vm);
      } else if (t != 0) {
        if (dir && q <= 32) st16s(wp, d05 + pd, v);
        if (mir && qm <= 32) st16s(wp, m05 + pm0, vm);
      }
    }
    if (q != 0 || pass) break;
    wperm_load(W, wr, wi, tb, l, 1);
    asm volatile("s_waitcnt vmcnt(0)" : WPIN(W) :: "memory");
  }
}

template <bool QT>
DI void k1_gemm(char* smem, const u16* x, const u16* gate, char* ws, int m, int nt, int w, int l
                ) {
  const int g = l >> 4, l15 = l & 15, rg = w & 3, chh = w >> 2;
  u32 gxv[5], gwv[5];
  {
    const int grow = 8 * nt + w;
    const char* gx = (const char*)x + (32 * (grow & 63) + 2 * m + (grow >> 6)) * 1280;
    const u32 lo = l * 4;
    asm volatile("global_load_dword %0, %10, %11\n\tglobal_load_dword %1, %10, %11 offset:256\n\tglobal_load_dword %2, %10, %11 offset:512\n\t"
                 "global_load_dword %3, %10, %11 offset:768\n\tglobal_load_dword %4, %10, %11 offset:1024\n\t"
                 "global_load_dword %5, %10, %12\n\tglobal_load_dword %6, %10, %12 offset:256\n\tglobal_load_dword %7, %10, %12 offset:512\n\t"
                 "global_load_dword %8, %10, %12 offset:768\n\tglobal_load_dword %9, %10, %12 offset:1024"
                 : "=&v"(gxv[0]), "=&v"(gxv[1]), "=&v"(gxv[2]), "=&v"(gxv[3]), "=&v"(gxv[4]),
                   "=&v"(gwv[0]), "=&v"(gwv[1]), "=&v"(gwv[2]), "=&v"(gwv[3]), "=&v"(gwv[4]) : "v"(lo), "s"(gx), "s"(gate) : "memory");
  }
  u32 xa[2], wa[4][2];
  xa[0] = rg * 4096 + l15 * 128 + ((g ^ ((l15 >> 1) & 7)) << 4); xa[1] = xa[0] ^ 64;
#pragma unroll
  for (int t2 = 0; t2 < 2; ++t2) {
    const int q4 = l15 >> 2, swz = (q4 << 2) | ((2 * g + t2) & 3);
    const u32 b0 = K1_B + (8 * g + 4 * t2 + q4) * 256 + (((((l >> 1) & 1) | ((QT ? 8 : 4) * chh)) ^ swz) << 4) + 8 * (l & 1);
#pragma unroll
    for (int i = 0; i < 4; ++i) { wa[i][t2] = b0 ^ ((QT ? i : (i & 1) + 4 * (i >> 1)) << 5); PIN(wa[i][t2]); }
  }
  asm volatile("s_waitcnt vmcnt(0)" : "+v"(gxv[0]), "+v"(gxv[1]), "+v"(gxv[2]), "+v"(gxv[3]), "+v"(gxv[4]),
               "+v"(gwv[0]), "+v"(gwv[1]), "+v"(gwv[2]), "+v"(gwv[3]), "+v"(gwv[4]) :: "memory");
  {
    float g0 = 0.f, g1 = 0.f;
#pragma unroll
    for (int i = 0; i < 5; ++i) {
      u32 pr = pk2(lo2f(gxv[i]) * lo2f(gwv[i]), hi2f(gxv[i]) * hi2f(gwv[i]));
      g0 += lo2f(pr); g1 += hi2f(pr);
    }
    g0 += g1;
    g0 += dppf<0xB1>(g0); g0 += dppf<0x4E>(g0); g0 += dppf<0x124>(g0); g0 += dppf<0x128>(g0);
    float tot = __uint_as_float(__builtin_amdgcn_readlane(__float_as_uint(g0), 0)) + __uint_as_float(__builtin_amdgcn_readlane(__float_as_uint(g0), 16))
              + __uint_as_float(__builtin_amdgcn_readlane(__float_as_uint(g0), 32)) + __uint_as_float(__builtin_amdgcn_readlane(__float_as_uint(g0), 48));
    float gt = bfr(__builtin_amdgcn_rcpf(1.f + __builtin_amdgcn_exp2f(bfr(tot) * -1.44269504088896f)));
    if (l == 0) ((float*)(ws + OFF_G))[128 * m + 8 * nt + w] = gt;
  }
  f32x4 acc[2][4];
#pragma unroll
  for (int rt = 0; rt < 2; ++rt)
#pragma unroll
    for (int i = 0; i < 4; ++i) acc[rt][i] = f32x4{0.f, 0.f, 0.f, 0.f};
  Frags F[2];
  auto rd = [&](Frags& f, int C) {
    const int so = (C & 3) * K1_SLOT;
#pragma unroll
    for (int ks = 0; ks < 2; ++ks) {
#pragma unroll
      for (int rt = 0; rt < 2; ++rt) f.x[ks][rt] = ldsr128(smem, xa[ks] + so + rt * 2048);
#pragma unroll
      for (int i = 0; i < 4; ++i) {
        u32x2 a = ldstr(smem, wa[i][0] + so + ks * 8192), b = ldstr(smem, wa[i][1] + so + ks * 8192);
        f.w[ks][i] = u32x4{a[0], a[1], b[0], b[1]};
      }
    }
  };
  auto mm = [&](const Frags& f) {
#pragma unroll
    for (int ks = 0; ks < 2; ++ks)
#pragma unroll
      for (int i = 0; i < 4; ++i)
#pragma unroll
        for (int rt = 0; rt < 2; ++rt)
          acc[rt][i] = QT ? mfma(f.w[ks][i], f.x[ks][rt], acc[rt][i]) : mfma(f.x[ks][rt], f.w[ks][i], acc[rt][i]);
  };
  STAMP();
  RAWBAR();
  STAMP();
  rd(F[0], 0);
  SB();
  u32 eo[2], ra[2], rb[2];
  if (QT) {
    const int ck = 16 * (nt - 11) + 8 * chh + (l & 7), hp = (ck * 205) >> 10;
    eo[0] = (hp * 2048 + 128 * m + 32 * rg + (l >> 3)) * 80 + (ck - 5 * hp) * 16;
    ra[0] = 2 * K1_SLOT + chh * K1_B + rg * 4608 + l15 * 144 + g * 8;
    rb[0] = 2 * K1_SLOT + chh * K1_B + rg * 4608 + (l >> 3) * 144 + (l & 7) * 16;
    PIN(eo[0]); PIN(ra[0]); PIN(rb[0]);
  } else {
#pragma unroll
    for (int k = 0; k < 2; ++k) {
      int P = w + 8 * k;
      int pi = (0x103322211100ull >> (4 * P)) & 15, pj = (0x333232121010ull >> (4 * P)) & 15;
      int d = 16 * pi + 4 * g, e = 16 * pj + l15, hd = (d * 205) >> 12, he = (e * 205) >> 12;
      int h = 3 * nt + hd;
      bool ok = P < 12 && d < 60 && h < 32 && (e == 60 || (e < 60 && he == hd));
      int ee = e == 60 ? 20 : e - 20 * he;
      eo[k] = ok ? (((m * 32 + h) * 21 + ee) * 20 + (d - 20 * hd)) * 4 : ~0u;
      ra[k] = 2 * K1_SLOT + pi * 1024 + l * 16; rb[k] = 2 * K1_SLOT + (4 + pj) * 1024 + l * 16;
      PIN(eo[k]); PIN(ra[k]); PIN(rb[k]);
    }
  }
  SB();
#pragma unroll
  for (int C = 0; C < 10; ++C) {
    Frags& cur = F[C & 1]; Frags& nxt = F[(C + 1) & 1];
    if (C == 8) { mm(cur); SB(); }
    if (C < 9) RAWBAR();
    SB();
    if (C < 9) rd(nxt, C + 1);
    if (C != 8) mm(cur);
    if (C < 8) {
#pragma unroll
      for (int i = 0; i < 4; ++i) {
        __builtin_amdgcn_sched_group_barrier(0x008, 1, 0); __builtin_amdgcn_sched_group_barrier(0x100, 2, 0);
#pragma unroll
        for (int k = 0; k < 3; ++k) { __builtin_amdgcn_sched_group_barrier(0x008, 1, 0); __builtin_amdgcn_sched_group_barrier(0x100, 1, 0); }
      }
    }
    SB();
  }
  STAMP();
  __builtin_amdgcn_s_barrier();
  if (QT) {
    u32x4 pa[4];
#pragma unroll
    for (int i = 0; i < 4; ++i) pa[i] = pkfrag(acc[0][i], acc[1][i]);
    phi4(pa);
#pragma unroll
    for (int i = 0; i < 4; ++i) { ldsw64(smem, ra[0] + i * 32, u32x2{pa[i][0], pa[i][1]}); ldsw64(smem, ra[0] + 2304 + i * 32, u32x2{pa[i][2], pa[i][3]}); }
    u32x4 o[4];
#pragma unroll
    for (int i = 0; i < 4; ++i) o[i] = ldsr128(smem, rb[0] + i * 1152);
    ST16SO(ws + OFF_PQ, eo[0], 0, o[0]); ST16SO(ws + OFF_PQ, eo[0], 640, o[1]); ST16SO(ws + OFF_PQ, eo[0], 1280, o[2]); ST16SO(ws + OFF_PQ, eo[0], 1920, o[3]);
  } else {
    u32x4 v1 = pkfrag(acc[0][3], acc[1][3]);
    if (chh && l15 == 12) v1 = u32x4{0x3f803f80u, 0x3f803f80u, 0x3f803f80u, 0x3f803f80u};
    const u32 fo = 2 * K1_SLOT + (rg * 8 + 2 * chh) * 1024 + l * 16;
    ldsw128(smem, fo + 4096, pkfrag(acc[0][2], acc[1][2]));
    ldsw128(smem, fo + 5120, v1);
    u32x4 pa[2] = {pkfrag(acc[0][0], acc[1][0]), pkfrag(acc[0][1], acc[1][1])};
    phi4(pa);
    ldsw128(smem, fo, pa[0]);
    ldsw128(smem, fo + 1024, pa[1]);
    RAWBAR();
    u32x4 fa[2][4], fb[2][4];
#pragma unroll
    for (int r = 0; r < 4; ++r)
#pragma unroll
      for (int k = 0; k < 2; ++k) { fa[k][r] = ldsr128(smem, ra[k] + r * 8192); fb[k][r] = ldsr128(smem, rb[k] + r * 8192); }
    SB();
    f32x4 r0 = {0.f, 0.f, 0.f, 0.f}, r1 = r0;
#pragma unroll
    for (int r = 0; r < 4; ++r) { r0 = mfma(fa[0][r], fb[0][r], r0); r1 = mfma(fa[1][r], fb[1][r], r1); }
    if (eo[0] != ~0u) *(f32x4*)(ws + OFF_KVP + eo[0]) = r0;
    if (eo[1] != ~0u) *(f32x4*)(ws + OFF_KVP + eo[1]) = r1;
  }
}

template <bool C4>
DI void k1_dft(const char* smem, const char* vb, const u32x4 (&gs)[8], u32 xs0, u32 xs1, u32 vo, u32 kv01, u32 kv2, float tc, float ts, int l) {
  const int g = l >> 4, h = (l >> 3) & 1, j = l & 7;
  u32x4 xq[4][3];
#pragma unroll
  for (int rt = 0; rt < 4; ++rt) { xq[rt][0] = ldsr128(smem, xs0 + rt * 256); xq[rt][1] = ldsr128(smem, xs0 + 4096 + rt * 256); xq[rt][2] = ldsr128(smem, xs1 + rt * 256); }
  SB();
  const u32 hm = (j & 1) ? 0xffff0000u : 0xffffu;
  u32x4 kd[3] = {onehot(((j & 1) ? kv01 << 16 : kv01) & hm, j >> 1), onehot(((j & 1) ? kv01 : kv01 >> 16) & hm, j >> 1), onehot(kv2 & hm, j >> 1)};
  f32x4 t[4];
#pragma unroll
  for (int s = 0; s < 3; ++s)
#pragma unroll
    for (int rt = 0; rt < 4; ++rt) t[rt] = mfma(xq[rt][s], kd[s], s ? t[rt] : f32x4{0.f, 0.f, 0.f, 0.f});
  u32x4 tb0 = pkfrag(t[0], t[1]), tb1 = pkfrag(t[2], t[3]);
  f32x4 y[4];
#pragma unroll
  for (int mt = 0; mt < 4; ++mt) { y[mt] = mfma(gs[2 * mt], tb0, f32x4{0.f, 0.f, 0.f, 0.f}); y[mt] = mfma(gs[2 * mt + 1], tb1, y[mt]); }
#pragma unroll
  for (int mt = 0; mt < 4; ++mt)
#pragma unroll
    for (int rp = 0; rp < 2; ++rp) {
      float o0 = y[mt][2 * rp], o1 = y[mt][2 * rp + 1];
      if (!C4) {
        float p0 = ror8(o0), p1 = ror8(o1), X = o0 - p1, Y = o1 + p0;
        if (mt == 0 && rp == 0) { X = g == 0 ? (h ? o1 : o0) : X; Y = g == 0 ? (h ? p1 : p0) : Y; }
        u32 pk = pk2(X, Y);
        ST4SO(vb, vo, (mt * 2 + rp) * 256, __builtin_amdgcn_alignbit(pk, pk, 16 * h));
      } else {
        if (mt == 0 && rp == 0) {
          if (g == 0) ST4SO(vb, vo, 2048, pk2(o1 * tc, o1 * ts));
          o1 = g == 0 ? 0.f : o1;
        }
        ST4SO(vb, vo, (mt * 2 + rp) * 256, pk2(o0 * tc - o1 * ts, o0 * ts + o1 * tc));
      }
    }
}

__global__ __launch_bounds__(768) __attribute__((amdgpu_waves_per_eu(3, 3)))
void k1(const u16* __restrict__ x, const u16* __restrict__ wqkv, const u16* __restrict__ wr, const u16* __restrict__ wi,
        const u16* __restrict__ gate, char* __restrict__ ws) {
  extern __shared__ __attribute__((aligned(16))) char smem[];
  const int tid = threadIdx.x, l = tid & 63, w = __builtin_amdgcn_readfirstlane(tid >> 6);
  const int b = blockIdx.x, xcd = b & 7, bi = b >> 3;
  const int m = (xcd >> 1) * 4 + (bi & 3), nt = (xcd & 1) * 8 + (bi >> 2);
  asm volatile("" :: "s"(x), "s"(wqkv), "s"(wr), "s"(wi), "s"(gate), "s"(ws));
  STAMP_DECL; STAMP();
#define TSARG
  if (w >= 8) {
    const int v = w - 8;
    int cn = ((nt & 7) * 5 >> 2) + ((m & 3) * 5 >> 1); if (cn >= 10) cn -= 10;
    const int cq = nt >= 11, cbase = cq ? 128 * (nt - 11) : 640 + 60 * nt, cs = cq ? 64 : 640, cmax = nt == 10 ? 4 : 7;
    u32 da[4], db[4];
#pragma unroll
    for (int e = 0; e < 4; ++e) {
      da[e] = (8 * e + (l >> 3)) * 40960 + (((l & 7) ^ ((4 * e + (l >> 4)) & 7)) << 4) - 1024 * e;
      int cbk = (l & 15) ^ (((l >> 4) << 2) | e);
      db[e] = (4 * e + (l >> 4)) * 3840 + ((cbk >> 3) * cs + 8 * min(cbk & 7, cmax)) * 2 - 1024 * e;
    }
    const char* xa = (const char*)x + ((v & 1) * 1024 + 2 * m + (v >> 1)) * 1280;
    const char* wa = (const char*)wqkv + v * (16 * 3840) + cbase * 2;
    const u32 dl = LDSADDR(smem) + v * 4096;
    int cc = cn * 1024;
    auto issue = [&](int slot) {
      dma4(dl + slot * K1_SLOT, xa + cn * 128, da[0], da[1], da[2], da[3]);
      dma4(dl + slot * K1_SLOT + K1_B, wa + cn * (64 * 3840), db[0], db[1], db[2], db[3]);
      cn = cn == 9 ? 0 : cn + 1;
    };
    __builtin_amdgcn_s_setprio(2);
    issue(0); issue(1);
    STAMP();
    const int n2s = nt >> 3, bg = nt & 7;
    const u32 ca = n2s * 8192 + l * 128 + ((bg ^ ((l >> 1) & 7)) << 4), cw = K1_AUX + l * 16;
    u32x4 cv;
    auto step = [&](int C) {
      RAWBAR();
      if (v == 0) {
        if (C >= 0) { ldsw128(smem, cw + cc, cv); cc = cc == 9216 ? 0 : cc + 1024; }
        cv = ldsr128(smem, ca + ((C + 1) & 3) * K1_SLOT);
      }
      if (C < 7) issue((C + 3) & 3);
    };
    WAITV(8); step(-1);
#pragma unroll 1
    for (int C = 0; C < 7; ++C) { WAITV(8); step(C); }
    STAMP();
    u32x4 gs[8];
    const char* gp = (const char*)dGsA.v + l * 16 + 4096;
    GLD(gs[0], gp, -4096); GLD(gs[1], gp, -3072); GLD(gs[2], gp, -2048); GLD(gs[3], gp, -1024);
    GLD(gs[4], gp, 0); GLD(gs[5], gp, 1024); GLD(gs[6], gp, 2048); GLD(gs[7], gp, 3072);
    const int tb = b;
    WRegs W;
    if (v == 3) {
      wperm_load(W, wr, wi, tb, l, 0);
      int ks = (tb >> 2) + 64 * (8 * (tb & 3) + (l & 7)), km = (2048 - ks) & 2047, kd = ((l >> 3) & 1) * 320;
      u32 o0 = (ks * 321 + kd) * 2, o1 = (km * 321 + kd) * 2;
      asm volatile("global_load_ushort %0, %4, %6\n\tglobal_load_ushort %1, %5, %6\n\tglobal_load_ushort %2, %4, %7\n\tglobal_load_ushort %3, %5, %7"
                   : "=&v"(W.sp[0]), "=&v"(W.sp[1]), "=&v"(W.sp[2]), "=&v"(W.sp[3]) : "v"(o0), "v"(o1), "s"(wr), "s"(wi) : "memory");
    }
    const int g = l >> 4, h = (l >> 3) & 1, bb = 8 * bg + (l & 7);
    float c3[3];
#pragma unroll
    for (int s = 0; s < 3; ++s) c3[s] = __builtin_amdgcn_cosf((float)((v + 1) * (64 * (4 * s + g) + bb)) * (1.f / 640.f) - 0.25f * h);
    u32 kv01 = pk2(c3[0], c3[1]), kv2 = g < 2 ? pk2(c3[2], c3[2]) : 0u;
    u32 k4 = (h & g) ? 0xbf80bf80u : 0x3f803f80u;
    float tc = h ? __builtin_amdgcn_cosf(bb * (1.f / 128.f)) : 1.f, ts = h ? __builtin_amdgcn_sinf(bb * (1.f / 128.f)) : 0.f;
    u32 xs0 = K1_AUX + (l & 15) * 16 + g * 1024, xs1 = K1_AUX + (l & 15) * 16 + (g < 2 ? 8 + g : 9) * 1024;
    u32 vo = ((2 * m + n2s) * 8 + bg) * VB_TS + l * 4;
    PIN(kv01); PIN(kv2); PIN(k4); PIN(tc); PIN(ts); PIN(xs0); PIN(xs1); PIN(vo);
    if (v == 3) WAITV(36); else WAITV(16);
    step(7);
    if (v == 3) WAITV(28); else WAITV(8);
    step(8);
    if (v == 0) ldsw128(smem, cw + cc, cv);
    __builtin_amdgcn_s_setprio(0);
    if (v == 3) WAITV(20); else WAITV(0);
    asm volatile("" : "+v"(gs[0]), "+v"(gs[1]), "+v"(gs[2]), "+v"(gs[3]), "+v"(gs[4]), "+v"(gs[5]), "+v"(gs[6]), "+v"(gs[7]));
    RAWBAR();
    STAMP();
    k1_dft<false>(smem, ws + OFF_VB, gs, xs0, xs1, vo + v * 2048, kv01, kv2, 0.f, 0.f, l);
    STAMP();
    if (v == 0 || v == 3) {
      if (nt < 11) __builtin_amdgcn_s_barrier();
      if (v == 0) k1_dft<true>(smem, ws + OFF_VB, gs, xs0, xs1, vo + 8192, k4, g < 2 ? k4 : 0u, tc, ts, l);
      else {
        asm volatile("s_waitcnt vmcnt(8)" : WPIN(W), "+v"(W.sp[0]), "+v"(W.sp[1]), "+v"(W.sp[2]), "+v"(W.sp[3]) :: "memory");
        wperm_store(W, wr, wi, ws, tb, l);
      }
    }
  } else if (nt >= 11) k1_gemm<true>(smem, x, gate, ws, m, nt, w, l TSARG);
  else k1_gemm<false>(smem, x, gate, ws, m, nt, w, l TSARG);
  STAMP();
  STAMP_FLUSH(0);
}

constexpr int K2_V = 0, K2_A = 16384, K2_U = 32768, K2_US = 272, K2_LDS = K2_U + 2 * 32 * K2_US;
constexpr int K2_NF = 168;
constexpr int K2_NO = 64;
constexpr int K2_NA = 20;

__global__ __launch_bounds__(512) __attribute__((amdgpu_waves_per_eu(2, 2)))
void k2(const u16* __restrict__ wo, char* __restrict__ ws) {
  extern __shared__ __attribute__((aligned(16))) char smem[];
  const int tid = threadIdx.x, l = tid & 63, w8 = __builtin_amdgcn_readfirstlane(tid >> 6);
  const int g = l >> 4, l15 = l & 15;
  const int b = blockIdx.x;
  const f32x4 z4 = {0.f, 0.f, 0.f, 0.f};
  asm volatile("" :: "s"(wo), "s"(ws));
  STAMP_DECL; STAMP();
  if (b < K2_NF) {
    const int sidx = (b & 7) * 21 + (b >> 3);
    if (sidx > 160) return;
    const int half = w8 >> 2, w4 = w8 & 3;
    {
      int kc0 = l15 ^ ((8 * w4 + g) & 15), kc1 = kc0 ^ 4;
      u32 v0 = ((8 * w4 + g) * 8 + (kc0 >> 1)) * VB_TS + (kc0 & 1) * 16, v1 = ((8 * w4 + 4 + g) * 8 + (kc1 >> 1)) * VB_TS + (kc1 & 1) * 16 - 1024;
      dma2(LDSADDR(smem) + K2_V + w8 * 2048, ws + OFF_VB + sidx * 64 + half * 32, v0, v1);
    }
    u32x4 tr[4], ti[4];
    { const char* fp = (const char*)dF64.v + w4 * 4096 + l * 16; GLD(tr[0], fp, 0); GLD(tr[1], fp, 1024); GLD(tr[2], fp, 2048); GLD(tr[3], fp, 3072); }
    u32x4 wf[2];
    { const char* wp = ws + OFF_WP + (size_t)sidx * 16384 + half * 8192 + w4 * 1024 + l * 16 + 2048; GLD(wf[0], wp, -2048); GLD(wf[1], wp, 2048); }
    const int c = sidx >> 5 > 4 ? 4 : sidx >> 5;
    const int ci = sidx == 160 ? 32 : ((sidx >> 3) & 3) * 8 + (sidx & 3) * 2 + ((sidx >> 2) & 1);
    const int q = c == 4 ? ci : (half == 0 ? ci : (ci == 0 ? 32 : 64 - ci));
    {
      const int ri = 16 * (w4 & 1) + l15;
      const int B = w4 < 2 ? q + 64 * ri : 64 * ri, A = (w4 < 2 ? 0 : ri * q) + B * 4 * g;
      float cs[8], sn[8];
#pragma unroll
      for (int j = 0; j < 8; ++j) {
        float rev = (float)((A + B * (j < 4 ? j : 12 + j)) & 2047) * (1.f / 2048.f);
        cs[j] = __builtin_amdgcn_cosf(rev); sn[j] = __builtin_amdgcn_sinf(rev);
      }
      ldsw128(smem, K2_A + w8 * 2048 + l * 16, u32x4{pk2(cs[0], cs[1]), pk2(cs[2], cs[3]), pk2(cs[4], cs[5]), pk2(cs[6], cs[7])});
      ldsw128(smem, K2_A + w8 * 2048 + 1024 + l * 16, u32x4{pk2(sn[0], sn[1]), pk2(sn[2], sn[3]), pk2(sn[4], sn[5]), pk2(sn[6], sn[7])});
    }
    const float sc = 2.f / (2048.f * 640.f), s2 = (c == 4 && (ci & 31) == 0) ? 0.5f * sc : sc;
    f32x4 tc, ts;
#pragma unroll
    for (int i = 0; i < 4; ++i) {
      float rev = (float)(16 * w4 + 4 * g + i) * (1.f / 128.f);
      tc[i] = __builtin_amdgcn_cosf(rev) * s2; ts[i] = __builtin_amdgcn_sinf(rev) * s2;
    }
    const int so = sidx == 160 ? 128 : sidx;
    char* dst = ws + OFF_AP + (size_t)((16 * half + l15) * 8 + 2 * w4 + (g >> 1)) * AP_TS + so * 64 + (g & 1) * 32;
    asm volatile("" : "+v"(tc), "+v"(ts), "+v"(dst));
    asm volatile("s_waitcnt vmcnt(2)" : "+v"(tr[0]), "+v"(tr[1]), "+v"(tr[2]), "+v"(tr[3]) :: "memory");
#pragma unroll
    for (int ks = 0; ks < 4; ++ks)
#pragma unroll
      for (int i = 0; i < 4; ++i) ti[ks][i] = __builtin_amdgcn_alignbit(tr[ks][i], tr[ks][i], 16) ^ 0x8000u;
    asm volatile("" : "+v"(ti[0]), "+v"(ti[1]), "+v"(ti[2]), "+v"(ti[3]));
    STAMP();
    RAWBAR();
    STAMP();
    u32x4 vf[2][4], cf[4], sf[4];
#pragma unroll
    for (int ks = 0; ks < 4; ++ks)
#pragma unroll
      for (int mt = 0; mt < 2; ++mt) vf[mt][ks] = ldsr128(smem, K2_V + half * 8192 + (16 * mt + l15) * 256 + (((4 * ks + g) ^ l15) << 4));
#pragma unroll
    for (int i = 0; i < 4; ++i) {
      cf[i] = ldsr128(smem, K2_A + half * 8192 + i * 2048 + l * 16); sf[i] = ldsr128(smem, K2_A + half * 8192 + i * 2048 + 1024 + l * 16);
    }
    SB();
    f32x4 Tr[2] = {z4, z4}, Ti[2] = {z4, z4};
#pragma unroll
    for (int ks = 0; ks < 4; ++ks)
#pragma unroll
      for (int mt = 0; mt < 2; ++mt) { Tr[mt] = mfma(vf[mt][ks], tr[ks], Tr[mt]); Ti[mt] = mfma(vf[mt][ks], ti[ks], Ti[mt]); }
    u32x4 br = pkfrag(Tr[0], Tr[1]), bi = pkfrag(Ti[0], Ti[1]), ni = bi ^ 0x80008000u;
    f32x4 zr[2], zn[2];
#pragma unroll
    for (int mt = 0; mt < 2; ++mt) {
      zr[mt] = mfma(cf[mt], br, z4); zn[mt] = mfma(cf[mt], bi, z4);
      zr[mt] = mfma(sf[mt], ni, zr[mt]); zn[mt] = mfma(sf[mt], br, zn[mt]);
    }
    asm volatile("s_waitcnt vmcnt(0)" : "+v"(wf[0]), "+v"(wf[1]) :: "memory");
    f32x4 Yr[2], Yi[2];
#pragma unroll
    for (int mt = 0; mt < 2; ++mt)
#pragma unroll
      for (int i = 0; i < 4; ++i) {
        float a = lo2f(wf[mt][i]), bq = hi2f(wf[mt][i]);
        Yr[mt][i] = zr[mt][i] * a + zn[mt][i] * bq; Yi[mt][i] = zr[mt][i] * bq - zn[mt][i] * a;
      }
    br = pkfrag(Yr[0], Yr[1]); bi = pkfrag(Yi[0], Yi[1]); ni = bi ^ 0x80008000u;
#pragma unroll
    for (int mt = 0; mt < 2; ++mt) {
      f32x4 ur = mfma(cf[2 + mt], br, z4), ui = mfma(cf[2 + mt], bi, z4);
      ur = mfma(sf[2 + mt], ni, ur); ui = mfma(sf[2 + mt], br, ui);
#pragma unroll
      for (int i = 0; i < 4; ++i)
        *(LDS_AS u32*)((LDS_AS char*)smem + K2_U + half * (32 * K2_US) + (16 * mt + 4 * g + i) * K2_US + (16 * w4 + l15) * 4) = pk2(ur[i], ui[i]);
    }
    STAMP();
    RAWBAR();
    STAMP();
    u32x4 uf[2][4];
#pragma unroll
    for (int ks = 0; ks < 4; ++ks)
#pragma unroll
      for (int cl = 0; cl < 2; ++cl) uf[cl][ks] = ldsr128(smem, K2_U + cl * (32 * K2_US) + (16 * half + l15) * K2_US + (4 * ks + g) * 16);
    SB();
    f32x4 Rr[2] = {z4, z4}, Ri[2] = {z4, z4};
#pragma unroll
    for (int ks = 0; ks < 4; ++ks)
#pragma unroll
      for (int cl = 0; cl < 2; ++cl) { Rr[cl] = mfma(tr[ks], uf[cl][ks], Rr[cl]); Ri[cl] = mfma(ti[ks], uf[cl][ks], Ri[cl]); }
    f32x4 e00, e01, e10, e11;
    if (c < 4) {
      f32x4 ar = Rr[0] * sc, ai = Ri[0] * -sc;
      if (ci > 0) { e00 = ar + Rr[1] * sc; e01 = ai - Ri[1] * sc; e10 = ai + Ri[1] * sc; e11 = Rr[1] * sc - ar; }
      else { e00 = ar; e01 = ai; e10 = Rr[1] * sc; e11 = Ri[1] * -sc; }
    } else {
      e00 = Rr[0] * s2; e01 = Rr[1] * tc - Ri[1] * ts;
      if (ci & 31) { e10 = Ri[0] * -s2; e11 = -(Rr[1] * ts + Ri[1] * tc); }
      else { e10 = e00; e11 = e01; }
    }
    if (sidx != 160) *(u32x4*)dst = pkfrag(e00, e01);
    if (sidx != 128) *(u32x4*)(dst + 16) = pkfrag(e10, e11);
    STAMP();
  } else if (b < K2_NF + K2_NO) {
    const int ob = b - K2_NF, hp = ob & 15, n2 = 8 * (ob >> 4) + w8;
    u32x4 s[8];
    const int rh = tid >= 256, rp = tid & 255;
    if (rp < 210) {
      const char* kb = ws + OFF_KVP + hp * 3360;
      const u32 ko = rp * 16 + rh * (8 * 53760);
#pragma unroll
      for (int mm = 0; mm < 8; ++mm) asm volatile("global_load_dwordx4 %0, %1, %2" : "=v"(s[mm]) : "v"(ko), "s"(kb + mm * 53760) : "memory");
    }
    const char* src = ws + OFF_PQ + ((size_t)hp * 2048 + 64 * n2) * 80;
    const u32 pl = 8192 + w8 * 5136;
    dma4(LDSADDR(smem) + pl, src, l * 16, l * 16, l * 16, l * 16);
    dma1(LDSADDR(smem) + pl + 4096, src + 4096, l * 16);
    const int gc = g > 2 ? 2 : g;
    const u32 m0 = g < 3 ? ~0u : 0u, m1 = g < 2 ? ~0u : 0u;
    u32 oo = (n2 * 16 + hp) * 5120 + l * 16;
    u32 a0 = l15 * 48 + gc * 16, a1 = (16 + (l15 & 3)) * 48 + gc * 16, a2 = 960 + gc * 16;
    u32 pr = pl + l15 * 80 + 16 * gc, pw = pl + l15 * 80 + 8 * g, pw2 = pl + l15 * 80 + 32 + 4 * (g & 1), pc = pl + l * 16;
    const int u_ = rp >= 105, in = rp - 105 * u_, e_ = (in * 205) >> 10;
    u32 kw = u_ * 1024 + e_ * 48 + (in - 5 * e_) * 8;
    PIN(oo); PIN(a0); PIN(a1); PIN(a2); PIN(pr); PIN(pw); PIN(pw2); PIN(pc); PIN(kw);
    asm volatile("s_waitcnt vmcnt(5)" : "+v"(s[0]), "+v"(s[1]), "+v"(s[2]), "+v"(s[3]), "+v"(s[4]), "+v"(s[5]), "+v"(s[6]), "+v"(s[7]) :: "memory");
    f32x4 sum = ((__builtin_bit_cast(f32x4, s[0]) + __builtin_bit_cast(f32x4, s[1])) + (__builtin_bit_cast(f32x4, s[2]) + __builtin_bit_cast(f32x4, s[3])))
              + ((__builtin_bit_cast(f32x4, s[4]) + __builtin_bit_cast(f32x4, s[5])) + (__builtin_bit_cast(f32x4, s[6]) + __builtin_bit_cast(f32x4, s[7])));
    if (rh) *(LDS_AS f32x4*)((LDS_AS char*)smem + 2048 + rp * 16) = sum;
    RAWBAR();
    if (!rh && rp < 210) {
      sum += *(const LDS_AS f32x4*)((const LDS_AS char*)smem + 2048 + rp * 16);
      ldsw64(smem, kw, u32x2{pk2(sum[0], sum[1]), pk2(sum[2], sum[3])});
    }
    STAMP();
    WAITV(0);
    u32x4 af[2][3], pf[4][2];
#pragma unroll
    for (int rt = 0; rt < 4; ++rt)
#pragma unroll
      for (int u = 0; u < 2; ++u) {
        u32x2 a = ldsr64(smem, pr + rt * 1280 + 40 * u), bq = ldsr64(smem, pr + rt * 1280 + 40 * u + 8);
        pf[rt][u] = u32x4{a[0], a[1], bq[0], bq[1]};
      }
    RAWBAR();
    STAMP();
#pragma unroll
    for (int u = 0; u < 2; ++u) { af[u][2] = ldsr128(smem, a2 + u * 1024); af[u][0] = ldsr128(smem, a0 + u * 1024); af[u][1] = ldsr128(smem, a1 + u * 1024); }
    SB();
    pf[3][1] &= u32x4{~0u, ~0u, m1, m1};
#pragma unroll
    for (int u = 0; u < 2; ++u)
#pragma unroll
      for (int tI = 0; tI < 3; ++tI) af[u][tI] &= u32x4{m0, m0, m1, m1};
    f32x4 n0[4][2], n1[4][2], dn[4][2];
#pragma unroll
    for (int u = 0; u < 2; ++u)
#pragma unroll
      for (int rt = 0; rt < 4; ++rt) dn[rt][u] = mfma(af[u][2], pf[rt][u], z4);
#pragma unroll
    for (int u = 0; u < 2; ++u)
#pragma unroll
      for (int rt = 0; rt < 4; ++rt) { n0[rt][u] = mfma(af[u][0], pf[rt][u], z4); n1[rt][u] = mfma(af[u][1], pf[rt][u], z4); }
#pragma unroll
    for (int u = 0; u < 2; ++u)
#pragma unroll
      for (int rt = 0; rt < 4; ++rt) {
        float rd = __builtin_amdgcn_rcpf(bfr(bfr(dn[rt][u][0]) + 1e-6f));
        u32 b0 = pk2(n0[rt][u][0], n0[rt][u][1]), b1 = pk2(n0[rt][u][2], n0[rt][u][3]);
        u32 b2 = pk2(n1[rt][u][g & 1 ? 2 : 0], n1[rt][u][g & 1 ? 3 : 1]);
        ldsw64(smem, pw + rt * 1280 + 40 * u, u32x2{pk2(lo2f(b0) * rd, hi2f(b0) * rd), pk2(lo2f(b1) * rd, hi2f(b1) * rd)});
        *(LDS_AS u32*)((LDS_AS char*)smem + pw2 + rt * 1280 + 40 * u) = pk2(lo2f(b2) * rd, hi2f(b2) * rd);
      }
    u32x4 o[5];
#pragma unroll
    for (int i = 0; i < 5; ++i) o[i] = ldsr128(smem, pc + i * 1024);
    ST16SO(ws + OFF_O2, oo, 0, o[0]); ST16SO(ws + OFF_O2, oo, 1024, o[1]); ST16SO(ws + OFF_O2, oo, 2048, o[2]); ST16SO(ws + OFF_O2, oo, 3072, o[3]);
    ST16SO(ws + OFF_O2 + 4096, oo, 0, o[4]);
    STAMP();
  } else {
    const int ks = b - K2_NF - K2_NO, Q = ks / 5, wq = ks - 5 * Q;
    const char* src = (const char*)wo + ((4 * Q + (w8 >> 1)) * 40 + 8 * wq + 4 * (w8 & 1)) * 1280;
    dma4(LDSADDR(smem) + w8 * 5120, src, l * 16, l * 16, l * 16, l * 16);
    dma1(LDSADDR(smem) + w8 * 5120 + 4096, src + 4096, l * 16);
    const u32 ta = (8 * g + (l15 >> 2)) * 1280 + ((l >> 1) & 1) * 128 + w8 * 16 + (l & 1) * 8;
    const u32 oo = (w8 * 100 + ks) * 1024 + l * 16;
    WAITV(0);
    RAWBAR();
#pragma unroll
    for (int ct = 0; ct < 5; ++ct) {
      u32x2 a = ldstr(smem, ta + ct * 256), bq = ldstr(smem, ta + ct * 256 + 5120);
      st16s(ws + OFF_WOF + ct * 20480, oo, u32x4{a[0], a[1], bq[0], bq[1]});
    }
    STAMP();
  }
  STAMP_FLUSH(1);
}

constexpr int K3_Y = 81920, K3_AP = K3_Y + 20480, K3_GS = K3_AP + 10240, K3_LDS = K3_GS + 8192;
#define WAIT5(n, a) asm volatile("s_waitcnt vmcnt(" #n ")" : "+v"(a[0]), "+v"(a[1]), "+v"(a[2]), "+v"(a[3]), "+v"(a[4]) :: "memory")

__global__ __launch_bounds__(640) __attribute__((amdgpu_waves_per_eu(3, 3)))
void k3(const char* __restrict__ ws, u16* __restrict__ out) {
  extern __shared__ __attribute__((aligned(16))) char smem[];
  const int tid = threadIdx.x, l = tid & 63, w = __builtin_amdgcn_readfirstlane(tid >> 6);
  const int g = l >> 4, l15 = l & 15;
  const int b = blockIdx.x, n2 = 4 * (b & 7) + ((b >> 3) & 3), bg = b >> 5;
  const f32x4 z4 = {0.f, 0.f, 0.f, 0.f};
  asm volatile("" :: "s"(ws), "s"(out));
  STAMP_DECL; STAMP();
  if (w >= 5) {
    __builtin_amdgcn_s_barrier();
    const int ct = w - 5;
    u32x4 wf[4][5];
    const char* wp = ws + OFF_WOF + ((size_t)((bg * 5 + ct) * 20) * 64 + l) * 16;
    const char* op = ws + OFF_O2 + (size_t)n2 * 81920 + ct * 4096;
    u32 gv[4];
    const char* gp = ws + OFF_G + (64 * n2 + l15) * 4;
    const int rot = (bg + (b >> 3)) & 3;
    auto issue = [&](int Q) {
      const int Qd = (Q + rot) & 3;
      dma4(LDSADDR(smem) + Q * 20480 + ct * 4096, op + Qd * 20480, l * 16, l * 16, l * 16, l * 16);
      const char* p = wp + Qd * 5120 + 2048;
      GLD(wf[Q][0], p, -2048); GLD(wf[Q][1], p, -1024); GLD(wf[Q][2], p, 0); GLD(wf[Q][3], p, 1024); GLD(wf[Q][4], p, 2048);
    };
    issue(0);
    __builtin_amdgcn_s_barrier();
    WAITV(4);
    issue(1);
    u16* op2 = out + (32 * l15 + n2) * 640 + 64 * (2 * ct + (g >> 1)) + 8 * bg + 4 * (g & 1);
    STAMP();
    f32x4 acc[4] = {z4, z4, z4, z4};
#pragma unroll
    for (int Q = 0; Q < 4; ++Q) {
      if (Q == 0) WAIT5(9, wf[0]); else if (Q == 1) WAIT5(9, wf[1]); else if (Q == 2) WAIT5(13, wf[2]); else WAIT5(4, wf[3]);
      RAWBAR();
      STAMP();
      if (Q < 2) issue(Q + 2);
      if (Q == 1) { GLD1(gv[0], gp, 0); GLD1(gv[1], gp, 64); GLD1(gv[2], gp, 128); GLD1(gv[3], gp, 192); }
      u32x4 of[5][4];
#pragma unroll
      for (int wv = 0; wv < 3; ++wv)
#pragma unroll
        for (int rt = 0; rt < 4; ++rt) of[wv][rt] = ldsr128(smem, (4 * Q + g) * 5120 + (16 * rt + l15) * 80 + wv * 16);
      SB();
#pragma unroll
      for (int wv = 0; wv < 2; ++wv)
#pragma unroll
        for (int rt = 0; rt < 4; ++rt) {
          acc[rt] = mfma(wf[Q][wv], of[wv][rt], acc[rt]);
          SB();
          of[3 + wv][rt] = ldsr128(smem, (4 * Q + g) * 5120 + (16 * rt + l15) * 80 + (3 + wv) * 16);
          SB();
        }
#pragma unroll
      for (int wv = 2; wv < 5; ++wv)
#pragma unroll
        for (int rt = 0; rt < 4; ++rt) acc[rt] = mfma(wf[Q][wv], of[wv][rt], acc[rt]);
      SB();
    }
    f32x4 yb[4];
#pragma unroll
    for (int rt = 0; rt < 4; ++rt) yb[rt] = *(const LDS_AS f32x4*)((const LDS_AS char*)smem + K3_Y + (ct * 4 + rt) * 1024 + l * 16);
    asm volatile("s_waitcnt vmcnt(0)" : "+v"(gv[0]), "+v"(gv[1]), "+v"(gv[2]), "+v"(gv[3]) :: "memory");
    STAMP();
#pragma unroll
    for (int rt = 0; rt < 4; ++rt) {
      float gg = __uint_as_float(gv[rt]);
      u32 a0 = pk2(acc[rt][0], acc[rt][1]), a1 = pk2(acc[rt][2], acc[rt][3]);
      a0 = pk2(gg * lo2f(a0), gg * hi2f(a0)); a1 = pk2(gg * lo2f(a1), gg * hi2f(a1));
      u32x2 v = {pk2(lo2f(a0) + yb[rt][0], hi2f(a0) + yb[rt][1]), pk2(lo2f(a1) + yb[rt][2], hi2f(a1) + yb[rt][3])};
      *(u32x2*)(op2 + rt * (512 * 640)) = v;
    }
    STAMP();
  } else {
    const int ct = w;
    dma2(LDSADDR(smem) + K3_AP + ct * 2048, ws + OFF_AP + (size_t)(n2 * 8 + bg) * AP_TS + ct * 2048, l * 16, l * 16);
    if (ct < 4) dma2(LDSADDR(smem) + K3_GS + ct * 2048, (const char*)dGsB.v + ct * 2048, l * 16, l * 16);
    float gt[4];
#pragma unroll
    for (int rt = 0; rt < 4; ++rt) gt[rt] = ((const float*)(ws + OFF_G))[64 * n2 + 16 * rt + l15];
    SB();
    __builtin_amdgcn_s_barrier();
    u32x4 kd[3];
    const int a = 2 * ct + (l15 >> 3), j = l15 & 7, d = 64 * a + 8 * bg + j;
#pragma unroll
    for (int s = 0; s < 3; ++s) {
      int kc = 4 * s + g, c = kc >> 1;
      float rev = c < 4 ? (float)((c + 1) * d) * (1.f / 640.f) : 0.f;
      float v0 = __builtin_amdgcn_cosf(rev), v1 = c < 4 ? __builtin_amdgcn_sinf(rev) : ((a & 1) ? -1.f : 1.f);
      bool on = kc < 10 && (kc & 1) == (j >> 2);
      u32 pp = on ? pk2(v0, v1) : 0u;
      u32 p0 = (pp & 0xffffu) << ((j & 1) * 16), p1 = (pp >> 16) << ((j & 1) * 16);
      kd[s] = (j & 2) ? u32x4{0u, p0, 0u, p1} : u32x4{p0, 0u, p1, 0u};
    }
#pragma unroll
    for (int rt = 0; rt < 4; ++rt) gt[rt] = bfr(1.f - gt[rt]);
    asm volatile("" : "+v"(gt[0]), "+v"(gt[1]), "+v"(gt[2]), "+v"(gt[3]), "+v"(kd[0]), "+v"(kd[1]), "+v"(kd[2]));
    STAMP();
    WAITV(0);
    RAWBAR();
    u32x4 af[4][3], gs[8];
    const u32 ao = K3_AP + ((l15 >> 1) & 1) * 256 + (l15 >> 2) * 64 + (l15 & 1) * 16 + (g >> 1) * 2048 + (g & 1) * 32;
    const u32 ao2 = K3_AP + ((l15 >> 1) & 1) * 256 + (l15 >> 2) * 64 + (l15 & 1) * 16 + 8192 + (g & 1) * 32;
#pragma unroll
    for (int rt = 0; rt < 4; ++rt) { af[rt][0] = ldsr128(smem, ao + rt * 512); af[rt][1] = ldsr128(smem, ao + 4096 + rt * 512); af[rt][2] = ldsr128(smem, ao2 + rt * 512); }
#pragma unroll
    for (int f = 0; f < 8; ++f) gs[f] = ldsr128(smem, K3_GS + f * 1024 + l * 16);
    SB();
    f32x4 P[4];
#pragma unroll
    for (int s = 0; s < 3; ++s)
#pragma unroll
      for (int rt = 0; rt < 4; ++rt) P[rt] = mfma(af[rt][s], kd[s], s ? P[rt] : z4);
    u32x4 p0 = pkfrag(P[0], P[1]), p1 = pkfrag(P[2], P[3]);
#pragma unroll
    for (int nt = 0; nt < 4; ++nt) {
      f32x4 yb = mfma(p0, gs[2 * nt], z4); yb = mfma(p1, gs[2 * nt + 1], yb);
      u32 b0 = pk2(yb[0], yb[1]), b1 = pk2(yb[2], yb[3]);
      b0 = pk2(gt[nt] * lo2f(b0), gt[nt] * hi2f(b0)); b1 = pk2(gt[nt] * lo2f(b1), gt[nt] * hi2f(b1));
      *(LDS_AS f32x4*)((LDS_AS char*)smem + K3_Y + (ct * 4 + nt) * 1024 + l * 16) = f32x4{lo2f(b0), hi2f(b0), lo2f(b1), hi2f(b1)};
    }
    __builtin_amdgcn_s_waitcnt(0xC07F);
    STAMP();
  }
  STAMP_FLUSH(2);
}

extern "C" void kernel_launch(void* const* d_in, const int* in_sizes, int n_in, void* d_out, int out_size, void* d_ws,
                              size_t ws_size, hipStream_t stream) {
  const u16* x = (const u16*)d_in[0];
  const u16* wqkv = (const u16*)d_in[1];
  const u16* wo = (const u16*)d_in[2];
  const u16* wr = (const u16*)d_in[4];
  const u16* wi = (const u16*)d_in[5];
  const u16* gate = (const u16*)d_in[6];
  char* ws = (char*)d_ws;
  (void)hipFuncSetAttribute((const void*)k1, hipFuncAttributeMaxDynamicSharedMemorySize, K1_LDS);
  (void)hipFuncSetAttribute((const void*)k2, hipFuncAttributeMaxDynamicSharedMemorySize, K2_LDS);
  (void)hipFuncSetAttribute((const void*)k3, hipFuncAttributeMaxDynamicSharedMemorySize, K3_LDS);
  k1<<<256, 768, K1_LDS, stream>>>(x, wqkv, wr, wi, gate, ws);
  k2<<<K2_NF + K2_NO + K2_NA, 512, K2_LDS, stream>>>(wo, ws);
  k3<<<256, 640, K3_LDS, stream>>>(ws, (u16*)d_out);
}
```

```cpp
#include <hip/hip_runtime.h>
#include <stdint.h>

typedef unsigned short u16;
typedef unsigned int u32;
typedef short s16x4 __attribute__((ext_vector_type(4)));
typedef short s16x8 __attribute__((ext_vector_type(8)));
typedef __bf16 bf16x8 __attribute__((ext_vector_type(8)));
typedef __bf16 bf16x2 __attribute__((ext_vector_type(2)));
typedef float f32x4 __attribute__((ext_vector_type(4)));
typedef float f32x2 __attribute__((ext_vector_type(2)));
typedef u32 u32x4 __attribute__((ext_vector_type(4)));
typedef u32 u32x2 __attribute__((ext_vector_type(2)));

#define LDS_AS __attribute__((address_space(3)))
#define GLB_AS __attribute__((address_space(1)))
#define DI __device__ __forceinline__
#define STAMP_DECL
#define STAMP()
#define STAMP_FLUSH(K)
__device__ __forceinline__ void dma1(u32 lds, const void* base, u32 v0) {
  u32 keep;
  asm volatile("s_mov_b32 %0, m0\n\ts_mov_b32 m0, %1\n\ts_nop 4\n\tglobal_load_lds_dwordx4 %2, %3\n\ts_mov_b32 m0, %0"
               : "=&s"(keep) : "s"(lds), "v"(v0), "s"(base) : "memory");
}
__device__ __forceinline__ void dma4(u32 lds, const void* base, u32 v0, u32 v1, u32 v2, u32 v3) {
  u32 keep;
  asm volatile("s_mov_b32 %0, m0\n\ts_mov_b32 m0, %1\n\ts_nop 4\n\tglobal_load_lds_dwordx4 %2, %6\n\t"
               "global_load_lds_dwordx4 %3, %6 offset:1024\n\tglobal_load_lds_dwordx4 %4, %6 offset:2048\n\t"
               "global_load_lds_dwordx4 %5, %6 offset:3072\n\ts_mov_b32 m0, %0"
               : "=&s"(keep) : "s"(lds), "v"(v0), "v"(v1), "v"(v2), "v"(v3), "s"(base) : "memory");
}
__device__ __forceinline__ void dma2(u32 lds, const void* base, u32 v0, u32 v1) {
  u32 keep;
  asm volatile("s_mov_b32 %0, m0\n\ts_mov_b32 m0, %1\n\ts_nop 4\n\tglobal_load_lds_dwordx4 %2, %4\n\t"
               "global_load_lds_dwordx4 %3, %4 offset:1024\n\ts_mov_b32 m0, %0"
               : "=&s"(keep) : "s"(lds), "v"(v0), "v"(v1), "s"(base) : "memory");
}
__device__ __forceinline__ void dma1v(u32 lds, const void* src) {
  u32 keep;
  asm volatile("s_mov_b32 %0, m0\n\ts_mov_b32 m0, %1\n\ts_nop 0\n\tglobal_load_lds_dwordx4 %2, off\n\ts_mov_b32 m0, %0"
               : "=&s"(keep) : "s"(lds), "v"(src) : "memory");
}
#define GLD(dst, p, imm) asm volatile("global_load_dwordx4 %0, %1, off offset:" #imm : "=v"(dst) : "v"(p) : "memory")
#define GLD1(dst, p, imm) asm volatile("global_load_dword %0, %1, off offset:" #imm : "=v"(dst) : "v"(p) : "memory")
#define GLDH(dst, p) asm volatile("global_load_ushort %0, %1, off" : "=v"(dst) : "v"(p) : "memory")
#define LDSADDR(p) ((u32)(uintptr_t)(LDS_AS char*)(p))
#define SB() __builtin_amdgcn_sched_barrier(0)
#define WAITV(n) asm volatile("s_waitcnt vmcnt(" #n ")" ::: "memory")
#define RAWBAR() do { asm volatile("" ::: "memory"); __builtin_amdgcn_s_waitcnt(0xC07F); __builtin_amdgcn_s_barrier(); asm volatile("" ::: "memory"); } while (0)

constexpr size_t OFF_PQ  = 0;
constexpr size_t OFF_O2  = OFF_PQ + 2621440;
constexpr size_t OFF_KVP = OFF_O2 + 2621440;
constexpr size_t OFF_VB  = OFF_KVP + 860160;
constexpr int    VB_TS   = 10368;
constexpr size_t OFF_AP  = OFF_VB + (size_t)256 * VB_TS;
constexpr int    AP_TS   = 10240;
constexpr size_t OFF_WP  = OFF_AP + (size_t)256 * AP_TS;
constexpr size_t OFF_WOF = OFF_WP + (size_t)161 * 16384;
constexpr size_t OFF_G   = OFF_WOF + 819200;

constexpr double kPi = 3.14159265358979323846;
constexpr double tcos(double x) {
  double x2 = x * x, term = 1.0, s = 1.0;
  for (int i = 1; i < 14; ++i) { term *= -x2 / ((2 * i - 1) * (2 * i)); s += term; }
  return s;
}
constexpr double cos64(int k) {
  k &= 63; if (k > 32) k = 64 - k;
  if (k == 16) return 0.0;
  return (k < 16) ? tcos(2 * kPi * k / 64) : -tcos(2 * kPi * (32 - k) / 64);
}
constexpr u16 tobf(double v) {
  u32 u = __builtin_bit_cast(u32, (float)v);
  return (u16)((u + 0x7fffu + ((u >> 16) & 1u)) >> 16);
}
struct C64 { u16 c[64]; };
constexpr C64 mkC64() { C64 t{}; for (int i = 0; i < 64; ++i) t.c[i] = tobf(cos64(i)); return t; }
constexpr C64 kC64 = mkC64();
constexpr u16 gsv(int qp, int n1) {
  int i = qp >> 1;
  if (qp == 1) return kC64.c[(32 * n1) & 63];
  return (qp & 1) ? kC64.c[(i * n1 + 48) & 63] : kC64.c[(i * n1) & 63];
}
constexpr int kperm(int s2, int g, int j) { return 32 * s2 + (j < 4 ? 4 * g + j : 12 + 4 * g + j); }
struct T16 { u16 v[4096]; };
struct T32 { u32 v[4096]; };
constexpr T16 mkGsA() { T16 t{};
  for (int f = 0; f < 8; ++f) for (int l = 0; l < 64; ++l) for (int j = 0; j < 8; ++j)
    t.v[(f * 64 + l) * 8 + j] = gsv(16 * (f >> 1) + (l & 15), kperm(f & 1, l >> 4, j));
  return t; }
constexpr T16 mkGsB() { T16 t{};
  for (int f = 0; f < 8; ++f) for (int l = 0; l < 64; ++l) for (int j = 0; j < 8; ++j)
    t.v[(f * 64 + l) * 8 + j] = gsv(kperm(f & 1, l >> 4, j), 16 * (f >> 1) + (l & 15));
  return t; }
constexpr T32 mkF64() { T32 t{};
  for (int f = 0; f < 16; ++f) for (int l = 0; l < 64; ++l) for (int j = 0; j < 4; ++j) {
    int u = 16 * (f >> 2) + (l & 15), v = 4 * (4 * (f & 3) + (l >> 4)) + j;
    t.v[(f * 64 + l) * 4 + j] = (u32)kC64.c[(u * v) & 63] | ((u32)kC64.c[(u * v + 16) & 63] << 16);
  }
  return t; }
__device__ __attribute__((aligned(128))) const T16 dGsA = mkGsA();
__device__ __attribute__((aligned(128))) const T16 dGsB = mkGsB();
__device__ __attribute__((aligned(128))) const T32 dF64 = mkF64();

DI f32x4 mfma(u32x4 a, u32x4 b, f32x4 c) {
  return __builtin_amdgcn_mfma_f32_16x16x32_bf16(__builtin_bit_cast(bf16x8, a), __builtin_bit_cast(bf16x8, b), c, 0, 0, 0);
}
DI u32 pk2(float lo, float hi) { f32x2 v = {lo, hi}; return __builtin_bit_cast(u32, __builtin_convertvector(v, bf16x2)); }
DI float lo2f(u32 p) { return __uint_as_float(p << 16); }
DI float hi2f(u32 p) { return __uint_as_float(p & 0xffff0000u); }
DI float bfr(float x) { return lo2f(pk2(x, x)); }
DI u32x4 pkfrag(f32x4 a, f32x4 b) { u32x4 r = {pk2(a[0], a[1]), pk2(a[2], a[3]), pk2(b[0], b[1]), pk2(b[2], b[3])}; return r; }
DI float ror8(float v) { return __int_as_float(__builtin_amdgcn_update_dpp(0, __float_as_int(v), 0x128, 0xf, 0xf, true)); }
DI u32x4 ldsr128(const char* smem, u32 off) { return *(const LDS_AS u32x4*)((const LDS_AS char*)smem + off); }
DI u32x2 ldstr(const char* smem, u32 off) {
  return __builtin_bit_cast(u32x2, __builtin_amdgcn_ds_read_tr16_b64_v4i16((LDS_AS s16x4*)((LDS_AS char*)smem + off)));
}
DI void st16wt(void* p, u32x4 v) { asm volatile("global_store_dwordx4 %0, %1, off sc1\n\ts_nop 1" :: "v"(p), "v"(v) : "memory"); }
DI void st4wt(void* p, u32 v) { asm volatile("global_store_dword %0, %1, off sc1" :: "v"(p), "v"(v) : "memory"); }
DI void l2touch(u32 lds, const void* p) {
  u32 keep;
  asm volatile("s_mov_b32 %0, m0\n\ts_mov_b32 m0, %1\n\ts_nop 0\n\tglobal_load_lds_dword %2, off\n\ts_mov_b32 m0, %0"
               : "=&s"(keep) : "s"(lds), "v"(p) : "memory");
}
DI void st16p(char* b, u32 o, f32x4 v) { *(f32x4*)(b + (size_t)o) = v; }
DI void st16s(const void* b, u32 o, u32x4 v) { asm volatile("global_store_dwordx4 %0, %1, %2 sc1\n\ts_nop 1" :: "v"(o), "v"(v), "s"(b) : "memory"); }
#define ST16SO(b, o, imm, v) asm volatile("global_store_dwordx4 %0, %1, %2 offset:" #imm " sc1\n\ts_nop 1" :: "v"(o), "v"(v), "s"(b) : "memory")
#define ST4SO(b, o, imm, v) asm volatile("global_store_dword %0, %1, %2 offset:%3 sc1" :: "v"(o), "v"(v), "s"(b), "i"(imm) : "memory")
#define ST4O(p, imm, v) asm volatile("global_store_dword %0, %1, off offset:%2 sc1" :: "v"(p), "v"(v), "i"(imm) : "memory")
DI void ldsw128(char* smem, u32 off, u32x4 v) { *(LDS_AS u32x4*)((LDS_AS char*)smem + off) = v; }
DI void ldsw64(char* smem, u32 off, u32x2 v) { *(LDS_AS u32x2*)((LDS_AS char*)smem + off) = v; }
DI u32x2 ldsr64(const char* smem, u32 off) { return *(const LDS_AS u32x2*)((const LDS_AS char*)smem + off); }
DI u32 ldsr16(const char* smem, u32 off) { return *(const LDS_AS u16*)((const LDS_AS char*)smem + off); }
template <int N> DI void phi4(u32x4 (&p)[N]) {
  f32x2 t[4 * N], e[4 * N];
#pragma unroll
  for (int i = 0; i < 4 * N; ++i) t[i] = f32x2{lo2f(p[i >> 2][i & 3]), hi2f(p[i >> 2][i & 3])};
  SB();
#pragma unroll
  for (int i = 0; i < 4 * N; ++i) e[i] = t[i] * 1.44269504088896f;
  SB();
#pragma unroll
  for (int i = 0; i < 4 * N; ++i) {
    e[i][0] = __builtin_amdgcn_fmed3f(__builtin_amdgcn_exp2f(e[i][0]), 0.f, 1.f); e[i][1] = __builtin_amdgcn_fmed3f(__builtin_amdgcn_exp2f(e[i][1]), 0.f, 1.f);
    t[i][0] = __builtin_fmaxf(t[i][0], 0.f); t[i][1] = __builtin_fmaxf(t[i][1], 0.f);
  }
  SB();
  float mn = 1.f;
#pragma unroll
  for (int i = 0; i < 4 * N; ++i) { mn = __builtin_fminf(__builtin_fminf(mn, e[i][0]), e[i][1]); e[i] += t[i]; }
  SB();
#pragma unroll
  for (int i = 0; i < 4 * N; ++i) p[i >> 2][i & 3] = pk2(e[i][0], e[i][1]);
  if (__builtin_amdgcn_ballot_w64(mn < 0.001f)) {
#pragma unroll
    for (int n = 0; n < N; ++n)
#pragma unroll
      for (int i = 0; i < 4; ++i) p[n][i] = pk2(lo2f(p[n][i]) + 1e-6f, hi2f(p[n][i]) + 1e-6f);
  }
}
template <int CTRL> DI float dppf(float v) { return __int_as_float(__builtin_amdgcn_update_dpp(0, __float_as_int(v), CTRL, 0xf, 0xf, true)); }
DI u32x4 onehot(u32 s, int d) { u32x4 r = {d == 0 ? s : 0u, d == 1 ? s : 0u, d == 2 ? s : 0u, d == 3 ? s : 0u}; return r; }

constexpr int K1_SLOT = 16384, K1_B = 65536, K1_AUX = 131072, K1_LDS = K1_AUX + 272;

struct Frags { u32x4 x[2][2], w[2][4]; };
#define PIN(v) asm volatile("" : "+v"(v))

DI int wslot(int q) { int ci = q <= 32 ? q : 64 - q; return ci == 32 ? 32 : ((ci >> 3) * 2 + (ci & 1)) * 4 + ((ci >> 1) & 3); }

struct WRegs { u32x4 R4[4], I4[4]; u32 R1[4], I1[4], sp[4]; };
DI void wperm_load(WRegs& W, const u16* wr, const u16* wi, int tb, int l, int pass) {
  const int q = tb >> 2, P = 2 * (tb & 3) + (l >> 5);
#pragma unroll
  for (int i = 0; i < 4; ++i) {
    const u32 off = (q + 64 * ((4 * P + i + pass) & 31)) * 642 + 20 * (l & 31);
    asm volatile("global_load_dwordx4 %0, %4, %5\n\tglobal_load_dword %1, %4, %5 offset:16\n\t"
                 "global_load_dwordx4 %2, %4, %6\n\tglobal_load_dword %3, %4, %6 offset:16"
                 : "=&v"(W.R4[i]), "=&v"(W.R1[i]), "=&v"(W.I4[i]), "=&v"(W.I1[i]) : "v"(off), "s"(wr), "s"(wi) : "memory");
  }
}
#define WPIN(W) "+v"(W.R4[0]), "+v"(W.R4[1]), "+v"(W.R4[2]), "+v"(W.R4[3]), "+v"(W.I4[0]), "+v"(W.I4[1]), "+v"(W.I4[2]), "+v"(W.I4[3]), \
                "+v"(W.R1[0]), "+v"(W.R1[1]), "+v"(W.R1[2]), "+v"(W.R1[3]), "+v"(W.I1[0]), "+v"(W.I1[1]), "+v"(W.I1[2]), "+v"(W.I1[3])
DI void k1_helper(char* smem, const u16* wr, const u16* wi, const u16* wo, char* ws, int b, int l) {
  const int ci = 8 * ((b & 7) >> 1) + 2 * (b >> 6) + (b & 1), tb = (((b >> 5) & 1) ? (ci ? 64 - ci : 32) : ci) * 4 + ((b >> 3) & 3);
  WRegs W;
  {
    const char* pc; int d0, d1;
    asm volatile("s_getpc_b64 %0\n1:\n\ts_mov_b32 %1, _Z2k1PKtS0_S0_S0_S0_S0_Pc-1b\n\ts_mov_b32 %2, _Z4kendv-1b" : "=s"(pc), "=s"(d0), "=s"(d1));
    const char *c0 = pc + d0, *c1 = pc + d1;
    const char* a = c0 + ((b >> 3) * 12 + l) * 128;
    if (l < 12 && a < c1) l2touch(LDSADDR(smem) + K1_AUX + 16, a);
  }
  __builtin_amdgcn_s_barrier();
  wperm_load(W, wr, wi, tb, l, 0);
  {
    const int ks = (tb >> 2) + 64 * (8 * (tb & 3) + (l & 7)), km = (2048 - ks) & 2047, kd = ((l >> 3) & 1) * 320;
    const u32 s0 = (ks * 321 + kd) * 2, s1 = (km * 321 + kd) * 2;
    asm volatile("global_load_ushort %0, %4, %6\n\tglobal_load_ushort %1, %5, %6\n\tglobal_load_ushort %2, %4, %7\n\tglobal_load_ushort %3, %5, %7"
                 : "=&v"(W.sp[0]), "=&v"(W.sp[1]), "=&v"(W.sp[2]), "=&v"(W.sp[3]) : "v"(s0), "v"(s1), "s"(wr), "s"(wi) : "memory");
  }
  const int id = 25 * b + (l < 25 ? l : 0), ko = (id * 52429) >> 22, cb = id - 80 * ko;
  u32x4 win[8];
  {
    const u32 o = ko * 10240 + cb * 16;
    asm volatile("global_load_dwordx4 %0, %8, %9\n\tglobal_load_dwordx4 %1, %8, %9 offset:1280\n\tglobal_load_dwordx4 %2, %8, %9 offset:2560\n\t"
                 "global_load_dwordx4 %3, %8, %9 offset:3840\n\tglobal_load_dwordx4 %4, %8, %10\n\tglobal_load_dwordx4 %5, %8, %10 offset:1280\n\t"
                 "global_load_dwordx4 %6, %8, %10 offset:2560\n\tglobal_load_dwordx4 %7, %8, %10 offset:3840"
                 : "=&v"(win[0]), "=&v"(win[1]), "=&v"(win[2]), "=&v"(win[3]), "=&v"(win[4]), "=&v"(win[5]), "=&v"(win[6]), "=&v"(win[7])
                 : "v"(o), "s"(wo), "s"((const char*)wo + 5120) : "memory");
  }
  const int q = tb >> 2, t = l & 31, P = 2 * (tb & 3) + (l >> 5), qm = (64 - q) & 63;
  char* wp = ws + OFF_WP;
  const int P4 = tb & 3;
  const int d14 = (wslot(q) & 31) * 16384 + (q >= 32) * 8192, m14 = (wslot(qm) & 31) * 16384 + (qm >= 32) * 8192;
  const int d05 = 4 * 524288 + wslot(q) * 16384, m05 = 4 * 524288 + wslot(qm) * 16384;
  u32 pd = (P >> 2) * 4096 + (P & 3) * 256 + (t >> 4) * 1024 + (t & 15) * 16;
  const u32 t0 = (64 - t) & 63;
  u32 pm0 = ((7 - P) >> 2) * 4096 + ((7 - P) & 3) * 256 + (t0 >> 4) * 1024 + (t0 & 15) * 16;
  const int p = 8 * (tb & 3) + (l & 7);
  u32 po = (p >> 4) * 4096 + ((p >> 2) & 3) * 256 + ((l >> 3) & 1) * 2048 + (p & 3) * 4;
  const int a = cb >> 3, hp = (ko * 205) >> 10, wq = ko - 5 * hp;
  u32 wd = ((((cb & 7) * 5 + (a >> 1)) * 20 + (hp >> 2) * 5 + wq) * 64 + (hp & 3) * 16 + (a & 1) * 8) * 16;
  PIN(pd); PIN(pm0); PIN(po); PIN(wd);
  __builtin_amdgcn_s_barrier(); __builtin_amdgcn_s_barrier(); __builtin_amdgcn_s_barrier();
  asm volatile("s_waitcnt vmcnt(0)" : WPIN(W), "+v"(W.sp[0]), "+v"(W.sp[1]), "+v"(W.sp[2]), "+v"(W.sp[3]),
               "+v"(win[0]), "+v"(win[1]), "+v"(win[2]), "+v"(win[3]), "+v"(win[4]), "+v"(win[5]), "+v"(win[6]), "+v"(win[7]) :: "memory");
  auto wr1 = [&](int r, bool dir, bool mir) {
    u32x4 v;
#pragma unroll
    for (int i = 0; i < 4; ++i)
      v[i] = __builtin_amdgcn_perm(r < 8 ? W.I4[i][(r >> 1) & 3] : W.I1[i], r < 8 ? W.R4[i][(r >> 1) & 3] : W.R1[i], (r & 1) ? 0x07060302u : 0x05040100u);
    const u32x4 vm = u32x4{v[3], v[2], v[1], v[0]} ^ 0x80000000u;
    if (r >= 1 && r <= 4) { if (dir) st16s(wp, (r - 1) * 524288 + d14 + pd, v); }
    else if (r >= 6) { if (mir) st16s(wp, (9 - r) * 524288 + m14 + 8176 - pd, vm); }
    else if (r == 5) {
      if (dir && q <= 32) st16s(wp, d05 + 8192 + pd, v);
      if (mir && qm <= 32) st16s(wp, m05 + 8192 + 8176 - pd, vm);
    } else if (t != 0) {
      if (dir && q <= 32) st16s(wp, d05 + pd, v);
      if (mir && qm <= 32) st16s(wp, m05 + pm0, vm);
    }
  };
#pragma unroll
  for (int k = 0; k < 6; ++k) {
    if (k < 3) { wr1(3 * k, true, q != 0); wr1(3 * k + 1, true, q != 0); wr1(3 * k + 2, true, q != 0); }
    if (k == 3) {
      wr1(9, true, q != 0);
      if (q <= 32 && l < 16)
        ST4SO(wp, d05 + po, 0, pk2(0.5f * (lo2f(W.sp[0]) + lo2f(W.sp[1])), 0.5f * (lo2f(W.sp[2]) - lo2f(W.sp[3]))));
    }
    if (k >= 4 && l < 25) {
#pragma unroll
      for (int jj = 4 * (k - 4); jj < 4 * (k - 3); ++jj) {
        u32x4 o2;
#pragma unroll
        for (int d = 0; d < 4; ++d) o2[d] = __builtin_amdgcn_perm(win[2 * d + 1][jj >> 1], win[2 * d][jj >> 1], (jj & 1) ? 0x07060302u : 0x05040100u);
        *(u32x4*)(ws + OFF_WOF + (size_t)(wd + jj * 16)) = o2;
      }
    }
    if (k < 5) __builtin_amdgcn_s_barrier();
  }
  if (q != 0) {
    WAITV(0);
    const int rg = l >> 3, ln = l & 7;
    const u32 po2 = (P4 >> 1) * 4096 + (P4 & 1) * 512 + (ln >> 2) * 1024 + (ln & 3) * 128, pm2 = 8064 - po2;
    l2touch(LDSADDR(smem) + K1_AUX + 16, wp + (size_t)(rg < 4 ? rg * 524288 + d14 + po2 : (rg - 4) * 524288 + m14 + pm2));
    if (l < 16) l2touch(LDSADDR(smem) + K1_AUX + 16, wp + (size_t)((q <= 32 ? d05 + po2 : m05 + pm2) + rg * 8192));
  }
  if (q == 0) {
    __builtin_amdgcn_s_barrier();
    wperm_load(W, wr, wi, tb, l, 1);
    asm volatile("s_waitcnt vmcnt(0)" : WPIN(W) :: "memory");
#pragma unroll
    for (int r = 0; r < 10; ++r) wr1(r, false, true);
  }
}

template <bool QT>
DI void k1_gemm(char* smem, const u16* x, const u16* gate, char* ws, int m, int nt, int w, int l
                ) {
  const int g = l >> 4, l15 = l & 15, rg = w & 3, chh = w >> 2;
  {
    u32 gxv[5], gwv[5];
    const int r0 = 8 * nt + w;
#pragma unroll
    for (int i = 0; i < 5; ++i) {
      gxv[i] = ((const u32*)(x + (32 * (r0 & 63) + 2 * m + (r0 >> 6)) * 640))[l + 64 * i];
      gwv[i] = ((const u32*)gate)[l + 64 * i];
    }
    float g0 = 0.f, g1 = 0.f;
#pragma unroll
    for (int i = 0; i < 5; ++i) {
      u32 pr = pk2(lo2f(gxv[i]) * lo2f(gwv[i]), hi2f(gxv[i]) * hi2f(gwv[i]));
      g0 += lo2f(pr); g1 += hi2f(pr);
    }
    g0 += g1;
    g0 += dppf<0xB1>(g0); g0 += dppf<0x4E>(g0); g0 += dppf<0x124>(g0); g0 += dppf<0x128>(g0);
    float tot = __uint_as_float(__builtin_amdgcn_readlane(__float_as_uint(g0), 0)) + __uint_as_float(__builtin_amdgcn_readlane(__float_as_uint(g0), 16))
              + __uint_as_float(__builtin_amdgcn_readlane(__float_as_uint(g0), 32)) + __uint_as_float(__builtin_amdgcn_readlane(__float_as_uint(g0), 48));
    float gt = bfr(__builtin_amdgcn_rcpf(1.f + __builtin_amdgcn_exp2f(bfr(tot) * -1.44269504088896f)));
    if (l == 0) ((float*)(ws + OFF_G))[128 * m + r0] = gt;
  }
  u32 xa[2], wa[4][2];
  xa[0] = rg * 4096 + l15 * 128 + ((g ^ ((l15 >> 1) & 7)) << 4); xa[1] = xa[0] ^ 64;
#pragma unroll
  for (int t2 = 0; t2 < 2; ++t2) {
    const int q4 = l15 >> 2, swz = (q4 << 2) | ((2 * g + t2) & 3);
    const u32 b0 = K1_B + (8 * g + 4 * t2 + q4) * 256 + (((((l >> 1) & 1) | ((QT ? 8 : 4) * chh)) ^ swz) << 4) + 8 * (l & 1);
#pragma unroll
    for (int i = 0; i < 4; ++i) { wa[i][t2] = b0 ^ ((QT ? i : (i & 1) + 4 * (i >> 1)) << 5); PIN(wa[i][t2]); }
  }
  f32x4 acc[2][4];
#pragma unroll
  for (int rt = 0; rt < 2; ++rt)
#pragma unroll
    for (int i = 0; i < 4; ++i) acc[rt][i] = f32x4{0.f, 0.f, 0.f, 0.f};
  Frags F[2];
  auto rd = [&](Frags& f, int C) {
    const int so = (C & 3) * K1_SLOT;
#pragma unroll
    for (int ks = 0; ks < 2; ++ks) {
#pragma unroll
      for (int rt = 0; rt < 2; ++rt) f.x[ks][rt] = ldsr128(smem, xa[ks] + so + rt * 2048);
#pragma unroll
      for (int i = 0; i < 4; ++i) {
        u32x2 a = ldstr(smem, wa[i][0] + so + ks * 8192), b = ldstr(smem, wa[i][1] + so + ks * 8192);
        f.w[ks][i] = u32x4{a[0], a[1], b[0], b[1]};
      }
    }
  };
  auto mm = [&](const Frags& f) {
#pragma unroll
    for (int ks = 0; ks < 2; ++ks)
#pragma unroll
      for (int i = 0; i < 4; ++i)
#pragma unroll
        for (int rt = 0; rt < 2; ++rt)
          acc[rt][i] = QT ? mfma(f.w[ks][i], f.x[ks][rt], acc[rt][i]) : mfma(f.x[ks][rt], f.w[ks][i], acc[rt][i]);
  };
  STAMP();
  RAWBAR();
  STAMP();
  rd(F[0], 0);
  SB();
  u32 eo[2], ra[2], rb[2];
  if (QT) {
    const int ck = 16 * (nt - 11) + 8 * chh + (l & 7), hp = (ck * 205) >> 10;
    eo[0] = (hp * 2048 + 128 * m + 32 * rg + (l >> 3)) * 80 + (ck - 5 * hp) * 16;
    ra[0] = 2 * K1_SLOT + chh * K1_B + rg * 4608 + l15 * 144 + g * 8;
    rb[0] = 2 * K1_SLOT + chh * K1_B + rg * 4608 + (l >> 3) * 144 + (l & 7) * 16;
    PIN(eo[0]); PIN(ra[0]); PIN(rb[0]);
  } else {
#pragma unroll
    for (int k = 0; k < 2; ++k) {
      int P = w + 8 * k;
      int pi = (0x103322211100ull >> (4 * P)) & 15, pj = (0x333232121010ull >> (4 * P)) & 15;
      int d = 16 * pi + 4 * g, e = 16 * pj + l15, hd = (d * 205) >> 12, he = (e * 205) >> 12;
      int h = 3 * nt + hd;
      bool ok = P < 12 && d < 60 && h < 32 && (e == 60 || (e < 60 && he == hd));
      int ee = e == 60 ? 20 : e - 20 * he;
      eo[k] = ok ? (((m * 32 + h) * 21 + ee) * 20 + (d - 20 * hd)) * 4 : ~0u;
      ra[k] = 2 * K1_SLOT + pi * 1024 + l * 16; rb[k] = 2 * K1_SLOT + (4 + pj) * 1024 + l * 16;
      PIN(eo[k]); PIN(ra[k]); PIN(rb[k]);
    }
  }
  SB();
#pragma unroll
  for (int C = 0; C < 10; ++C) {
    Frags& cur = F[C & 1]; Frags& nxt = F[(C + 1) & 1];
    if (C == 8) { mm(cur); SB(); }
    if (C < 9) RAWBAR();
    SB();
    if (C < 9) rd(nxt, C + 1);
    if (C != 8) mm(cur);
    if (C < 8) {
#pragma unroll
      for (int i = 0; i < 4; ++i) {
        __builtin_amdgcn_sched_group_barrier(0x008, 1, 0); __builtin_amdgcn_sched_group_barrier(0x100, 2, 0);
#pragma unroll
        for (int k = 0; k < 3; ++k) { __builtin_amdgcn_sched_group_barrier(0x008, 1, 0); __builtin_amdgcn_sched_group_barrier(0x100, 1, 0); }
      }
    }
    SB();
  }
  STAMP();
  if (QT) {
    u32x4 pa[4];
#pragma unroll
    for (int i = 0; i < 4; ++i) pa[i] = pkfrag(acc[0][i], acc[1][i]);
    SB();
    phi4(pa);
#pragma unroll
    for (int i = 0; i < 4; ++i) { ldsw64(smem, ra[0] + i * 32, u32x2{pa[i][0], pa[i][1]}); ldsw64(smem, ra[0] + 2304 + i * 32, u32x2{pa[i][2], pa[i][3]}); }
    u32x4 o[4];
#pragma unroll
    for (int i = 0; i < 4; ++i) o[i] = ldsr128(smem, rb[0] + i * 1152);
    ST16SO(ws + OFF_PQ, eo[0], 0, o[0]); ST16SO(ws + OFF_PQ, eo[0], 640, o[1]); ST16SO(ws + OFF_PQ, eo[0], 1280, o[2]); ST16SO(ws + OFF_PQ, eo[0], 1920, o[3]);
  } else {
    u32x4 v1 = pkfrag(acc[0][3], acc[1][3]);
    if (chh && l15 == 12) v1 = u32x4{0x3f803f80u, 0x3f803f80u, 0x3f803f80u, 0x3f803f80u};
    const u32 fo = 2 * K1_SLOT + (rg * 8 + 2 * chh) * 1024 + l * 16;
    ldsw128(smem, fo + 4096, pkfrag(acc[0][2], acc[1][2]));
    ldsw128(smem, fo + 5120, v1);
    u32x4 pa[2] = {pkfrag(acc[0][0], acc[1][0]), pkfrag(acc[0][1], acc[1][1])};
    phi4(pa);
    ldsw128(smem, fo, pa[0]);
    ldsw128(smem, fo + 1024, pa[1]);
    STAMP();
    RAWBAR();
    STAMP();
    u32x4 fa[4], fb[4];
    f32x4 r0 = {0.f, 0.f, 0.f, 0.f};
#pragma unroll
    for (int r = 0; r < 4; ++r) { fa[r] = ldsr128(smem, ra[0] + r * 8192); fb[r] = ldsr128(smem, rb[0] + r * 8192); }
    if (w < 4) {
      u32x4 fc[4], fd[4];
      f32x4 r1 = r0;
#pragma unroll
      for (int r = 0; r < 4; ++r) { fc[r] = ldsr128(smem, ra[1] + r * 8192); fd[r] = ldsr128(smem, rb[1] + r * 8192); }
      SB();
#pragma unroll
      for (int r = 0; r < 4; ++r) { r0 = mfma(fa[r], fb[r], r0); r1 = mfma(fc[r], fd[r], r1); }
      if (eo[1] != ~0u) st16p(ws + OFF_KVP, eo[1], r1);
    } else {
      SB();
#pragma unroll
      for (int r = 0; r < 4; ++r) r0 = mfma(fa[r], fb[r], r0);
    }
    if (eo[0] != ~0u) st16p(ws + OFF_KVP, eo[0], r0);
  }
}

template <int NB, bool B4>
DI void k1_dft(const char* vb, const u32x4 (&gs)[8], const u32x4 (&xq)[4][3], u32 vo, const u32 (&kv01)[2], const u32 (&kv2)[2],
               const int (&co)[2], float tc, float ts, int l) {
  const int g = l >> 4, h = (l >> 3) & 1, j = l & 7;
  const u32 hm = (j & 1) ? 0xffff0000u : 0xffffu;
  const f32x4 z4 = {0.f, 0.f, 0.f, 0.f};
  u32x4 kd[NB][3], tb[NB][2];
  f32x4 t[NB][4], y[NB][4];
#pragma unroll
  for (int u = 0; u < NB; ++u) {
    kd[u][0] = onehot(((j & 1) ? kv01[u] << 16 : kv01[u]) & hm, j >> 1); kd[u][1] = onehot(((j & 1) ? kv01[u] : kv01[u] >> 16) & hm, j >> 1);
    kd[u][2] = onehot(kv2[u] & hm, j >> 1);
  }
  SB();
#pragma unroll
  for (int s = 0; s < 3; ++s)
#pragma unroll
    for (int u = 0; u < NB; ++u)
#pragma unroll
      for (int rt = 0; rt < 4; ++rt) t[u][rt] = mfma(xq[rt][s], kd[u][s], s ? t[u][rt] : z4);
#pragma unroll
  for (int u = 0; u < NB; ++u) { tb[u][0] = pkfrag(t[u][0], t[u][1]); tb[u][1] = pkfrag(t[u][2], t[u][3]); }
#pragma unroll
  for (int k = 0; k < 2; ++k)
#pragma unroll
    for (int u = 0; u < NB; ++u)
#pragma unroll
      for (int mt = 0; mt < 4; ++mt) y[u][mt] = mfma(gs[2 * mt + k], tb[u][k], k ? y[u][mt] : z4);
  SB();
#pragma unroll
  for (int u = 0; u < NB; ++u) {
    float X[8], Y[8]; u32 pk[8];
    const u32 vu = vo + co[u];
    if (!(B4 && u == 1)) {
#pragma unroll
      for (int i = 0; i < 8; ++i) { X[i] = y[u][i >> 1][2 * (i & 1)] - ror8(y[u][i >> 1][2 * (i & 1) + 1]); Y[i] = y[u][i >> 1][2 * (i & 1) + 1] + ror8(y[u][i >> 1][2 * (i & 1)]); }
      float p0 = ror8(y[u][0][0]), p1 = ror8(y[u][0][1]);
      X[0] = g == 0 ? (h ? y[u][0][1] : y[u][0][0]) : X[0]; Y[0] = g == 0 ? (h ? p1 : p0) : Y[0];
      SB();
#pragma unroll
      for (int i = 0; i < 8; ++i) pk[i] = pk2(X[i], Y[i]);
      SB();
#pragma unroll
      for (int i = 0; i < 8; ++i) pk[i] = __builtin_amdgcn_alignbit(pk[i], pk[i], 16 * h);
    } else {
      const float o1 = y[u][0][1];
      if (g == 0) ST4SO(vb, vu, 2048, pk2(o1 * tc, o1 * ts));
      y[u][0][1] = g == 0 ? 0.f : o1;
#pragma unroll
      for (int i = 0; i < 8; ++i) {
        float o0 = y[u][i >> 1][2 * (i & 1)], o2 = y[u][i >> 1][2 * (i & 1) + 1];
        X[i] = o0 * tc - o2 * ts; Y[i] = o0 * ts + o2 * tc;
      }
      SB();
#pragma unroll
      for (int i = 0; i < 8; ++i) pk[i] = pk2(X[i], Y[i]);
    }
    SB();
#pragma unroll
    for (int i = 0; i < 8; ++i) ST4SO(vb, vu, i * 256, pk[i]);
  }
}

__global__ __launch_bounds__(832) __attribute__((amdgpu_waves_per_eu(4, 4)))
void k1(const u16* __restrict__ x, const u16* __restrict__ wqkv, const u16* __restrict__ wr, const u16* __restrict__ wi,
        const u16* __restrict__ gate, const u16* __restrict__ wo, char* __restrict__ ws) {
  extern __shared__ __attribute__((aligned(16))) char smem[];
  const int tid = threadIdx.x, l = tid & 63, w = __builtin_amdgcn_readfirstlane(tid >> 6);
  const int b = blockIdx.x, xcd = b & 7, bi = b >> 3;
  const int m = (xcd >> 1) * 4 + (bi & 3), nt = (xcd & 1) * 8 + (bi >> 2);
  asm volatile("" :: "s"(x), "s"(wqkv), "s"(wr), "s"(wi), "s"(gate), "s"(wo), "s"(ws));
  STAMP_DECL; STAMP();
#define TSARG
  if (w == 12) k1_helper(smem, wr, wi, wo, ws, b, l);
  else if (w >= 8) {
    const int v = w - 8;
    int cn = ((nt & 7) * 5 >> 2) + ((m & 3) * 5 >> 1); if (cn >= 10) cn -= 10;
    const int cq = nt >= 11, cbase = cq ? 128 * (nt - 11) : 640 + 60 * nt, cs = cq ? 64 : 640, cmax = nt == 10 ? 4 : 7;
    u32 da[4], db[4];
#pragma unroll
    for (int e = 0; e < 4; ++e) {
      da[e] = (8 * e + (l >> 3)) * 40960 + (((l & 7) ^ ((4 * e + (l >> 4)) & 7)) << 4) - 1024 * e;
      int cbk = (l & 15) ^ (((l >> 4) << 2) | e);
      db[e] = (4 * e + (l >> 4)) * 3840 + ((cbk >> 3) * cs + 8 * min(cbk & 7, cmax)) * 2 - 1024 * e;
    }
    const char* xa = (const char*)x + ((v & 1) * 1024 + 2 * m + (v >> 1)) * 1280;
    const char* wa = (const char*)wqkv + v * (16 * 3840) + cbase * 2;
    const u32 dl = LDSADDR(smem) + v * 4096;
    int cc = cn;
    auto issue = [&](int slot) {
      dma4(dl + slot * K1_SLOT, xa + cn * 128, da[0], da[1], da[2], da[3]);
      dma4(dl + slot * K1_SLOT + K1_B, wa + cn * (64 * 3840), db[0], db[1], db[2], db[3]);
      cn = cn == 9 ? 0 : cn + 1;
    };
    __builtin_amdgcn_s_setprio(2);
    issue(0); issue(1);
    STAMP();
    const int n2s = nt >> 3, bg = nt & 7, g = l >> 4;
    const u32 ca = n2s * 8192 + (l & 15) * 128 + ((bg ^ ((l >> 1) & 7)) << 4);
    u32x4 xq[4][3];
#pragma unroll
    for (int rt = 0; rt < 4; ++rt) xq[rt][0] = xq[rt][1] = xq[rt][2] = u32x4{0u, 0u, 0u, 0u};
    auto step = [&](int C) {
      RAWBAR();
      if (C < 7) issue((C + 3) & 3);
      {
        const u32 o = LDSADDR(smem) + ca + ((C + 1) & 3) * K1_SLOT;
        const unsigned long long mk = 0xffffull << (16 * (cc & 3));
        asm volatile("s_mov_b64 exec, %13\n\ts_cmp_lt_u32 %14, 4\n\ts_cbranch_scc0 1f\n\t"
                     "ds_read_b128 %0, %12\n\tds_read_b128 %1, %12 offset:2048\n\tds_read_b128 %2, %12 offset:4096\n\tds_read_b128 %3, %12 offset:6144\n\ts_branch 3f\n"
                     "1:\n\ts_cmp_lt_u32 %14, 8\n\ts_cbranch_scc0 2f\n\t"
                     "ds_read_b128 %4, %12\n\tds_read_b128 %5, %12 offset:2048\n\tds_read_b128 %6, %12 offset:4096\n\tds_read_b128 %7, %12 offset:6144\n\ts_branch 3f\n"
                     "2:\n\tds_read_b128 %8, %12\n\tds_read_b128 %9, %12 offset:2048\n\tds_read_b128 %10, %12 offset:4096\n\tds_read_b128 %11, %12 offset:6144\n"
                     "3:\n\ts_mov_b64 exec, -1"
                     : "+v"(xq[0][0]), "+v"(xq[1][0]), "+v"(xq[2][0]), "+v"(xq[3][0]), "+v"(xq[0][1]), "+v"(xq[1][1]), "+v"(xq[2][1]), "+v"(xq[3][1]),
                       "+v"(xq[0][2]), "+v"(xq[1][2]), "+v"(xq[2][2]), "+v"(xq[3][2]) : "v"(o), "s"(mk), "s"(cc) : "memory", "scc");
      }
      cc = cc == 9 ? 0 : cc + 1;
    };
#pragma unroll 1
    for (int C = -1; C < 2; ++C) { WAITV(8); step(C); }
    u32x4 gs[8];
    {
      const char* gp = (const char*)dGsA.v + l * 16 + 4096;
      GLD(gs[0], gp, -4096); GLD(gs[1], gp, -3072); GLD(gs[2], gp, -2048); GLD(gs[3], gp, -1024);
      GLD(gs[4], gp, 0); GLD(gs[5], gp, 1024); GLD(gs[6], gp, 2048); GLD(gs[7], gp, 3072);
    }
    const int h = (l >> 3) & 1, bb = 8 * bg + (l & 7);
    u32 kv01[2], kv2[2];
    {
      float c3[3];
#pragma unroll
      for (int s = 0; s < 3; ++s) c3[s] = __builtin_amdgcn_cosf((float)((v + 1) * (64 * (4 * s + g) + bb)) * (1.f / 640.f) - 0.25f * h);
      kv01[0] = pk2(c3[0], c3[1]); kv2[0] = pk2(c3[2], c3[2]);
      kv01[1] = kv2[1] = (h & g) ? 0xbf80bf80u : 0x3f803f80u;
    }
    float tc = h ? __builtin_amdgcn_cosf(bb * (1.f / 128.f)) : 1.f, ts = h ? __builtin_amdgcn_sinf(bb * (1.f / 128.f)) : 0.f;
    u32 vo = ((2 * m + n2s) * 8 + bg) * VB_TS + l * 4;
    PIN(kv01[0]); PIN(kv01[1]); PIN(kv2[0]); PIN(kv2[1]); PIN(tc); PIN(ts); PIN(vo);
    WAITV(16); step(2);
#pragma unroll 1
    for (int C = 3; C < 7; ++C) { WAITV(8); step(C); }
    STAMP();
    WAITV(8); step(7);
    WAITV(0); step(8);
    asm volatile("s_waitcnt lgkmcnt(0)" : "+v"(xq[0][0]), "+v"(xq[1][0]), "+v"(xq[2][0]), "+v"(xq[3][0]), "+v"(xq[0][1]), "+v"(xq[1][1]), "+v"(xq[2][1]), "+v"(xq[3][1]),
                 "+v"(xq[0][2]), "+v"(xq[1][2]), "+v"(xq[2][2]), "+v"(xq[3][2]),
                 "+v"(gs[0]), "+v"(gs[1]), "+v"(gs[2]), "+v"(gs[3]), "+v"(gs[4]), "+v"(gs[5]), "+v"(gs[6]), "+v"(gs[7]) :: "memory");
    STAMP();
    if (v == 0) { const int co[2] = {0, 8192}; k1_dft<2, true>(ws + OFF_VB, gs, xq, vo, kv01, kv2, co, tc, ts, l); }
    else {
      if (l < 4 && v == 1) l2touch(LDSADDR(smem) + K1_AUX + 16, (const char*)dF64.v + (bi * 4 + l) * 128);
      const int co[2] = {v * 2048, 0}; k1_dft<1, false>(ws + OFF_VB, gs, xq, vo, kv01, kv2, co, tc, ts, l);
    }
  } else if (nt >= 11) k1_gemm<true>(smem, x, gate, ws, m, nt, w, l TSARG);
  else k1_gemm<false>(smem, x, gate, ws, m, nt, w, l TSARG);
  STAMP();
  STAMP_FLUSH(0);
}

constexpr int K2_V = 0, K2_A = 16384, K2_U = 32768, K2_US = 272, K2_PF = K2_U + 2 * 32 * K2_US, K2_LDS = K2_PF + 256;
constexpr int K2_NF = 168;
constexpr int K2_NO = 64;

__global__ __launch_bounds__(512) __attribute__((amdgpu_waves_per_eu(2, 2)))
void k2(char* __restrict__ ws) {
  extern __shared__ __attribute__((aligned(16))) char smem[];
  const int tid = threadIdx.x, l = tid & 63, w8 = __builtin_amdgcn_readfirstlane(tid >> 6);
  const int g = l >> 4, l15 = l & 15;
  const int b = blockIdx.x;
  const f32x4 z4 = {0.f, 0.f, 0.f, 0.f};
  auto prefetch = [&]() {
    const int i = (b >> 3) * 224 + tid;
    if (tid < 224 && i < 6400) l2touch(LDSADDR(smem) + K2_PF, ws + OFF_WOF + (size_t)i * 128);
    if (tid >= 448 && tid < 451 && (b >> 3) * 3 + tid - 448 < 64) l2touch(LDSADDR(smem) + K2_PF, (const char*)dGsB.v + ((b >> 3) * 3 + tid - 448) * 128);
  };
  STAMP_DECL; STAMP();
  if (b < K2_NF) {
    const int sidx = (b >> 5) * 32 + (b & 7) * 4 + ((b >> 3) & 3);
    if (sidx > 160) { prefetch(); return; }
    const int half = w8 >> 2, w4 = w8 & 3;
    {
      int kc0 = l15 ^ ((8 * w4 + g) & 15), kc1 = kc0 ^ 4;
      u32 v0 = ((8 * w4 + g) * 8 + (kc0 >> 1)) * VB_TS + (kc0 & 1) * 16, v1 = ((8 * w4 + 4 + g) * 8 + (kc1 >> 1)) * VB_TS + (kc1 & 1) * 16 - 1024;
      dma2(LDSADDR(smem) + K2_V + w8 * 2048, ws + OFF_VB + sidx * 64 + half * 32, v0, v1);
    }
    u32x4 tr[4], ti[4];
    { const char* fp = (const char*)dF64.v + w4 * 4096 + l * 16; GLD(tr[0], fp, 0); GLD(tr[1], fp, 1024); GLD(tr[2], fp, 2048); GLD(tr[3], fp, 3072); }
    u32x4 wf[2];
    { const char* wp = ws + OFF_WP + (size_t)sidx * 16384 + half * 8192 + w4 * 1024 + l * 16 + 2048; GLD(wf[0], wp, -2048); GLD(wf[1], wp, 2048); }
    const int c = sidx >> 5 > 4 ? 4 : sidx >> 5;
    const int ci = sidx == 160 ? 32 : ((sidx >> 3) & 3) * 8 + (sidx & 3) * 2 + ((sidx >> 2) & 1);
    const int q = c == 4 ? ci : (half == 0 ? ci : (ci == 0 ? 32 : 64 - ci));
    {
      const int ri = 16 * (w4 & 1) + l15;
      const int B = w4 < 2 ? q + 64 * ri : 64 * ri, A = (w4 < 2 ? 0 : ri * q) + B * 4 * g;
      float cs[8], sn[8];
#pragma unroll
      for (int j = 0; j < 8; ++j) {
        float rev = (float)((A + B * (j < 4 ? j : 12 + j)) & 2047) * (1.f / 2048.f);
        cs[j] = __builtin_amdgcn_cosf(rev); sn[j] = __builtin_amdgcn_sinf(rev);
      }
      ldsw128(smem, K2_A + w8 * 2048 + l * 16, u32x4{pk2(cs[0], cs[1]), pk2(cs[2], cs[3]), pk2(cs[4], cs[5]), pk2(cs[6], cs[7])});
      ldsw128(smem, K2_A + w8 * 2048 + 1024 + l * 16, u32x4{pk2(sn[0], sn[1]), pk2(sn[2], sn[3]), pk2(sn[4], sn[5]), pk2(sn[6], sn[7])});
    }
    const float sc = 2.f / (2048.f * 640.f), s2 = (c == 4 && (ci & 31) == 0) ? 0.5f * sc : sc;
    f32x4 tc = z4, ts = z4;
    if (c == 4) {
#pragma unroll
      for (int i = 0; i < 4; ++i) {
        float rev = (float)(16 * w4 + 4 * g + i) * (1.f / 128.f);
        tc[i] = __builtin_amdgcn_cosf(rev) * s2; ts[i] = __builtin_amdgcn_sinf(rev) * s2;
      }
    }
    const int so = sidx == 160 ? 128 : sidx;
    char* dst = ws + OFF_AP + (size_t)((16 * half + l15) * 8 + 2 * w4 + (g >> 1)) * AP_TS + so * 64 + (g & 1) * 32;
    u32 uw = K2_U + half * (32 * K2_US) + 4 * g * K2_US + (16 * w4 + l15) * 4, ur0 = K2_U + (16 * half + l15) * K2_US + g * 16;
    u32 va = K2_V + half * 8192 + l15 * 256 + ((g ^ (l15 & 3)) << 4) + ((l15 >> 2) << 6), aa = K2_A + half * 8192 + l * 16;
    asm volatile("" : "+v"(tc), "+v"(ts), "+v"(dst), "+v"(uw), "+v"(ur0), "+v"(va), "+v"(aa));
    asm volatile("s_waitcnt vmcnt(2)" : "+v"(tr[0]), "+v"(tr[1]), "+v"(tr[2]), "+v"(tr[3]) :: "memory");
#pragma unroll
    for (int ks = 0; ks < 4; ++ks)
#pragma unroll
      for (int i = 0; i < 4; ++i) ti[ks][i] = __builtin_amdgcn_alignbit(tr[ks][i], tr[ks][i], 16) ^ 0x8000u;
    asm volatile("" : "+v"(ti[0]), "+v"(ti[1]), "+v"(ti[2]), "+v"(ti[3]));
    STAMP();
    RAWBAR();
    STAMP();
    u32x4 vf[2][4], cf[4], sf[4];
#pragma unroll
    for (int ks = 0; ks < 4; ++ks)
#pragma unroll
      for (int mt = 0; mt < 2; ++mt) vf[mt][ks] = ldsr128(smem, (va ^ (ks << 6)) + mt * 4096);
#pragma unroll
    for (int i = 0; i < 4; ++i) {
      cf[i] = ldsr128(smem, aa + i * 2048); sf[i] = ldsr128(smem, aa + i * 2048 + 1024);
    }
    SB();
    f32x4 Tr[2] = {z4, z4}, Ti[2] = {z4, z4};
#pragma unroll
    for (int ks = 0; ks < 4; ++ks)
#pragma unroll
      for (int mt = 0; mt < 2; ++mt) { Tr[mt] = mfma(vf[mt][ks], tr[ks], Tr[mt]); Ti[mt] = mfma(vf[mt][ks], ti[ks], Ti[mt]); }
    SB();
    asm volatile("s_waitcnt vmcnt(0)" : "+v"(wf[0]), "+v"(wf[1]) :: "memory");
    f32x4 wa[2], wb[2];
#pragma unroll
    for (int mt = 0; mt < 2; ++mt)
#pragma unroll
      for (int i = 0; i < 4; ++i) { wa[mt][i] = lo2f(wf[mt][i]); wb[mt][i] = hi2f(wf[mt][i]); }
    asm volatile("" : "+v"(wa[0]), "+v"(wa[1]), "+v"(wb[0]), "+v"(wb[1]));
    SB();
    u32x4 br = pkfrag(Tr[0], Tr[1]), bi = pkfrag(Ti[0], Ti[1]), ni = bi ^ 0x80008000u;
    f32x4 zr[2], zn[2];
#pragma unroll
    for (int mt = 0; mt < 2; ++mt) { zr[mt] = mfma(cf[mt], br, z4); zn[mt] = mfma(cf[mt], bi, z4); }
#pragma unroll
    for (int mt = 0; mt < 2; ++mt) { zr[mt] = mfma(sf[mt], ni, zr[mt]); zn[mt] = mfma(sf[mt], br, zn[mt]); }
    SB();
    prefetch();
    f32x4 Yr[2], Yi[2];
#pragma unroll
    for (int mt = 0; mt < 2; ++mt) { Yr[mt] = zr[mt] * wa[mt]; Yi[mt] = zr[mt] * wb[mt]; }
    SB();
#pragma unroll
    for (int mt = 0; mt < 2; ++mt) { Yr[mt] += zn[mt] * wb[mt]; Yi[mt] -= zn[mt] * wa[mt]; }
    SB();
    br = pkfrag(Yr[0], Yr[1]); bi = pkfrag(Yi[0], Yi[1]); ni = bi ^ 0x80008000u;
    f32x4 ur[2], ui[2];
#pragma unroll
    for (int mt = 0; mt < 2; ++mt) { ur[mt] = mfma(cf[2 + mt], br, z4); ui[mt] = mfma(cf[2 + mt], bi, z4); }
#pragma unroll
    for (int mt = 0; mt < 2; ++mt) { ur[mt] = mfma(sf[2 + mt], ni, ur[mt]); ui[mt] = mfma(sf[2 + mt], br, ui[mt]); }
    SB();
#pragma unroll
    for (int mt = 0; mt < 2; ++mt)
#pragma unroll
      for (int i = 0; i < 4; ++i) *(LDS_AS u32*)((LDS_AS char*)smem + uw + (16 * mt + i) * K2_US) = pk2(ur[mt][i], ui[mt][i]);
    STAMP();
    RAWBAR();
    STAMP();
    u32x4 uf[2][4];
#pragma unroll
    for (int ks = 0; ks < 4; ++ks)
#pragma unroll
      for (int cl = 0; cl < 2; ++cl) uf[cl][ks] = ldsr128(smem, ur0 + cl * (32 * K2_US) + ks * 64);
    SB();
    f32x4 Rr[2] = {z4, z4}, Ri[2] = {z4, z4};
#pragma unroll
    for (int ks = 0; ks < 4; ++ks)
#pragma unroll
      for (int cl = 0; cl < 2; ++cl) { Rr[cl] = mfma(tr[ks], uf[cl][ks], Rr[cl]); Ri[cl] = mfma(ti[ks], uf[cl][ks], Ri[cl]); }
    f32x4 e00, e01, e10, e11;
    if (c < 4) {
      f32x4 ar = Rr[0] * sc, ai = Ri[0] * -sc;
      if (ci > 0) { e00 = ar + Rr[1] * sc; e01 = ai - Ri[1] * sc; e10 = ai + Ri[1] * sc; e11 = Rr[1] * sc - ar; }
      else { e00 = ar; e01 = ai; e10 = Rr[1] * sc; e11 = Ri[1] * -sc; }
    } else {
      e00 = Rr[0] * s2; e01 = Rr[1] * tc - Ri[1] * ts;
      if (ci & 31) { e10 = Ri[0] * -s2; e11 = -(Rr[1] * ts + Ri[1] * tc); }
      else { e10 = e00; e11 = e01; }
    }
    if (sidx != 160) *(u32x4*)dst = pkfrag(e00, e01);
    if (sidx != 128) *(u32x4*)(dst + 16) = pkfrag(e10, e11);
    STAMP();
  } else {
    const int ob = b - K2_NF, hp = ob & 15, n2 = 8 * (ob >> 4) + w8;
    u32x4 s[8];
    const int rh = tid >= 256, rp = tid & 255;
    if (rp < 210) {
      const char* kb = ws + OFF_KVP + hp * 3360;
      const u32 ko = rp * 16 + rh * (8 * 53760);
#pragma unroll
      for (int mm = 0; mm < 8; ++mm) asm volatile("global_load_dwordx4 %0, %1, %2" : "=v"(s[mm]) : "v"(ko), "s"(kb + mm * 53760) : "memory");
    }
    const char* src = ws + OFF_PQ + ((size_t)hp * 2048 + 64 * n2) * 80;
    const u32 pl = 8192 + w8 * 5136;
    dma4(LDSADDR(smem) + pl, src, l * 16, l * 16, l * 16, l * 16);
    dma1(LDSADDR(smem) + pl + 4096, src + 4096, l * 16);
    const int gc = g > 2 ? 2 : g;
    const u32 m0 = g < 3 ? ~0u : 0u, m1 = g < 2 ? ~0u : 0u;
    u32 oo = (n2 * 16 + hp) * 5120 + l * 16;
    u32 a0 = l15 * 48 + gc * 16, a1 = (16 + (l15 & 3)) * 48 + gc * 16, a2 = 960 + gc * 16;
    u32 pr = pl + l15 * 80 + 16 * gc, pw = pl + l15 * 80 + 8 * g, pw2 = pl + l15 * 80 + 32 + 4 * (g & 1), pc = pl + l * 16;
    const int u_ = rp >= 105, in = rp - 105 * u_, e_ = (in * 205) >> 10;
    u32 kw = u_ * 1024 + e_ * 48 + (in - 5 * e_) * 8;
    PIN(oo); PIN(a0); PIN(a1); PIN(a2); PIN(pr); PIN(pw); PIN(pw2); PIN(pc); PIN(kw);
    asm volatile("s_waitcnt vmcnt(5)" : "+v"(s[0]), "+v"(s[1]), "+v"(s[2]), "+v"(s[3]), "+v"(s[4]), "+v"(s[5]), "+v"(s[6]), "+v"(s[7]) :: "memory");
    f32x4 sum = ((__builtin_bit_cast(f32x4, s[0]) + __builtin_bit_cast(f32x4, s[1])) + (__builtin_bit_cast(f32x4, s[2]) + __builtin_bit_cast(f32x4, s[3])))
              + ((__builtin_bit_cast(f32x4, s[4]) + __builtin_bit_cast(f32x4, s[5])) + (__builtin_bit_cast(f32x4, s[6]) + __builtin_bit_cast(f32x4, s[7])));
    if (rh) *(LDS_AS f32x4*)((LDS_AS char*)smem + 2048 + rp * 16) = sum;
    RAWBAR();
    if (!rh && rp < 210) {
      sum += *(const LDS_AS f32x4*)((const LDS_AS char*)smem + 2048 + rp * 16);
      ldsw64(smem, kw, u32x2{pk2(sum[0], sum[1]), pk2(sum[2], sum[3])});
    }
    STAMP();
    WAITV(0);
    prefetch();
    u32x4 af[2][3], pf[4][2];
#pragma unroll
    for (int rt = 0; rt < 4; ++rt)
#pragma unroll
      for (int u = 0; u < 2; ++u) {
        u32x2 a = ldsr64(smem, pr + rt * 1280 + 40 * u), bq = ldsr64(smem, pr + rt * 1280 + 40 * u + 8);
        pf[rt][u] = u32x4{a[0], a[1], bq[0], bq[1]};
      }
    RAWBAR();
    STAMP();
#pragma unroll
    for (int u = 0; u < 2; ++u) { af[u][2] = ldsr128(smem, a2 + u * 1024); af[u][0] = ldsr128(smem, a0 + u * 1024); af[u][1] = ldsr128(smem, a1 + u * 1024); }
    SB();
    pf[3][1] &= u32x4{~0u, ~0u, m1, m1};
#pragma unroll
    for (int u = 0; u < 2; ++u)
#pragma unroll
      for (int tI = 0; tI < 3; ++tI) af[u][tI] &= u32x4{m0, m0, m1, m1};
    f32x4 n0[4][2], n1[4][2], dn[4][2];
#pragma unroll
    for (int u = 0; u < 2; ++u)
#pragma unroll
      for (int rt = 0; rt < 4; ++rt) dn[rt][u] = mfma(af[u][2], pf[rt][u], z4);
#pragma unroll
    for (int u = 0; u < 2; ++u)
#pragma unroll
      for (int rt = 0; rt < 4; ++rt) { n0[rt][u] = mfma(af[u][0], pf[rt][u], z4); n1[rt][u] = mfma(af[u][1], pf[rt][u], z4); }
    SB();
    float rd[8]; u32 b0[8], b1[8], b2[8];
#pragma unroll
    for (int k = 0; k < 8; ++k) rd[k] = bfr(dn[k & 3][k >> 2][0]) + 1e-6f;
    SB();
#pragma unroll
    for (int k = 0; k < 8; ++k) rd[k] = __builtin_amdgcn_rcpf(bfr(rd[k]));
    SB();
#pragma unroll
    for (int k = 0; k < 8; ++k) {
      const f32x4 a = n0[k & 3][k >> 2], c2 = n1[k & 3][k >> 2];
      b0[k] = pk2(a[0], a[1]); b1[k] = pk2(a[2], a[3]); b2[k] = (g & 1) ? pk2(c2[2], c2[3]) : pk2(c2[0], c2[1]);
    }
    SB();
#pragma unroll
    for (int k = 0; k < 8; ++k) {
      f32x2 x0 = f32x2{lo2f(b0[k]), hi2f(b0[k])} * rd[k], x1 = f32x2{lo2f(b1[k]), hi2f(b1[k])} * rd[k], x2 = f32x2{lo2f(b2[k]), hi2f(b2[k])} * rd[k];
      b0[k] = pk2(x0[0], x0[1]); b1[k] = pk2(x1[0], x1[1]); b2[k] = pk2(x2[0], x2[1]);
    }
#pragma unroll
    for (int k = 0; k < 8; ++k) {
      ldsw64(smem, pw + (k & 3) * 1280 + 40 * (k >> 2), u32x2{b0[k], b1[k]});
      *(LDS_AS u32*)((LDS_AS char*)smem + pw2 + (k & 3) * 1280 + 40 * (k >> 2)) = b2[k];
    }
    u32x4 o[5];
#pragma unroll
    for (int i = 0; i < 5; ++i) o[i] = ldsr128(smem, pc + i * 1024);
    ST16SO(ws + OFF_O2, oo, 0, o[0]); ST16SO(ws + OFF_O2, oo, 1024, o[1]); ST16SO(ws + OFF_O2, oo, 2048, o[2]); ST16SO(ws + OFF_O2, oo, 3072, o[3]);
    ST16SO(ws + OFF_O2 + 4096, oo, 0, o[4]);
    STAMP();
  }
  STAMP_FLUSH(1);
}

constexpr int K3_W = 81920, K3_AP = 61440, K3_GS = K3_AP + 10240, K3_LDS = K3_W + 3 * 25600;

__global__ __launch_bounds__(896) __attribute__((amdgpu_waves_per_eu(4, 4)))
void k3(const char* __restrict__ ws, u16* __restrict__ out) {
  extern __shared__ __attribute__((aligned(16))) char smem[];
  const int tid = threadIdx.x, l = tid & 63, w = __builtin_amdgcn_readfirstlane(tid >> 6);
  const int g = l >> 4, l15 = l & 15;
  const int b = blockIdx.x, n2 = 4 * (b & 7) + ((b >> 3) & 3), bg = b >> 5;
  const f32x4 z4 = {0.f, 0.f, 0.f, 0.f};
  asm volatile("" :: "s"(ws), "s"(out));
  STAMP_DECL; STAMP();
  if (w >= 10) {
    const int v = w - 10, rot = (bg + (b >> 3)) & 3;
    const u32 lo = l * 16, L = LDSADDR(smem);
    __builtin_amdgcn_s_setprio(2);
    if (v < 2) {
      const char* p = ws + OFF_AP + (size_t)(n2 * 8 + bg) * AP_TS + v * 5120;
      dma4(L + K3_AP + v * 5120, p, lo, lo, lo, lo); dma1(L + K3_AP + v * 5120 + 4096, p + 4096, lo);
    } else dma4(L + K3_GS + (v - 2) * 4096, (const char*)dGsB.v + (v - 2) * 4096, lo, lo, lo, lo);
    const char* op = ws + OFF_O2 + (size_t)n2 * 81920 + v * 5120;
    const char* wp = ws + OFF_WOF + (size_t)bg * 102400;
    auto issue = [&](int Q) {
      const int Qd = (Q + rot) & 3;
      const u32 lw = L + K3_W + (Q == 3 ? 0 : Q) * 25600;
      dma4(L + Q * 20480 + v * 5120, op + Qd * 20480, lo, lo, lo, lo); dma1(L + Q * 20480 + v * 5120 + 4096, op + Qd * 20480 + 4096, lo);
      dma4(lw + v * 5120, wp + v * 20480 + Qd * 5120, lo, lo, lo, lo); dma1(lw + v * 5120 + 4096, wp + v * 20480 + Qd * 5120 + 4096, lo);
      dma1(lw + 20480 + v * 1024, wp + 81920 + Qd * 5120 + v * 1024, lo);
      if (v == 0) dma1(lw + 20480 + 4096, wp + 81920 + Qd * 5120 + 4096, lo);
    };
    issue(0);
    WAITV(11); __builtin_amdgcn_s_barrier();
    issue(1);
    WAITV(11); __builtin_amdgcn_s_barrier();
    issue(2);
    WAITV(11); __builtin_amdgcn_s_barrier();
    issue(3);
    WAITV(11); __builtin_amdgcn_s_barrier();
    WAITV(0); __builtin_amdgcn_s_barrier();
  } else {
    const int ga = w < 5, ct = ga ? w : w - 5;
    const u32 oa = g * 5120 + l15 * 80, wa = K3_W + ct * 5120 + l * 16;
    f32x4 acc[4] = {z4, z4, z4, z4};
    auto quarter = [&](int Q) {
      u32x4 of[5][4], wf[5];
#pragma unroll
      for (int wv = 0; wv < 5; ++wv) {
        wf[wv] = ldsr128(smem, wa + (Q == 3 ? 0 : Q) * 25600 + wv * 1024);
        if (wv < 3) {
#pragma unroll
          for (int rt = 0; rt < 4; ++rt) of[wv][rt] = ldsr128(smem, oa + Q * 20480 + rt * 1280 + wv * 16);
        }
      }
      SB();
#pragma unroll
      for (int wv = 0; wv < 2; ++wv)
#pragma unroll
        for (int rt = 0; rt < 4; ++rt) {
          acc[rt] = mfma(wf[wv], of[wv][rt], acc[rt]);
          SB();
          of[3 + wv][rt] = ldsr128(smem, oa + Q * 20480 + rt * 1280 + (3 + wv) * 16);
          SB();
        }
#pragma unroll
      for (int wv = 2; wv < 5; ++wv)
#pragma unroll
        for (int rt = 0; rt < 4; ++rt) acc[rt] = mfma(wf[wv], of[wv][rt], acc[rt]);
      SB();
      if (Q == 2) __builtin_amdgcn_s_barrier();
    };
    if (!ga) {
      __builtin_amdgcn_s_barrier();
      __builtin_amdgcn_s_barrier();
      STAMP();
      quarter(0);
      RAWBAR();
      __builtin_amdgcn_s_barrier();
      STAMP();
      quarter(2);
#pragma unroll
      for (int rt = 0; rt < 4; ++rt) *(LDS_AS f32x4*)((LDS_AS char*)smem + (ct * 4 + rt) * 1024 + l * 16) = acc[rt];
      __builtin_amdgcn_s_waitcnt(0xC07F);
      STAMP();
    } else {
      u32 gv[4];
      const char* gp = ws + OFF_G + (64 * n2 + l15) * 4;
      GLD1(gv[0], gp, 0); GLD1(gv[1], gp, 64); GLD1(gv[2], gp, 128); GLD1(gv[3], gp, 192);
      u32x4 kd[3];
      const int a = 2 * ct + (l15 >> 3), j = l15 & 7, d = 64 * a + 8 * bg + j;
#pragma unroll
      for (int s = 0; s < 3; ++s) {
        int kc = 4 * s + g, c = kc >> 1;
        float rev = c < 4 ? (float)((c + 1) * d) * (1.f / 640.f) : 0.f;
        float v0 = __builtin_amdgcn_cosf(rev), v1 = c < 4 ? __builtin_amdgcn_sinf(rev) : ((a & 1) ? -1.f : 1.f);
        bool on = kc < 10 && (kc & 1) == (j >> 2);
        u32 pp = on ? pk2(v0, v1) : 0u;
        u32 p0 = (pp & 0xffffu) << ((j & 1) * 16), p1 = (pp >> 16) << ((j & 1) * 16);
        kd[s] = (j & 2) ? u32x4{0u, p0, 0u, p1} : u32x4{p0, 0u, p1, 0u};
        PIN(kd[s]);
      }
      u32 ao = K3_AP + ((l15 >> 1) & 1) * 256 + (l15 >> 2) * 64 + (l15 & 1) * 16 + (g & 1) * 32, ao1 = ao + (g >> 1) * 2048;
      u32 oo = ((32 * l15 + n2) * 640 + 64 * (2 * ct + (g >> 1)) + 8 * bg + 4 * (g & 1)) * 2;
      PIN(ao); PIN(ao1); PIN(oo);
      STAMP();
      __builtin_amdgcn_s_barrier();
      asm volatile("" ::: "memory");
      u32x4 af[4][3], gs[8];
#pragma unroll
      for (int rt = 0; rt < 4; ++rt) { af[rt][0] = ldsr128(smem, ao1 + rt * 512); af[rt][1] = ldsr128(smem, ao1 + 4096 + rt * 512); af[rt][2] = ldsr128(smem, ao + 8192 + rt * 512); }
#pragma unroll
      for (int f = 0; f < 8; ++f) gs[f] = ldsr128(smem, K3_GS + f * 1024 + l * 16);
      SB();
      f32x4 P[4];
#pragma unroll
      for (int s = 0; s < 3; ++s)
#pragma unroll
        for (int rt = 0; rt < 4; ++rt) P[rt] = mfma(af[rt][s], kd[s], s ? P[rt] : z4);
      u32x4 p0 = pkfrag(P[0], P[1]), p1 = pkfrag(P[2], P[3]);
      SB();
      RAWBAR();
      f32x4 yb[4];
#pragma unroll
      for (int nt = 0; nt < 4; ++nt) yb[nt] = mfma(p0, gs[2 * nt], z4);
#pragma unroll
      for (int nt = 0; nt < 4; ++nt) yb[nt] = mfma(p1, gs[2 * nt + 1], yb[nt]);
      asm volatile("s_waitcnt vmcnt(0)" : "+v"(gv[0]), "+v"(gv[1]), "+v"(gv[2]), "+v"(gv[3]) :: "memory");
      u32 yp[4][2];
#pragma unroll
      for (int nt = 0; nt < 4; ++nt) {
        const float hh = bfr(1.f - __uint_as_float(gv[nt]));
        u32 b0 = pk2(yb[nt][0], yb[nt][1]), b1 = pk2(yb[nt][2], yb[nt][3]);
        yp[nt][0] = pk2(hh * lo2f(b0), hh * hi2f(b0)); yp[nt][1] = pk2(hh * lo2f(b1), hh * hi2f(b1));
        PIN(yp[nt][0]); PIN(yp[nt][1]);
      }
      STAMP();
      __builtin_amdgcn_s_barrier();
      asm volatile("" ::: "memory");
      STAMP();
      quarter(1);
      __builtin_amdgcn_s_barrier();
      __builtin_amdgcn_s_barrier();
      asm volatile("" ::: "memory");
      STAMP();
      quarter(3);
      __builtin_amdgcn_s_barrier();
      asm volatile("" ::: "memory");
      f32x4 pb[4];
#pragma unroll
      for (int rt = 0; rt < 4; ++rt) pb[rt] = *(const LDS_AS f32x4*)((const LDS_AS char*)smem + (ct * 4 + rt) * 1024 + l * 16);
      STAMP();
      u32 a0[4], a1[4];
#pragma unroll
      for (int rt = 0; rt < 4; ++rt) { f32x4 t = acc[rt] + pb[rt]; a0[rt] = pk2(t[0], t[1]); a1[rt] = pk2(t[2], t[3]); }
      SB();
#pragma unroll
      for (int rt = 0; rt < 4; ++rt) {
        const float gg = __uint_as_float(gv[rt]);
        a0[rt] = pk2(gg * lo2f(a0[rt]), gg * hi2f(a0[rt])); a1[rt] = pk2(gg * lo2f(a1[rt]), gg * hi2f(a1[rt]));
      }
      SB();
#pragma unroll
      for (int rt = 0; rt < 4; ++rt) {
        u32x2 o = {pk2(lo2f(a0[rt]) + lo2f(yp[rt][0]), hi2f(a0[rt]) + hi2f(yp[rt][0])), pk2(lo2f(a1[rt]) + lo2f(yp[rt][1]), hi2f(a1[rt]) + hi2f(yp[rt][1]))};
        *(u32x2*)((char*)out + (size_t)(oo + rt * (512 * 1280))) = o;
      }
      STAMP();
    }
  }
  STAMP_FLUSH(2);
}

__global__ void kend() {}

extern "C" void kernel_launch(void* const* d_in, const int* in_sizes, int n_in, void* d_out, int out_size, void* d_ws,
                              size_t ws_size, hipStream_t stream) {
  const u16* x = (const u16*)d_in[0];
  const u16* wqkv = (const u16*)d_in[1];
  const u16* wo = (const u16*)d_in[2];
  const u16* wr = (const u16*)d_in[4];
  const u16* wi = (const u16*)d_in[5];
  const u16* gate = (const u16*)d_in[6];
  char* ws = (char*)d_ws;
  (void)hipFuncSetAttribute((const void*)k1, hipFuncAttributeMaxDynamicSharedMemorySize, K1_LDS);
  (void)hipFuncSetAttribute((const void*)k2, hipFuncAttributeMaxDynamicSharedMemorySize, K2_LDS);
  (void)hipFuncSetAttribute((const void*)k3, hipFuncAttributeMaxDynamicSharedMemorySize, K3_LDS);
  k1<<<256, 832, K1_LDS, stream>>>(x, wqkv, wr, wi, gate, wo, ws);
  k2<<<K2_NF + K2_NO, 512, K2_LDS, stream>>>(ws);
  k3<<<256, 896, K3_LDS, stream>>>(ws, (u16*)d_out);
}
```

```cpp
#include <hip/hip_runtime.h>
#include <stdint.h>

typedef unsigned short u16;
typedef unsigned int u32;
typedef short s16x4 __attribute__((ext_vector_type(4)));
typedef short s16x8 __attribute__((ext_vector_type(8)));
typedef __bf16 bf16x8 __attribute__((ext_vector_type(8)));
typedef __bf16 bf16x2 __attribute__((ext_vector_type(2)));
typedef float f32x4 __attribute__((ext_vector_type(4)));
typedef float f32x2 __attribute__((ext_vector_type(2)));
typedef u32 u32x4 __attribute__((ext_vector_type(4)));
typedef u32 u32x2 __attribute__((ext_vector_type(2)));

#define LDS_AS __attribute__((address_space(3)))
#define GLB_AS __attribute__((address_space(1)))
#define DI __device__ __forceinline__
#define STAMP_DECL
#define STAMP()
#define STAMP_FLUSH(K)
__device__ __forceinline__ void dma1(u32 lds, const void* base, u32 v0) {
  u32 keep;
  asm volatile("s_mov_b32 %0, m0\n\ts_mov_b32 m0, %1\n\ts_nop 4\n\tglobal_load_lds_dwordx4 %2, %3\n\ts_mov_b32 m0, %0"
               : "=&s"(keep) : "s"(lds), "v"(v0), "s"(base) : "memory");
}
__device__ __forceinline__ void dma4(u32 lds, const void* base, u32 v0, u32 v1, u32 v2, u32 v3) {
  u32 keep;
  asm volatile("s_mov_b32 %0, m0\n\ts_mov_b32 m0, %1\n\ts_nop 4\n\tglobal_load_lds_dwordx4 %2, %6\n\t"
               "global_load_lds_dwordx4 %3, %6 offset:1024\n\tglobal_load_lds_dwordx4 %4, %6 offset:2048\n\t"
               "global_load_lds_dwordx4 %5, %6 offset:3072\n\ts_mov_b32 m0, %0"
               : "=&s"(keep) : "s"(lds), "v"(v0), "v"(v1), "v"(v2), "v"(v3), "s"(base) : "memory");
}
__device__ __forceinline__ void dma2(u32 lds, const void* base, u32 v0, u32 v1) {
  u32 keep;
  asm volatile("s_mov_b32 %0, m0\n\ts_mov_b32 m0, %1\n\ts_nop 4\n\tglobal_load_lds_dwordx4 %2, %4\n\t"
               "global_load_lds_dwordx4 %3, %4 offset:1024\n\ts_mov_b32 m0, %0"
               : "=&s"(keep) : "s"(lds), "v"(v0), "v"(v1), "s"(base) : "memory");
}
__device__ __forceinline__ void dma1v(u32 lds, const void* src) {
  u32 keep;
  asm volatile("s_mov_b32 %0, m0\n\ts_mov_b32 m0, %1\n\ts_nop 0\n\tglobal_load_lds_dwordx4 %2, off\n\ts_mov_b32 m0, %0"
               : "=&s"(keep) : "s"(lds), "v"(src) : "memory");
}
#define GLD(dst, p, imm) asm volatile("global_load_dwordx4 %0, %1, off offset:" #imm : "=v"(dst) : "v"(p) : "memory")
#define GLD1(dst, p, imm) asm volatile("global_load_dword %0, %1, off offset:" #imm : "=v"(dst) : "v"(p) : "memory")
#define GLDH(dst, p) asm volatile("global_load_ushort %0, %1, off" : "=v"(dst) : "v"(p) : "memory")
#define LDSADDR(p) ((u32)(uintptr_t)(LDS_AS char*)(p))
#define SB() __builtin_amdgcn_sched_barrier(0)
#define WAITV(n) asm volatile("s_waitcnt vmcnt(" #n ")" ::: "memory")
#define RAWBAR() do { asm volatile("" ::: "memory"); __builtin_amdgcn_s_waitcnt(0xC07F); __builtin_amdgcn_s_barrier(); asm volatile("" ::: "memory"); } while (0)

constexpr size_t OFF_PQ  = 0;
constexpr size_t OFF_O2  = OFF_PQ + 2621440;
constexpr size_t OFF_KVP = OFF_O2 + 2621440;
constexpr size_t OFF_VB  = OFF_KVP + 860160;
constexpr int    VB_TS   = 10368;
constexpr size_t OFF_AP  = OFF_VB + (size_t)256 * VB_TS;
constexpr int    AP_TS   = 10240;
constexpr size_t OFF_WOF = OFF_AP + (size_t)256 * AP_TS;
constexpr size_t OFF_G   = OFF_WOF + 819200;

constexpr double kPi = 3.14159265358979323846;
constexpr double tcos(double x) {
  double x2 = x * x, term = 1.0, s = 1.0;
  for (int i = 1; i < 14; ++i) { term *= -x2 / ((2 * i - 1) * (2 * i)); s += term; }
  return s;
}
constexpr double cos64(int k) {
  k &= 63; if (k > 32) k = 64 - k;
  if (k == 16) return 0.0;
  return (k < 16) ? tcos(2 * kPi * k / 64) : -tcos(2 * kPi * (32 - k) / 64);
}
constexpr u16 tobf(double v) {
  u32 u = __builtin_bit_cast(u32, (float)v);
  return (u16)((u + 0x7fffu + ((u >> 16) & 1u)) >> 16);
}
struct C64 { u16 c[64]; };
constexpr C64 mkC64() { C64 t{}; for (int i = 0; i < 64; ++i) t.c[i] = tobf(cos64(i)); return t; }
constexpr C64 kC64 = mkC64();
constexpr u16 gsv(int qp, int n1) {
  int i = qp >> 1;
  if (qp == 1) return kC64.c[(32 * n1) & 63];
  return (qp & 1) ? kC64.c[(i * n1 + 48) & 63] : kC64.c[(i * n1) & 63];
}
constexpr int kperm(int s2, int g, int j) { return 32 * s2 + (j < 4 ? 4 * g + j : 12 + 4 * g + j); }
struct T16 { u16 v[4096]; };
struct T32 { u32 v[4096]; };
constexpr T16 mkGsA() { T16 t{};
  for (int f = 0; f < 8; ++f) for (int l = 0; l < 64; ++l) for (int j = 0; j < 8; ++j)
    t.v[(f * 64 + l) * 8 + j] = gsv(16 * (f >> 1) + (l & 15), kperm(f & 1, l >> 4, j));
  return t; }
constexpr T16 mkGsB() { T16 t{};
  for (int f = 0; f < 8; ++f) for (int l = 0; l < 64; ++l) for (int j = 0; j < 8; ++j)
    t.v[(f * 64 + l) * 8 + j] = gsv(kperm(f & 1, l >> 4, j), 16 * (f >> 1) + (l & 15));
  return t; }
constexpr T32 mkF64() { T32 t{};
  for (int f = 0; f < 16; ++f) for (int l = 0; l < 64; ++l) for (int j = 0; j < 4; ++j) {
    int u = 16 * (f >> 2) + (l & 15), v = 4 * (4 * (f & 3) + (l >> 4)) + j;
    t.v[(f * 64 + l) * 4 + j] = (u32)kC64.c[(u * v) & 63] | ((u32)kC64.c[(u * v + 16) & 63] << 16);
  }
  return t; }
__device__ __attribute__((aligned(128))) const T16 dGsA = mkGsA();
__device__ __attribute__((aligned(128))) const T16 dGsB = mkGsB();
__device__ __attribute__((aligned(128))) const T32 dF64 = mkF64();

DI f32x4 mfma(u32x4 a, u32x4 b, f32x4 c) {
  return __builtin_amdgcn_mfma_f32_16x16x32_bf16(__builtin_bit_cast(bf16x8, a), __builtin_bit_cast(bf16x8, b), c, 0, 0, 0);
}
DI u32 pk2(float lo, float hi) { f32x2 v = {lo, hi}; return __builtin_bit_cast(u32, __builtin_convertvector(v, bf16x2)); }
DI float lo2f(u32 p) { return __uint_as_float(p << 16); }
DI float hi2f(u32 p) { return __uint_as_float(p & 0xffff0000u); }
DI float bfr(float x) { return lo2f(pk2(x, x)); }
DI u32x4 pkfrag(f32x4 a, f32x4 b) { u32x4 r = {pk2(a[0], a[1]), pk2(a[2], a[3]), pk2(b[0], b[1]), pk2(b[2], b[3])}; return r; }
DI float ror8(float v) { return __int_as_float(__builtin_amdgcn_update_dpp(0, __float_as_int(v), 0x128, 0xf, 0xf, true)); }
DI u32x4 ldsr128(const char* smem, u32 off) { return *(const LDS_AS u32x4*)((const LDS_AS char*)smem + off); }
DI u32x2 ldstr(const char* smem, u32 off) {
  return __builtin_bit_cast(u32x2, __builtin_amdgcn_ds_read_tr16_b64_v4i16((LDS_AS s16x4*)((LDS_AS char*)smem + off)));
}
DI void st16wt(void* p, u32x4 v) { asm volatile("global_store_dwordx4 %0, %1, off sc1\n\ts_nop 1" :: "v"(p), "v"(v) : "memory"); }
DI void st4wt(void* p, u32 v) { asm volatile("global_store_dword %0, %1, off sc1" :: "v"(p), "v"(v) : "memory"); }
DI void l2touch(u32 lds, const void* p) {
  u32 keep;
  asm volatile("s_mov_b32 %0, m0\n\ts_mov_b32 m0, %1\n\ts_nop 0\n\tglobal_load_lds_dword %2, off\n\ts_mov_b32 m0, %0"
               : "=&s"(keep) : "s"(lds), "v"(p) : "memory");
}
DI void st16p(char* b, u32 o, f32x4 v) { *(f32x4*)(b + (size_t)o) = v; }
DI void st16s(const void* b, u32 o, u32x4 v) { asm volatile("global_store_dwordx4 %0, %1, %2 sc1\n\ts_nop 1" :: "v"(o), "v"(v), "s"(b) : "memory"); }
#define ST16SO(b, o, imm, v) asm volatile("global_store_dwordx4 %0, %1, %2 offset:" #imm " sc1\n\ts_nop 1" :: "v"(o), "v"(v), "s"(b) : "memory")
#define ST4SO(b, o, imm, v) asm volatile("global_store_dword %0, %1, %2 offset:%3 sc1" :: "v"(o), "v"(v), "s"(b), "i"(imm) : "memory")
#define ST4O(p, imm, v) asm volatile("global_store_dword %0, %1, off offset:%2 sc1" :: "v"(p), "v"(v), "i"(imm) : "memory")
DI void ldsw128(char* smem, u32 off, u32x4 v) { *(LDS_AS u32x4*)((LDS_AS char*)smem + off) = v; }
DI void ldsw64(char* smem, u32 off, u32x2 v) { *(LDS_AS u32x2*)((LDS_AS char*)smem + off) = v; }
DI u32x2 ldsr64(const char* smem, u32 off) { return *(const LDS_AS u32x2*)((const LDS_AS char*)smem + off); }
DI u32 ldsr16(const char* smem, u32 off) { return *(const LDS_AS u16*)((const LDS_AS char*)smem + off); }
template <int N> DI void phi4(u32x4 (&p)[N]) {
  f32x2 t[4 * N], e[4 * N];
#pragma unroll
  for (int i = 0; i < 4 * N; ++i) t[i] = f32x2{lo2f(p[i >> 2][i & 3]), hi2f(p[i >> 2][i & 3])};
  SB();
#pragma unroll
  for (int i = 0; i < 4 * N; ++i) e[i] = t[i] * 1.44269504088896f;
  SB();
#pragma unroll
  for (int i = 0; i < 4 * N; ++i) {
    e[i][0] = __builtin_amdgcn_fmed3f(__builtin_amdgcn_exp2f(e[i][0]), 0.f, 1.f); e[i][1] = __builtin_amdgcn_fmed3f(__builtin_amdgcn_exp2f(e[i][1]), 0.f, 1.f);
    t[i][0] = __builtin_fmaxf(t[i][0], 0.f); t[i][1] = __builtin_fmaxf(t[i][1], 0.f);
  }
  SB();
  float mn = 1.f;
#pragma unroll
  for (int i = 0; i < 4 * N; ++i) { mn = __builtin_fminf(__builtin_fminf(mn, e[i][0]), e[i][1]); e[i] += t[i]; }
  SB();
#pragma unroll
  for (int i = 0; i < 4 * N; ++i) p[i >> 2][i & 3] = pk2(e[i][0], e[i][1]);
  if (__builtin_amdgcn_ballot_w64(mn < 0.001f)) {
#pragma unroll
    for (int n = 0; n < N; ++n)
#pragma unroll
      for (int i = 0; i < 4; ++i) p[n][i] = pk2(lo2f(p[n][i]) + 1e-6f, hi2f(p[n][i]) + 1e-6f);
  }
}
template <int CTRL> DI float dppf(float v) { return __int_as_float(__builtin_amdgcn_update_dpp(0, __float_as_int(v), CTRL, 0xf, 0xf, true)); }
DI u32x4 onehot(u32 s, int d) { u32x4 r = {d == 0 ? s : 0u, d == 1 ? s : 0u, d == 2 ? s : 0u, d == 3 ? s : 0u}; return r; }

constexpr int K1_SLOT = 16384, K1_B = 65536, K1_AUX = 131072, K1_LDS = K1_AUX + 272;

struct Frags { u32x4 x[2][2], w[2][4]; };
#define PIN(v) asm volatile("" : "+v"(v))

template <bool QT>
DI void k1_gemm(char* smem, const u16* x, const u16* gate, char* ws, int m, int nt, int w, int l
                ) {
  const int g = l >> 4, l15 = l & 15, rg = w & 3, chh = w >> 2;
  {
    u32 gxv[5], gwv[5];
    const int r0 = 8 * nt + w;
#pragma unroll
    for (int i = 0; i < 5; ++i) {
      gxv[i] = ((const u32*)(x + (32 * (r0 & 63) + 2 * m + (r0 >> 6)) * 640))[l + 64 * i];
      gwv[i] = ((const u32*)gate)[l + 64 * i];
    }
    float g0 = 0.f, g1 = 0.f;
#pragma unroll
    for (int i = 0; i < 5; ++i) {
      u32 pr = pk2(lo2f(gxv[i]) * lo2f(gwv[i]), hi2f(gxv[i]) * hi2f(gwv[i]));
      g0 += lo2f(pr); g1 += hi2f(pr);
    }
    g0 += g1;
    g0 += dppf<0xB1>(g0); g0 += dppf<0x4E>(g0); g0 += dppf<0x124>(g0); g0 += dppf<0x128>(g0);
    float tot = __uint_as_float(__builtin_amdgcn_readlane(__float_as_uint(g0), 0)) + __uint_as_float(__builtin_amdgcn_readlane(__float_as_uint(g0), 16))
              + __uint_as_float(__builtin_amdgcn_readlane(__float_as_uint(g0), 32)) + __uint_as_float(__builtin_amdgcn_readlane(__float_as_uint(g0), 48));
    float gt = bfr(__builtin_amdgcn_rcpf(1.f + __builtin_amdgcn_exp2f(bfr(tot) * -1.44269504088896f)));
    if (l == 0) ((float*)(ws + OFF_G))[128 * m + r0] = gt;
  }
  u32 xa[2], wa[4][2];
  xa[0] = rg * 4096 + l15 * 128 + ((g ^ ((l15 >> 1) & 7)) << 4); xa[1] = xa[0] ^ 64;
#pragma unroll
  for (int t2 = 0; t2 < 2; ++t2) {
    const int q4 = l15 >> 2, swz = (q4 << 2) | ((2 * g + t2) & 3);
    const u32 b0 = K1_B + (8 * g + 4 * t2 + q4) * 256 + (((((l >> 1) & 1) | ((QT ? 8 : 4) * chh)) ^ swz) << 4) + 8 * (l & 1);
#pragma unroll
    for (int i = 0; i < 4; ++i) { wa[i][t2] = b0 ^ ((QT ? i : (i & 1) + 4 * (i >> 1)) << 5); PIN(wa[i][t2]); }
  }
  f32x4 acc[2][4];
#pragma unroll
  for (int rt = 0; rt < 2; ++rt)
#pragma unroll
    for (int i = 0; i < 4; ++i) acc[rt][i] = f32x4{0.f, 0.f, 0.f, 0.f};
  Frags F[2];
  auto rd = [&](Frags& f, int C) {
    const int so = (C & 3) * K1_SLOT;
#pragma unroll
    for (int ks = 0; ks < 2; ++ks) {
#pragma unroll
      for (int rt = 0; rt < 2; ++rt) f.x[ks][rt] = ldsr128(smem, xa[ks] + so + rt * 2048);
#pragma unroll
      for (int i = 0; i < 4; ++i) {
        u32x2 a = ldstr(smem, wa[i][0] + so + ks * 8192), b = ldstr(smem, wa[i][1] + so + ks * 8192);
        f.w[ks][i] = u32x4{a[0], a[1], b[0], b[1]};
      }
    }
  };
  auto mm = [&](const Frags& f) {
#pragma unroll
    for (int ks = 0; ks < 2; ++ks)
#pragma unroll
      for (int i = 0; i < 4; ++i)
#pragma unroll
        for (int rt = 0; rt < 2; ++rt)
          acc[rt][i] = QT ? mfma(f.w[ks][i], f.x[ks][rt], acc[rt][i]) : mfma(f.x[ks][rt], f.w[ks][i], acc[rt][i]);
  };
  STAMP();
  RAWBAR();
  STAMP();
  rd(F[0], 0);
  SB();
  u32 eo[2], ra[2], rb[2];
  if (QT) {
    const int ck = 16 * (nt - 11) + 8 * chh + (l & 7), hp = (ck * 205) >> 10;
    eo[0] = (hp * 2048 + 128 * m + 32 * rg + (l >> 3)) * 80 + (ck - 5 * hp) * 16;
    ra[0] = 2 * K1_SLOT + chh * K1_B + rg * 4608 + l15 * 144 + g * 8;
    rb[0] = 2 * K1_SLOT + chh * K1_B + rg * 4608 + (l >> 3) * 144 + (l & 7) * 16;
    PIN(eo[0]); PIN(ra[0]); PIN(rb[0]);
  } else {
#pragma unroll
    for (int k = 0; k < 2; ++k) {
      int P = w + 8 * k;
      int pi = (0x103322211100ull >> (4 * P)) & 15, pj = (0x333232121010ull >> (4 * P)) & 15;
      int d = 16 * pi + 4 * g, e = 16 * pj + l15, hd = (d * 205) >> 12, he = (e * 205) >> 12;
      int h = 3 * nt + hd;
      bool ok = P < 12 && d < 60 && h < 32 && (e == 60 || (e < 60 && he == hd));
      int ee = e == 60 ? 20 : e - 20 * he;
      eo[k] = ok ? (((m * 32 + h) * 21 + ee) * 20 + (d - 20 * hd)) * 4 : ~0u;
      ra[k] = 2 * K1_SLOT + pi * 1024 + l * 16; rb[k] = 2 * K1_SLOT + (4 + pj) * 1024 + l * 16;
      PIN(eo[k]); PIN(ra[k]); PIN(rb[k]);
    }
  }
  SB();
#pragma unroll
  for (int C = 0; C < 10; ++C) {
    Frags& cur = F[C & 1]; Frags& nxt = F[(C + 1) & 1];
    if (C == 8) { mm(cur); SB(); }
    if (C < 9) RAWBAR();
    SB();
    if (C < 9) rd(nxt, C + 1);
    if (C != 8) mm(cur);
    if (C < 8) {
#pragma unroll
      for (int i = 0; i < 4; ++i) {
        __builtin_amdgcn_sched_group_barrier(0x008, 1, 0); __builtin_amdgcn_sched_group_barrier(0x100, 2, 0);
#pragma unroll
        for (int k = 0; k < 3; ++k) { __builtin_amdgcn_sched_group_barrier(0x008, 1, 0); __builtin_amdgcn_sched_group_barrier(0x100, 1, 0); }
      }
    }
    SB();
  }
  STAMP();
  if (QT) {
    u32x4 pa[4];
#pragma unroll
    for (int i = 0; i < 4; ++i) pa[i] = pkfrag(acc[0][i], acc[1][i]);
    SB();
    phi4(pa);
#pragma unroll
    for (int i = 0; i < 4; ++i) { ldsw64(smem, ra[0] + i * 32, u32x2{pa[i][0], pa[i][1]}); ldsw64(smem, ra[0] + 2304 + i * 32, u32x2{pa[i][2], pa[i][3]}); }
    u32x4 o[4];
#pragma unroll
    for (int i = 0; i < 4; ++i) o[i] = ldsr128(smem, rb[0] + i * 1152);
    ST16SO(ws + OFF_PQ, eo[0], 0, o[0]); ST16SO(ws + OFF_PQ, eo[0], 640, o[1]); ST16SO(ws + OFF_PQ, eo[0], 1280, o[2]); ST16SO(ws + OFF_PQ, eo[0], 1920, o[3]);
  } else {
    u32x4 v1 = pkfrag(acc[0][3], acc[1][3]);
    if (chh && l15 == 12) v1 = u32x4{0x3f803f80u, 0x3f803f80u, 0x3f803f80u, 0x3f803f80u};
    const u32 fo = 2 * K1_SLOT + (rg * 8 + 2 * chh) * 1024 + l * 16;
    ldsw128(smem, fo + 4096, pkfrag(acc[0][2], acc[1][2]));
    ldsw128(smem, fo + 5120, v1);
    u32x4 pa[2] = {pkfrag(acc[0][0], acc[1][0]), pkfrag(acc[0][1], acc[1][1])};
    phi4(pa);
    ldsw128(smem, fo, pa[0]);
    ldsw128(smem, fo + 1024, pa[1]);
    STAMP();
    __builtin_amdgcn_s_waitcnt(0xC07F);
    LDS_AS u32* cnt = (LDS_AS u32*)((LDS_AS char*)smem + K1_AUX);
    if (l == 0) __hip_atomic_fetch_add(cnt, 1u, __ATOMIC_RELAXED, __HIP_MEMORY_SCOPE_WORKGROUP);
    while (__builtin_amdgcn_readfirstlane(__hip_atomic_load(cnt, __ATOMIC_RELAXED, __HIP_MEMORY_SCOPE_WORKGROUP)) < 8u) {}
    asm volatile("" ::: "memory");
    STAMP();
    u32x4 fa[4], fb[4];
    f32x4 r0 = {0.f, 0.f, 0.f, 0.f};
#pragma unroll
    for (int r = 0; r < 4; ++r) { fa[r] = ldsr128(smem, ra[0] + r * 8192); fb[r] = ldsr128(smem, rb[0] + r * 8192); }
    if (w < 4) {
      u32x4 fc[4], fd[4];
      f32x4 r1 = r0;
#pragma unroll
      for (int r = 0; r < 4; ++r) { fc[r] = ldsr128(smem, ra[1] + r * 8192); fd[r] = ldsr128(smem, rb[1] + r * 8192); }
      SB();
#pragma unroll
      for (int r = 0; r < 4; ++r) { r0 = mfma(fa[r], fb[r], r0); r1 = mfma(fc[r], fd[r], r1); }
      if (eo[1] != ~0u) st16p(ws + OFF_KVP, eo[1], r1);
    } else {
      SB();
#pragma unroll
      for (int r = 0; r < 4; ++r) r0 = mfma(fa[r], fb[r], r0);
    }
    if (eo[0] != ~0u) st16p(ws + OFF_KVP, eo[0], r0);
  }
}

template <bool C4>
DI void k1_dft(const char* vb, const u32x4 (&gs)[8], const u32x4 (&xq)[4][3], u32 vo, u32 k01, u32 k2, float tc, float ts, int l) {
  constexpr int NB = 1;
  const u32 kv01[1] = {k01}, kv2[1] = {k2};
  const int g = l >> 4, h = (l >> 3) & 1, j = l & 7;
  const u32 hm = (j & 1) ? 0xffff0000u : 0xffffu;
  const f32x4 z4 = {0.f, 0.f, 0.f, 0.f};
  u32x4 kd[NB][3], tb[NB][2];
  f32x4 t[NB][4], y[NB][4];
#pragma unroll
  for (int u = 0; u < NB; ++u) {
    kd[u][0] = onehot(((j & 1) ? kv01[u] << 16 : kv01[u]) & hm, j >> 1); kd[u][1] = onehot(((j & 1) ? kv01[u] : kv01[u] >> 16) & hm, j >> 1);
    kd[u][2] = onehot(kv2[u] & hm, j >> 1);
  }
  SB();
#pragma unroll
  for (int s = 0; s < 3; ++s)
#pragma unroll
    for (int u = 0; u < NB; ++u)
#pragma unroll
      for (int rt = 0; rt < 4; ++rt) t[u][rt] = mfma(xq[rt][s], kd[u][s], s ? t[u][rt] : z4);
#pragma unroll
  for (int u = 0; u < NB; ++u) { tb[u][0] = pkfrag(t[u][0], t[u][1]); tb[u][1] = pkfrag(t[u][2], t[u][3]); }
#pragma unroll
  for (int k = 0; k < 2; ++k)
#pragma unroll
    for (int u = 0; u < NB; ++u)
#pragma unroll
      for (int mt = 0; mt < 4; ++mt) y[u][mt] = mfma(gs[2 * mt + k], tb[u][k], k ? y[u][mt] : z4);
  SB();
#pragma unroll
  for (int u = 0; u < NB; ++u) {
    float X[8], Y[8]; u32 pk[8];
    const u32 vu = vo;
    if (!C4) {
#pragma unroll
      for (int i = 0; i < 8; ++i) { X[i] = y[u][i >> 1][2 * (i & 1)] - ror8(y[u][i >> 1][2 * (i & 1) + 1]); Y[i] = y[u][i >> 1][2 * (i & 1) + 1] + ror8(y[u][i >> 1][2 * (i & 1)]); }
      float p0 = ror8(y[u][0][0]), p1 = ror8(y[u][0][1]);
      X[0] = g == 0 ? (h ? y[u][0][1] : y[u][0][0]) : X[0]; Y[0] = g == 0 ? (h ? p1 : p0) : Y[0];
      SB();
#pragma unroll
      for (int i = 0; i < 8; ++i) pk[i] = pk2(X[i], Y[i]);
      SB();
#pragma unroll
      for (int i = 0; i < 8; ++i) pk[i] = __builtin_amdgcn_alignbit(pk[i], pk[i], 16 * h);
    } else {
      const float o1 = y[u][0][1];
      if (g == 0) ST4SO(vb, vu, 2048, pk2(o1 * tc, o1 * ts));
      y[u][0][1] = g == 0 ? 0.f : o1;
#pragma unroll
      for (int i = 0; i < 8; ++i) {
        float o0 = y[u][i >> 1][2 * (i & 1)], o2 = y[u][i >> 1][2 * (i & 1) + 1];
        X[i] = o0 * tc - o2 * ts; Y[i] = o0 * ts + o2 * tc;
      }
      SB();
#pragma unroll
      for (int i = 0; i < 8; ++i) pk[i] = pk2(X[i], Y[i]);
    }
    SB();
#pragma unroll
    for (int i = 0; i < 8; ++i) ST4SO(vb, vu, i * 256, pk[i]);
  }
}

__global__ __launch_bounds__(1024) __attribute__((amdgpu_waves_per_eu(4, 4)))
void k1(const u16* __restrict__ x, const u16* __restrict__ wqkv, const u16* __restrict__ wr, const u16* __restrict__ wi,
        const u16* __restrict__ gate, const u16* __restrict__ wo, char* __restrict__ ws) {
  extern __shared__ __attribute__((aligned(16))) char smem[];
  const int tid = threadIdx.x, l = tid & 63, w = __builtin_amdgcn_readfirstlane(tid >> 6);
  const int b = blockIdx.x, xcd = b & 7, bi = b >> 3;
  const int m = (xcd >> 1) * 4 + (bi & 3), nt = (xcd & 1) * 8 + (bi >> 2);
  asm volatile("" :: "s"(x), "s"(wqkv), "s"(wr), "s"(wi), "s"(gate), "s"(wo), "s"(ws));
  STAMP_DECL; STAMP();
#define TSARG
  if (w >= 8) {
    const int v = w - 8, u = v >> 1;
    int cn = ((nt & 7) * 5 >> 2) + ((m & 3) * 5 >> 1); if (cn >= 10) cn -= 10;
    const int cq = nt >= 11, cbase = cq ? 128 * (nt - 11) : 640 + 60 * nt, cs = cq ? 64 : 640, cmax = nt == 10 ? 4 : 7;
    u32 da[4];
#pragma unroll
    for (int e = 0; e < 4; ++e) {
      const int cbk = (l & 15) ^ (((l >> 4) << 2) | e);
      da[e] = ((v & 1) ? (4 * e + (l >> 4)) * 3840 + ((cbk >> 3) * cs + 8 * min(cbk & 7, cmax)) * 2
                       : (8 * e + (l >> 3)) * 40960 + (((l & 7) ^ ((4 * e + (l >> 4)) & 7)) << 4)) - 1024 * e;
    }
    const char* sa = (v & 1) ? (const char*)wqkv + u * (16 * 3840) + cbase * 2 : (const char*)x + ((u & 1) * 1024 + 2 * m + (u >> 1)) * 1280;
    const int cstep = (v & 1) ? 64 * 3840 : 128;
    const u32 dl = LDSADDR(smem) + u * 4096 + (v & 1) * K1_B;
    int cc = cn;
    auto issue = [&](int slot) {
      dma4(dl + slot * K1_SLOT, sa + cn * cstep, da[0], da[1], da[2], da[3]);
      cn = cn == 9 ? 0 : cn + 1;
    };
    __builtin_amdgcn_s_setprio(2);
    issue(0); issue(1);
    STAMP();
    if (l == 0 && v == 0) *(LDS_AS u32*)((LDS_AS char*)smem + K1_AUX) = 0u;
    const int n2s = nt >> 3, bg = nt & 7, g = l >> 4;
    const u32 ca = n2s * 8192 + (l & 15) * 128 + ((bg ^ ((l >> 1) & 7)) << 4);
    u32x4 xq[4][3];
#pragma unroll
    for (int rt = 0; rt < 4; ++rt) xq[rt][0] = xq[rt][1] = xq[rt][2] = u32x4{0u, 0u, 0u, 0u};
    auto step = [&](int C) {
      RAWBAR();
      if (C < 7) issue((C + 3) & 3);
      {
        const u32 o = LDSADDR(smem) + ca + ((C + 1) & 3) * K1_SLOT;
        const unsigned long long mk = (v < 5 ? 0xffffull : 0ull) << (16 * (cc & 3));
        asm volatile("s_mov_b64 exec, %13\n\ts_cmp_lt_u32 %14, 4\n\ts_cbranch_scc0 1f\n\t"
                     "ds_read_b128 %0, %12\n\tds_read_b128 %1, %12 offset:2048\n\tds_read_b128 %2, %12 offset:4096\n\tds_read_b128 %3, %12 offset:6144\n\ts_branch 3f\n"
                     "1:\n\ts_cmp_lt_u32 %14, 8\n\ts_cbranch_scc0 2f\n\t"
                     "ds_read_b128 %4, %12\n\tds_read_b128 %5, %12 offset:2048\n\tds_read_b128 %6, %12 offset:4096\n\tds_read_b128 %7, %12 offset:6144\n\ts_branch 3f\n"
                     "2:\n\tds_read_b128 %8, %12\n\tds_read_b128 %9, %12 offset:2048\n\tds_read_b128 %10, %12 offset:4096\n\tds_read_b128 %11, %12 offset:6144\n"
                     "3:\n\ts_mov_b64 exec, -1"
                     : "+v"(xq[0][0]), "+v"(xq[1][0]), "+v"(xq[2][0]), "+v"(xq[3][0]), "+v"(xq[0][1]), "+v"(xq[1][1]), "+v"(xq[2][1]), "+v"(xq[3][1]),
                       "+v"(xq[0][2]), "+v"(xq[1][2]), "+v"(xq[2][2]), "+v"(xq[3][2]) : "v"(o), "s"(mk), "s"(cc) : "memory", "scc");
      }
      cc = cc == 9 ? 0 : cc + 1;
    };
#pragma unroll 1
    for (int C = -1; C < 2; ++C) { WAITV(4); step(C); }
    u32x4 gs[8];
    {
      const char* gp = (const char*)dGsA.v + l * 16 + 4096;
      GLD(gs[0], gp, -4096); GLD(gs[1], gp, -3072); GLD(gs[2], gp, -2048); GLD(gs[3], gp, -1024);
      GLD(gs[4], gp, 0); GLD(gs[5], gp, 1024); GLD(gs[6], gp, 2048); GLD(gs[7], gp, 3072);
    }
    const int h = (l >> 3) & 1, bb = 8 * bg + (l & 7);
    u32 kv01, kv2;
    {
      float c3[3];
#pragma unroll
      for (int s = 0; s < 3; ++s) c3[s] = __builtin_amdgcn_cosf((float)((v + 1) * (64 * (4 * s + g) + bb)) * (1.f / 640.f) - 0.25f * h);
      kv01 = pk2(c3[0], c3[1]); kv2 = pk2(c3[2], c3[2]);
      if (v == 4) kv01 = kv2 = (h & g) ? 0xbf80bf80u : 0x3f803f80u;
    }
    float tc = h ? __builtin_amdgcn_cosf(bb * (1.f / 128.f)) : 1.f, ts = h ? __builtin_amdgcn_sinf(bb * (1.f / 128.f)) : 0.f;
    u32 vo = ((2 * m + n2s) * 8 + bg) * VB_TS + l * 4;
    PIN(kv01); PIN(kv2); PIN(tc); PIN(ts); PIN(vo);
    WAITV(12); step(2);
#pragma unroll 1
    for (int C = 3; C < 7; ++C) { WAITV(4); step(C); }
    STAMP();
    {
      const u32 dump = LDSADDR(smem) + K1_AUX + 16;
      const int ci = 8 * ((b & 7) >> 1) + 2 * (b >> 6) + (b & 1), q = ((b >> 5) & 1) ? (ci ? 64 - ci : 32) : ci;
      const u32 wo2 = ((q + 64 * (8 * ((b >> 3) & 3) + (l >> 3))) * 642 + min((l & 7) * 128, 638)) & ~3u;
      const char* pc; int d0, d1;
      asm volatile("s_getpc_b64 %0\n1:\n\ts_mov_b32 %1, _Z2k1PKtS0_S0_S0_S0_S0_Pc-1b\n\ts_mov_b32 %2, _Z4kendv-1b" : "=s"(pc), "=s"(d0), "=s"(d1));
      const char* ca2 = l < 12 ? pc + d0 + (bi * 12 + l) * 128 : (const char*)dF64.v + (bi * 4 + (l & 3)) * 128;
      if (ca2 >= pc + d1 && l < 12) ca2 = pc;
      l2touch(dump, v == 6 ? (const char*)wr + wo2 : (v == 7 ? (const char*)wi + wo2 : (v == 5 ? ca2 : pc)));
    }
    WAITV(5); step(7);
    WAITV(1); step(8);
    asm volatile("s_waitcnt lgkmcnt(0)" : "+v"(xq[0][0]), "+v"(xq[1][0]), "+v"(xq[2][0]), "+v"(xq[3][0]), "+v"(xq[0][1]), "+v"(xq[1][1]), "+v"(xq[2][1]), "+v"(xq[3][1]),
                 "+v"(xq[0][2]), "+v"(xq[1][2]), "+v"(xq[2][2]), "+v"(xq[3][2]),
                 "+v"(gs[0]), "+v"(gs[1]), "+v"(gs[2]), "+v"(gs[3]), "+v"(gs[4]), "+v"(gs[5]), "+v"(gs[6]), "+v"(gs[7]) :: "memory");
    STAMP();
    if (v == 5) {
      const int id = 25 * b + min(l, 24), ko = (id * 52429) >> 22, cb = id - 80 * ko, a = cb >> 3, hp = (ko * 205) >> 10, wq = ko - 5 * hp;
#pragma unroll
      for (int i = 0; i < 8; ++i) gs[i] = *(const u32x4*)((const char*)wo + ko * 10240 + cb * 16 + i * 1280);
      u32x4* dst = (u32x4*)(ws + OFF_WOF) + ((((cb & 7) * 5 + (a >> 1)) * 20 + (hp >> 2) * 5 + wq) * 64 + (hp & 3) * 16 + (a & 1) * 8);
#pragma unroll
      for (int jj = 0; jj < 8; ++jj) {
        u32x4 o2;
#pragma unroll
        for (int d = 0; d < 4; ++d) o2[d] = __builtin_amdgcn_perm(gs[2 * d + 1][jj >> 1], gs[2 * d][jj >> 1], (jj & 1) ? 0x07060302u : 0x05040100u);
        dst[jj] = o2;
      }
    }
    asm volatile("" : "+v"(gs[0]), "+v"(gs[1]), "+v"(gs[2]), "+v"(gs[3]), "+v"(gs[4]), "+v"(gs[5]), "+v"(gs[6]), "+v"(gs[7]));
    if (v < 4) k1_dft<false>(ws + OFF_VB, gs, xq, vo + v * 2048, kv01, kv2, tc, ts, l);
    else if (v == 4) k1_dft<true>(ws + OFF_VB, gs, xq, vo + 8192, kv01, kv2, tc, ts, l);
  } else if (nt >= 11) k1_gemm<true>(smem, x, gate, ws, m, nt, w, l TSARG);
  else k1_gemm<false>(smem, x, gate, ws, m, nt, w, l TSARG);
  STAMP();
  STAMP_FLUSH(0);
}

constexpr int K2_V = 0, K2_A = 16384, K2_U = 32768, K2_US = 272, K2_PF = K2_U + 2 * 32 * K2_US, K2_W = K2_PF + 272, WROW = 656, K2_LDS = K2_W + 128 * WROW;
constexpr int K2_NF = 168;
constexpr int K2_NO = 64;

__global__ __launch_bounds__(1024) __attribute__((amdgpu_waves_per_eu(4, 4)))
void k2(const u16* __restrict__ wr, const u16* __restrict__ wi, char* __restrict__ ws) {
  extern __shared__ __attribute__((aligned(16))) char smem[];
  const int tid = threadIdx.x, l = tid & 63, w8 = __builtin_amdgcn_readfirstlane(tid >> 6);
  const int g = l >> 4, l15 = l & 15;
  const int b = blockIdx.x;
  const f32x4 z4 = {0.f, 0.f, 0.f, 0.f};
  asm volatile("" :: "s"(wr), "s"(wi), "s"(ws));
  auto prefetch = [&]() {
    const int i = (b >> 3) * 224 + tid;
    if (tid < 224 && i < 6400) l2touch(LDSADDR(smem) + K2_PF, ws + OFF_WOF + (size_t)i * 128);
    if (tid >= 448 && tid < 451 && (b >> 3) * 3 + tid - 448 < 64) l2touch(LDSADDR(smem) + K2_PF, (const char*)dGsB.v + ((b >> 3) * 3 + tid - 448) * 128);
  };
  STAMP_DECL; STAMP();
  if (b < K2_NF) {
    const int sidx = (b >> 5) * 32 + (b & 7) * 4 + ((b >> 3) & 3);
    if (sidx > 160) { prefetch(); return; }
    const int c = sidx >> 5 > 4 ? 4 : sidx >> 5;
    const int ci = sidx == 160 ? 32 : ((sidx >> 3) & 3) * 8 + (sidx & 3) * 2 + ((sidx >> 2) & 1);
    const int qa = ci, qb = (c < 4 && ci == 0) ? 32 : (64 - ci) & 63;
    LDS_AS u32* cnt = (LDS_AS u32*)((LDS_AS char*)smem + K2_PF + 256);
    if (w8 >= 8) {
      const int v = w8 - 8;
      if (tid == 512) *cnt = 0u;
      u32 vo[11];
#pragma unroll
      for (int i = 0; i < 11; ++i) { const int P = 64 * i + l; vo[i] = (P - 64 * (i & 3)) * 16 + ((P * 1599) >> 16) * (41088 - 656); }
      const char* wsrc = (const char*)((v >> 2) ? wi : wr) + (((((v >> 1) & 1) ? qb : qa) * 642) & ~15) + (v & 1) * (16 * 41088);
      const u32 L = LDSADDR(smem) + K2_W + v * 16 * WROW;
      RAWBAR();
      dma4(L, wsrc, vo[0], vo[1], vo[2], vo[3]); dma4(L + 4096, wsrc, vo[4], vo[5], vo[6], vo[7]); dma2(L + 8192, wsrc, vo[8], vo[9]);
      if (l < 16) dma1(L + 10240, wsrc + 2048, vo[10]);
      WAITV(0);
      STAMP_FLUSH(1);
      return;
    }
    const int half = w8 >> 2, w4 = w8 & 3;
    {
      int kc0 = l15 ^ ((8 * w4 + g) & 15), kc1 = kc0 ^ 4;
      u32 v0 = ((8 * w4 + g) * 8 + (kc0 >> 1)) * VB_TS + (kc0 & 1) * 16, v1 = ((8 * w4 + 4 + g) * 8 + (kc1 >> 1)) * VB_TS + (kc1 & 1) * 16 - 1024;
      dma2(LDSADDR(smem) + K2_V + w8 * 2048, ws + OFF_VB + sidx * 64 + half * 32, v0, v1);
    }
    u32x4 tr[4], ti[4];
    { const char* fp = (const char*)dF64.v + w4 * 4096 + l * 16; GLD(tr[0], fp, 0); GLD(tr[1], fp, 1024); GLD(tr[2], fp, 2048); GLD(tr[3], fp, 3072); }
    __builtin_amdgcn_s_barrier();
    const int q = c == 4 ? ci : (half == 0 ? ci : (ci == 0 ? 32 : 64 - ci));
    {
      const int ri = 16 * (w4 & 1) + l15;
      const int B = w4 < 2 ? q + 64 * ri : 64 * ri, A = (w4 < 2 ? 0 : ri * q) + B * 4 * g;
      float cs[8], sn[8];
#pragma unroll
      for (int j = 0; j < 8; ++j) {
        float rev = (float)((A + B * (j < 4 ? j : 12 + j)) & 2047) * (1.f / 2048.f);
        cs[j] = __builtin_amdgcn_cosf(rev); sn[j] = __builtin_amdgcn_sinf(rev);
      }
      ldsw128(smem, K2_A + w8 * 2048 + l * 16, u32x4{pk2(cs[0], cs[1]), pk2(cs[2], cs[3]), pk2(cs[4], cs[5]), pk2(cs[6], cs[7])});
      ldsw128(smem, K2_A + w8 * 2048 + 1024 + l * 16, u32x4{pk2(sn[0], sn[1]), pk2(sn[2], sn[3]), pk2(sn[4], sn[5]), pk2(sn[6], sn[7])});
    }
    const float sc = 2.f / (2048.f * 640.f), s2 = (c == 4 && (ci & 31) == 0) ? 0.5f * sc : sc;
    f32x4 tc = z4, ts = z4;
    if (c == 4) {
#pragma unroll
      for (int i = 0; i < 4; ++i) {
        float rev = (float)(16 * w4 + 4 * g + i) * (1.f / 128.f);
        tc[i] = __builtin_amdgcn_cosf(rev) * s2; ts[i] = __builtin_amdgcn_sinf(rev) * s2;
      }
    }
    const int so = sidx == 160 ? 128 : sidx;
    char* dst = ws + OFF_AP + (size_t)((16 * half + l15) * 8 + 2 * w4 + (g >> 1)) * AP_TS + so * 64 + (g & 1) * 32;
    u32 uw = K2_U + half * (32 * K2_US) + 4 * g * K2_US + (16 * w4 + l15) * 4, ur0 = K2_U + (16 * half + l15) * K2_US + g * 16;
    u32 va = K2_V + half * 8192 + l15 * 256 + ((g ^ (l15 & 3)) << 4) + ((l15 >> 2) << 6), aa = K2_A + half * 8192 + l * 16;
    asm volatile("" : "+v"(tc), "+v"(ts), "+v"(dst), "+v"(uw), "+v"(ur0), "+v"(va), "+v"(aa));
    const int r = c == 4 ? 5 * half : c + 1, kd = r + 10 * (16 * w4 + l15);
    const bool mir = w4 >= 2;
    u32 wg[2], wg0[2];
#pragma unroll
    for (int k = 0; k < 2; ++k) {
      const bool mr = mir != (k == 1);
      const int qs = mr ? (64 - q) & 63 : q, col = k ? (mir ? 320 : 0) : (mir ? 640 - kd : kd);
      wg[k] = K2_W + (qs != qa) * 32 * WROW + ((2 * qs) & 15) + 2 * col + (mr ? ((q == 0 ? 32 : 31) - 19 - 4 * g) * WROW : 4 * g * WROW);
      wg0[k] = (mr && q == 0 && g == 0) ? wg[k] - 13 * WROW : wg[k] + (mr ? 19 * WROW : 0);
      PIN(wg[k]); PIN(wg0[k]);
    }
    asm volatile("s_waitcnt vmcnt(0)" : "+v"(tr[0]), "+v"(tr[1]), "+v"(tr[2]), "+v"(tr[3]) :: "memory");
#pragma unroll
    for (int ks = 0; ks < 4; ++ks)
#pragma unroll
      for (int i = 0; i < 4; ++i) ti[ks][i] = __builtin_amdgcn_alignbit(tr[ks][i], tr[ks][i], 16) ^ 0x8000u;
    asm volatile("" : "+v"(ti[0]), "+v"(ti[1]), "+v"(ti[2]), "+v"(ti[3]));
    STAMP();
    __builtin_amdgcn_s_waitcnt(0xC07F);
    if (l == 0) __hip_atomic_fetch_add(cnt, 1u, __ATOMIC_RELAXED, __HIP_MEMORY_SCOPE_WORKGROUP);
    while (__builtin_amdgcn_readfirstlane(__hip_atomic_load(cnt, __ATOMIC_RELAXED, __HIP_MEMORY_SCOPE_WORKGROUP)) < 8u) {}
    asm volatile("" ::: "memory");
    STAMP();
    u32x4 vf[2][4], cf[4], sf[4];
#pragma unroll
    for (int ks = 0; ks < 4; ++ks)
#pragma unroll
      for (int mt = 0; mt < 2; ++mt) vf[mt][ks] = ldsr128(smem, (va ^ (ks << 6)) + mt * 4096);
#pragma unroll
    for (int i = 0; i < 4; ++i) {
      cf[i] = ldsr128(smem, aa + i * 2048); sf[i] = ldsr128(smem, aa + i * 2048 + 1024);
    }
    SB();
    f32x4 Tr[2] = {z4, z4}, Ti[2] = {z4, z4};
#pragma unroll
    for (int ks = 0; ks < 4; ++ks)
#pragma unroll
      for (int mt = 0; mt < 2; ++mt) { Tr[mt] = mfma(vf[mt][ks], tr[ks], Tr[mt]); Ti[mt] = mfma(vf[mt][ks], ti[ks], Ti[mt]); }
    u32x4 br = pkfrag(Tr[0], Tr[1]), bi = pkfrag(Ti[0], Ti[1]), ni = bi ^ 0x80008000u;
    f32x4 zr[2], zn[2];
#pragma unroll
    for (int mt = 0; mt < 2; ++mt) { zr[mt] = mfma(cf[mt], br, z4); zn[mt] = mfma(cf[mt], bi, z4); }
#pragma unroll
    for (int mt = 0; mt < 2; ++mt) { zr[mt] = mfma(sf[mt], ni, zr[mt]); zn[mt] = mfma(sf[mt], br, zn[mt]); }
    SB();
    __builtin_amdgcn_s_barrier();
    asm volatile("" ::: "memory");
    STAMP();
    f32x4 wa[2], wb[2];
    {
      u32 ra[8], rb[8];
      auto gather = [&](int s, bool mr) {
#pragma unroll
        for (int k = 0; k < 8; ++k) {
          const int dk = 16 * (k >> 2) + (k & 3);
          const u32 o = k == 0 ? wg0[s] : wg[s] + (mr ? 19 - dk : dk) * WROW;
          ra[k] = ldsr16(smem, o); rb[k] = ldsr16(smem, o + 64 * WROW);
        }
      };
      if (mir) gather(0, true); else gather(0, false);
      prefetch();
      const u32 sg = mir ? 0x80000000u : 0u;
#pragma unroll
      for (int k = 0; k < 8; ++k) { wa[k >> 2][k & 3] = lo2f(ra[k]); wb[k >> 2][k & 3] = __uint_as_float((rb[k] << 16) ^ sg); }
      if (r == 0 && (w4 & 1) == 0) {
        if (mir) gather(1, false); else gather(1, true);
        const float al = l15 == 0 ? 0.5f : 1.f, be = l15 == 0 ? 0.5f : 0.f;
#pragma unroll
        for (int k = 0; k < 8; ++k) {
          wa[k >> 2][k & 3] = l15 == 0 ? 0.5f * (wa[k >> 2][k & 3] + lo2f(ra[k])) : wa[k >> 2][k & 3];
          wb[k >> 2][k & 3] = l15 == 0 ? 0.5f * (wb[k >> 2][k & 3] - __uint_as_float((rb[k] << 16) ^ sg)) : wb[k >> 2][k & 3];
        }
        (void)al; (void)be;
      }
    }
    f32x4 Yr[2], Yi[2];
#pragma unroll
    for (int mt = 0; mt < 2; ++mt) { Yr[mt] = zr[mt] * wa[mt]; Yi[mt] = zr[mt] * wb[mt]; }
    SB();
#pragma unroll
    for (int mt = 0; mt < 2; ++mt) { Yr[mt] += zn[mt] * wb[mt]; Yi[mt] -= zn[mt] * wa[mt]; }
    SB();
    br = pkfrag(Yr[0], Yr[1]); bi = pkfrag(Yi[0], Yi[1]); ni = bi ^ 0x80008000u;
    f32x4 ur[2], ui[2];
#pragma unroll
    for (int mt = 0; mt < 2; ++mt) { ur[mt] = mfma(cf[2 + mt], br, z4); ui[mt] = mfma(cf[2 + mt], bi, z4); }
#pragma unroll
    for (int mt = 0; mt < 2; ++mt) { ur[mt] = mfma(sf[2 + mt], ni, ur[mt]); ui[mt] = mfma(sf[2 + mt], br, ui[mt]); }
    SB();
#pragma unroll
    for (int mt = 0; mt < 2; ++mt)
#pragma unroll
      for (int i = 0; i < 4; ++i) *(LDS_AS u32*)((LDS_AS char*)smem + uw + (16 * mt + i) * K2_US) = pk2(ur[mt][i], ui[mt][i]);
    STAMP();
    RAWBAR();
    STAMP();
    u32x4 uf[2][4];
#pragma unroll
    for (int ks = 0; ks < 4; ++ks)
#pragma unroll
      for (int cl = 0; cl < 2; ++cl) uf[cl][ks] = ldsr128(smem, ur0 + cl * (32 * K2_US) + ks * 64);
    SB();
    f32x4 Rr[2] = {z4, z4}, Ri[2] = {z4, z4};
#pragma unroll
    for (int ks = 0; ks < 4; ++ks)
#pragma unroll
      for (int cl = 0; cl < 2; ++cl) { Rr[cl] = mfma(tr[ks], uf[cl][ks], Rr[cl]); Ri[cl] = mfma(ti[ks], uf[cl][ks], Ri[cl]); }
    f32x4 e00, e01, e10, e11;
    if (c < 4) {
      f32x4 ar = Rr[0] * sc, ai = Ri[0] * -sc;
      if (ci > 0) { e00 = ar + Rr[1] * sc; e01 = ai - Ri[1] * sc; e10 = ai + Ri[1] * sc; e11 = Rr[1] * sc - ar; }
      else { e00 = ar; e01 = ai; e10 = Rr[1] * sc; e11 = Ri[1] * -sc; }
    } else {
      e00 = Rr[0] * s2; e01 = Rr[1] * tc - Ri[1] * ts;
      if (ci & 31) { e10 = Ri[0] * -s2; e11 = -(Rr[1] * ts + Ri[1] * tc); }
      else { e10 = e00; e11 = e01; }
    }
    if (sidx != 160) *(u32x4*)dst = pkfrag(e00, e01);
    if (sidx != 128) *(u32x4*)(dst + 16) = pkfrag(e10, e11);
    STAMP();
  } else {
    if (w8 >= 8) return;
    const int ob = b - K2_NF, hp = ob & 15, n2 = 8 * (ob >> 4) + w8;
    u32x4 s[8];
    const int rh = tid >= 256, rp = tid & 255;
    if (rp < 210) {
      const char* kb = ws + OFF_KVP + hp * 3360;
      const u32 ko = rp * 16 + rh * (8 * 53760);
#pragma unroll
      for (int mm = 0; mm < 8; ++mm) asm volatile("global_load_dwordx4 %0, %1, %2" : "=v"(s[mm]) : "v"(ko), "s"(kb + mm * 53760) : "memory");
    }
    const char* src = ws + OFF_PQ + ((size_t)hp * 2048 + 64 * n2) * 80;
    const u32 pl = 8192 + w8 * 5136;
    dma4(LDSADDR(smem) + pl, src, l * 16, l * 16, l * 16, l * 16);
    dma1(LDSADDR(smem) + pl + 4096, src + 4096, l * 16);
    const int gc = g > 2 ? 2 : g;
    const u32 m0 = g < 3 ? ~0u : 0u, m1 = g < 2 ? ~0u : 0u;
    u32 oo = (n2 * 16 + hp) * 5120 + l * 16;
    u32 a0 = l15 * 48 + gc * 16, a1 = (16 + (l15 & 3)) * 48 + gc * 16, a2 = 960 + gc * 16;
    u32 pr = pl + l15 * 80 + 16 * gc, pw = pl + l15 * 80 + 8 * g, pw2 = pl + l15 * 80 + 32 + 4 * (g & 1), pc = pl + l * 16;
    const int u_ = rp >= 105, in = rp - 105 * u_, e_ = (in * 205) >> 10;
    u32 kw = u_ * 1024 + e_ * 48 + (in - 5 * e_) * 8;
    PIN(oo); PIN(a0); PIN(a1); PIN(a2); PIN(pr); PIN(pw); PIN(pw2); PIN(pc); PIN(kw);
    asm volatile("s_waitcnt vmcnt(5)" : "+v"(s[0]), "+v"(s[1]), "+v"(s[2]), "+v"(s[3]), "+v"(s[4]), "+v"(s[5]), "+v"(s[6]), "+v"(s[7]) :: "memory");
    f32x4 sum = ((__builtin_bit_cast(f32x4, s[0]) + __builtin_bit_cast(f32x4, s[1])) + (__builtin_bit_cast(f32x4, s[2]) + __builtin_bit_cast(f32x4, s[3])))
              + ((__builtin_bit_cast(f32x4, s[4]) + __builtin_bit_cast(f32x4, s[5])) + (__builtin_bit_cast(f32x4, s[6]) + __builtin_bit_cast(f32x4, s[7])));
    if (rh) *(LDS_AS f32x4*)((LDS_AS char*)smem + 2048 + rp * 16) = sum;
    RAWBAR();
    if (!rh && rp < 210) {
      sum += *(const LDS_AS f32x4*)((const LDS_AS char*)smem + 2048 + rp * 16);
      ldsw64(smem, kw, u32x2{pk2(sum[0], sum[1]), pk2(sum[2], sum[3])});
    }
    STAMP();
    WAITV(0);
    prefetch();
    u32x4 af[2][3], pf[4][2];
#pragma unroll
    for (int rt = 0; rt < 4; ++rt)
#pragma unroll
      for (int u = 0; u < 2; ++u) {
        u32x2 a = ldsr64(smem, pr + rt * 1280 + 40 * u), bq = ldsr64(smem, pr + rt * 1280 + 40 * u + 8);
        pf[rt][u] = u32x4{a[0], a[1], bq[0], bq[1]};
      }
    RAWBAR();
    STAMP();
#pragma unroll
    for (int u = 0; u < 2; ++u) { af[u][2] = ldsr128(smem, a2 + u * 1024); af[u][0] = ldsr128(smem, a0 + u * 1024); af[u][1] = ldsr128(smem, a1 + u * 1024); }
    SB();
    pf[3][1] &= u32x4{~0u, ~0u, m1, m1};
#pragma unroll
    for (int u = 0; u < 2; ++u)
#pragma unroll
      for (int tI = 0; tI < 3; ++tI) af[u][tI] &= u32x4{m0, m0, m1, m1};
    f32x4 n0[4][2], n1[4][2], dn[4][2];
#pragma unroll
    for (int u = 0; u < 2; ++u)
#pragma unroll
      for (int rt = 0; rt < 4; ++rt) dn[rt][u] = mfma(af[u][2], pf[rt][u], z4);
#pragma unroll
    for (int u = 0; u < 2; ++u)
#pragma unroll
      for (int rt = 0; rt < 4; ++rt) { n0[rt][u] = mfma(af[u][0], pf[rt][u], z4); n1[rt][u] = mfma(af[u][1], pf[rt][u], z4); }
    SB();
    float rd[8]; u32 b0[8], b1[8], b2[8];
#pragma unroll
    for (int k = 0; k < 8; ++k) rd[k] = bfr(dn[k & 3][k >> 2][0]) + 1e-6f;
    SB();
#pragma unroll
    for (int k = 0; k < 8; ++k) rd[k] = __builtin_amdgcn_rcpf(bfr(rd[k]));
    SB();
#pragma unroll
    for (int k = 0; k < 8; ++k) {
      const f32x4 a = n0[k & 3][k >> 2], c2 = n1[k & 3][k >> 2];
      b0[k] = pk2(a[0], a[1]); b1[k] = pk2(a[2], a[3]); b2[k] = (g & 1) ? pk2(c2[2], c2[3]) : pk2(c2[0], c2[1]);
    }
    SB();
#pragma unroll
    for (int k = 0; k < 8; ++k) {
      f32x2 x0 = f32x2{lo2f(b0[k]), hi2f(b0[k])} * rd[k], x1 = f32x2{lo2f(b1[k]), hi2f(b1[k])} * rd[k], x2 = f32x2{lo2f(b2[k]), hi2f(b2[k])} * rd[k];
      b0[k] = pk2(x0[0], x0[1]); b1[k] = pk2(x1[0], x1[1]); b2[k] = pk2(x2[0], x2[1]);
    }
#pragma unroll
    for (int k = 0; k < 8; ++k) {
      ldsw64(smem, pw + (k & 3) * 1280 + 40 * (k >> 2), u32x2{b0[k], b1[k]});
      *(LDS_AS u32*)((LDS_AS char*)smem + pw2 + (k & 3) * 1280 + 40 * (k >> 2)) = b2[k];
    }
    u32x4 o[5];
#pragma unroll
    for (int i = 0; i < 5; ++i) o[i] = ldsr128(smem, pc + i * 1024);
    ST16SO(ws + OFF_O2, oo, 0, o[0]); ST16SO(ws + OFF_O2, oo, 1024, o[1]); ST16SO(ws + OFF_O2, oo, 2048, o[2]); ST16SO(ws + OFF_O2, oo, 3072, o[3]);
    ST16SO(ws + OFF_O2 + 4096, oo, 0, o[4]);
    STAMP();
  }
  STAMP_FLUSH(1);
}

constexpr int K3_W = 81920, K3_AP = 61440, K3_GS = K3_AP + 10240, K3_LDS = K3_W + 3 * 25600;

__global__ __launch_bounds__(1024) __attribute__((amdgpu_waves_per_eu(4, 4)))
void k3(const char* __restrict__ ws, u16* __restrict__ out) {
  extern __shared__ __attribute__((aligned(16))) char smem[];
  const int tid = threadIdx.x, l = tid & 63, w = __builtin_amdgcn_readfirstlane(tid >> 6);
  const int g = l >> 4, l15 = l & 15;
  const int b = blockIdx.x, n2 = 4 * (b & 7) + ((b >> 3) & 3), bg = b >> 5;
  const f32x4 z4 = {0.f, 0.f, 0.f, 0.f};
  asm volatile("" :: "s"(ws), "s"(out));
  STAMP_DECL; STAMP();
  if (w >= 10) {
    const int v = w - 10, rot = (bg + (b >> 3)) & 3;
    const u32 lo = l * 16, L = LDSADDR(smem);
    __builtin_amdgcn_s_setprio(2);
    {
      const char* ap = ws + OFF_AP + (size_t)(n2 * 8 + bg) * AP_TS;
      if (v < 5) dma2(L + K3_AP + v * 2048, ap + v * 2048, lo, lo);
      if (v < 5) dma1(L + K3_GS + v * 1024, (const char*)dGsB.v + v * 1024, lo);
      else { dma2(L + K3_GS + 5120, (const char*)dGsB.v + 5120, lo, lo); dma1(L + K3_GS + 7168, (const char*)dGsB.v + 7168, lo); }
    }
    const char* op = ws + OFF_O2 + (size_t)n2 * 81920 + v * 4096;
    const char* wp = ws + OFF_WOF + (size_t)bg * 102400;
    auto issue = [&](int Q) {
      const int Qd = (Q + rot) & 3;
      const u32 lw = L + K3_W + (Q == 3 ? 0 : Q) * 25600;
      if (v < 5) {
        dma4(L + Q * 20480 + v * 4096, op + Qd * 20480, lo, lo, lo, lo);
        dma4(lw + v * 5120, wp + v * 20480 + Qd * 5120, lo, lo, lo, lo);
      } else {
#pragma unroll
        for (int c = 0; c < 5; ++c) dma1(lw + c * 5120 + 4096, wp + c * 20480 + Qd * 5120 + 4096, lo);
      }
    };
#define K3W() do { if (v < 5) WAITV(8); else WAITV(5); __builtin_amdgcn_s_barrier(); } while (0)
    issue(0);
    K3W();
    issue(1);
    K3W();
    issue(2);
    K3W();
    issue(3);
    K3W();
    WAITV(0); __builtin_amdgcn_s_barrier();
  } else {
    const int ga = w < 5, ct = ga ? w : w - 5;
    const u32 oa = g * 5120 + l15 * 80, wa = K3_W + ct * 5120 + l * 16;
    f32x4 acc[4] = {z4, z4, z4, z4};
    auto quarter = [&](int Q) {
      u32x4 of[5][4], wf[5];
#pragma unroll
      for (int wv = 0; wv < 5; ++wv) {
        wf[wv] = ldsr128(smem, wa + (Q == 3 ? 0 : Q) * 25600 + wv * 1024);
        if (wv < 3) {
#pragma unroll
          for (int rt = 0; rt < 4; ++rt) of[wv][rt] = ldsr128(smem, oa + Q * 20480 + rt * 1280 + wv * 16);
        }
      }
      SB();
#pragma unroll
      for (int wv = 0; wv < 2; ++wv)
#pragma unroll
        for (int rt = 0; rt < 4; ++rt) {
          acc[rt] = mfma(wf[wv], of[wv][rt], acc[rt]);
          SB();
          of[3 + wv][rt] = ldsr128(smem, oa + Q * 20480 + rt * 1280 + (3 + wv) * 16);
          SB();
        }
#pragma unroll
      for (int wv = 2; wv < 5; ++wv)
#pragma unroll
        for (int rt = 0; rt < 4; ++rt) acc[rt] = mfma(wf[wv], of[wv][rt], acc[rt]);
      SB();
      if (Q == 2) __builtin_amdgcn_s_barrier();
    };
    if (!ga) {
      __builtin_amdgcn_s_barrier();
      __builtin_amdgcn_s_barrier();
      STAMP();
      quarter(0);
      RAWBAR();
      __builtin_amdgcn_s_barrier();
      STAMP();
      quarter(2);
#pragma unroll
      for (int rt = 0; rt < 4; ++rt) *(LDS_AS f32x4*)((LDS_AS char*)smem + (ct * 4 + rt) * 1024 + l * 16) = acc[rt];
      __builtin_amdgcn_s_waitcnt(0xC07F);
      STAMP();
    } else {
      u32 gv[4];
      const char* gp = ws + OFF_G + (64 * n2 + l15) * 4;
      GLD1(gv[0], gp, 0); GLD1(gv[1], gp, 64); GLD1(gv[2], gp, 128); GLD1(gv[3], gp, 192);
      u32x4 kd[3];
      const int a = 2 * ct + (l15 >> 3), j = l15 & 7, d = 64 * a + 8 * bg + j;
#pragma unroll
      for (int s = 0; s < 3; ++s) {
        int kc = 4 * s + g, c = kc >> 1;
        float rev = c < 4 ? (float)((c + 1) * d) * (1.f / 640.f) : 0.f;
        float v0 = __builtin_amdgcn_cosf(rev), v1 = c < 4 ? __builtin_amdgcn_sinf(rev) : ((a & 1) ? -1.f : 1.f);
        bool on = kc < 10 && (kc & 1) == (j >> 2);
        u32 pp = on ? pk2(v0, v1) : 0u;
        u32 p0 = (pp & 0xffffu) << ((j & 1) * 16), p1 = (pp >> 16) << ((j & 1) * 16);
        kd[s] = (j & 2) ? u32x4{0u, p0, 0u, p1} : u32x4{p0, 0u, p1, 0u};
        PIN(kd[s]);
      }
      u32 ao = K3_AP + ((l15 >> 1) & 1) * 256 + (l15 >> 2) * 64 + (l15 & 1) * 16 + (g & 1) * 32, ao1 = ao + (g >> 1) * 2048;
      u32 oo = ((32 * l15 + n2) * 640 + 64 * (2 * ct + (g >> 1)) + 8 * bg + 4 * (g & 1)) * 2;
      PIN(ao); PIN(ao1); PIN(oo);
      STAMP();
      __builtin_amdgcn_s_barrier();
      asm volatile("" ::: "memory");
      u32x4 af[4][3], gs[8];
#pragma unroll
      for (int rt = 0; rt < 4; ++rt) { af[rt][0] = ldsr128(smem, ao1 + rt * 512); af[rt][1] = ldsr128(smem, ao1 + 4096 + rt * 512); af[rt][2] = ldsr128(smem, ao + 8192 + rt * 512); }
#pragma unroll
      for (int f = 0; f < 8; ++f) gs[f] = ldsr128(smem, K3_GS + f * 1024 + l * 16);
      SB();
      f32x4 P[4];
#pragma unroll
      for (int s = 0; s < 3; ++s)
#pragma unroll
        for (int rt = 0; rt < 4; ++rt) P[rt] = mfma(af[rt][s], kd[s], s ? P[rt] : z4);
      u32x4 p0 = pkfrag(P[0], P[1]), p1 = pkfrag(P[2], P[3]);
      SB();
      RAWBAR();
      f32x4 yb[4];
#pragma unroll
      for (int nt = 0; nt < 4; ++nt) yb[nt] = mfma(p0, gs[2 * nt], z4);
#pragma unroll
      for (int nt = 0; nt < 4; ++nt) yb[nt] = mfma(p1, gs[2 * nt + 1], yb[nt]);
      asm volatile("s_waitcnt vmcnt(0)" : "+v"(gv[0]), "+v"(gv[1]), "+v"(gv[2]), "+v"(gv[3]) :: "memory");
      u32 yp[4][2];
#pragma unroll
      for (int nt = 0; nt < 4; ++nt) {
        const float hh = bfr(1.f - __uint_as_float(gv[nt]));
        u32 b0 = pk2(yb[nt][0], yb[nt][1]), b1 = pk2(yb[nt][2], yb[nt][3]);
        yp[nt][0] = pk2(hh * lo2f(b0), hh * hi2f(b0)); yp[nt][1] = pk2(hh * lo2f(b1), hh * hi2f(b1));
        PIN(yp[nt][0]); PIN(yp[nt][1]);
      }
      STAMP();
      __builtin_amdgcn_s_barrier();
      asm volatile("" ::: "memory");
      STAMP();
      quarter(1);
      __builtin_amdgcn_s_barrier();
      __builtin_amdgcn_s_barrier();
      asm volatile("" ::: "memory");
      STAMP();
      quarter(3);
      __builtin_amdgcn_s_barrier();
      asm volatile("" ::: "memory");
      f32x4 pb[4];
#pragma unroll
      for (int rt = 0; rt < 4; ++rt) pb[rt] = *(const LDS_AS f32x4*)((const LDS_AS char*)smem + (ct * 4 + rt) * 1024 + l * 16);
      STAMP();
      u32 a0[4], a1[4];
#pragma unroll
      for (int rt = 0; rt < 4; ++rt) { f32x4 t = acc[rt] + pb[rt]; a0[rt] = pk2(t[0], t[1]); a1[rt] = pk2(t[2], t[3]); }
      SB();
#pragma unroll
      for (int rt = 0; rt < 4; ++rt) {
        const float gg = __uint_as_float(gv[rt]);
        a0[rt] = pk2(gg * lo2f(a0[rt]), gg * hi2f(a0[rt])); a1[rt] = pk2(gg * lo2f(a1[rt]), gg * hi2f(a1[rt]));
      }
      SB();
#pragma unroll
      for (int rt = 0; rt < 4; ++rt) {
        u32x2 o = {pk2(lo2f(a0[rt]) + lo2f(yp[rt][0]), hi2f(a0[rt]) + hi2f(yp[rt][0])), pk2(lo2f(a1[rt]) + lo2f(yp[rt][1]), hi2f(a1[rt]) + hi2f(yp[rt][1]))};
        *(u32x2*)((char*)out + (size_t)(oo + rt * (512 * 1280))) = o;
      }
      STAMP();
    }
  }
  STAMP_FLUSH(2);
}

__global__ void kend() {}

extern "C" void kernel_launch(void* const* d_in, const int* in_sizes, int n_in, void* d_out, int out_size, void* d_ws,
                              size_t ws_size, hipStream_t stream) {
  const u16* x = (const u16*)d_in[0];
  const u16* wqkv = (const u16*)d_in[1];
  const u16* wo = (const u16*)d_in[2];
  const u16* wr = (const u16*)d_in[4];
  const u16* wi = (const u16*)d_in[5];
  const u16* gate = (const u16*)d_in[6];
  char* ws = (char*)d_ws;
  (void)hipFuncSetAttribute((const void*)k1, hipFuncAttributeMaxDynamicSharedMemorySize, K1_LDS);
  (void)hipFuncSetAttribute((const void*)k2, hipFuncAttributeMaxDynamicSharedMemorySize, K2_LDS);
  (void)hipFuncSetAttribute((const void*)k3, hipFuncAttributeMaxDynamicSharedMemorySize, K3_LDS);
  k1<<<256, 1024, K1_LDS, stream>>>(x, wqkv, wr, wi, gate, wo, ws);
  k2<<<K2_NF + K2_NO, 1024, K2_LDS, stream>>>(wr, wi, ws);
  k3<<<256, 1024, K3_LDS, stream>>>(ws, (u16*)d_out);
}
```

```cpp
#include <hip/hip_runtime.h>
#include <stdint.h>

typedef unsigned short u16;
typedef unsigned int u32;
typedef short s16x4 __attribute__((ext_vector_type(4)));
typedef short s16x8 __attribute__((ext_vector_type(8)));
typedef __bf16 bf16x8 __attribute__((ext_vector_type(8)));
typedef __bf16 bf16x2 __attribute__((ext_vector_type(2)));
typedef float f32x4 __attribute__((ext_vector_type(4)));
typedef float f32x2 __attribute__((ext_vector_type(2)));
typedef u32 u32x4 __attribute__((ext_vector_type(4)));
typedef u32 u32x2 __attribute__((ext_vector_type(2)));

#define LDS_AS __attribute__((address_space(3)))
#define GLB_AS __attribute__((address_space(1)))
#define DI __device__ __forceinline__
#define STAMP_DECL
#define STAMP()
#define STAMP_FLUSH(K)
__device__ __forceinline__ void dma1(u32 lds, const void* base, u32 v0) {
  u32 keep;
  asm volatile("s_mov_b32 %0, m0\n\ts_mov_b32 m0, %1\n\ts_nop 4\n\tglobal_load_lds_dwordx4 %2, %3\n\ts_mov_b32 m0, %0"
               : "=&s"(keep) : "s"(lds), "v"(v0), "s"(base) : "memory");
}
__device__ __forceinline__ void dma4(u32 lds, const void* base, u32 v0, u32 v1, u32 v2, u32 v3) {
  u32 keep;
  asm volatile("s_mov_b32 %0, m0\n\ts_mov_b32 m0, %1\n\ts_nop 4\n\tglobal_load_lds_dwordx4 %2, %6\n\t"
               "global_load_lds_dwordx4 %3, %6 offset:1024\n\tglobal_load_lds_dwordx4 %4, %6 offset:2048\n\t"
               "global_load_lds_dwordx4 %5, %6 offset:3072\n\ts_mov_b32 m0, %0"
               : "=&s"(keep) : "s"(lds), "v"(v0), "v"(v1), "v"(v2), "v"(v3), "s"(base) : "memory");
}
__device__ __forceinline__ void dma2(u32 lds, const void* base, u32 v0, u32 v1) {
  u32 keep;
  asm volatile("s_mov_b32 %0, m0\n\ts_mov_b32 m0, %1\n\ts_nop 4\n\tglobal_load_lds_dwordx4 %2, %4\n\t"
               "global_load_lds_dwordx4 %3, %4 offset:1024\n\ts_mov_b32 m0, %0"
               : "=&s"(keep) : "s"(lds), "v"(v0), "v"(v1), "s"(base) : "memory");
}
__device__ __forceinline__ void dma1v(u32 lds, const void* src) {
  u32 keep;
  asm volatile("s_mov_b32 %0, m0\n\ts_mov_b32 m0, %1\n\ts_nop 0\n\tglobal_load_lds_dwordx4 %2, off\n\ts_mov_b32 m0, %0"
               : "=&s"(keep) : "s"(lds), "v"(src) : "memory");
}
#define GLD(dst, p, imm) asm volatile("global_load_dwordx4 %0, %1, off offset:" #imm : "=v"(dst) : "v"(p) : "memory")
#define GLD1(dst, p, imm) asm volatile("global_load_dword %0, %1, off offset:" #imm : "=v"(dst) : "v"(p) : "memory")
#define GLDH(dst, p) asm volatile("global_load_ushort %0, %1, off" : "=v"(dst) : "v"(p) : "memory")
#define LDSADDR(p) ((u32)(uintptr_t)(LDS_AS char*)(p))
#define SB() __builtin_amdgcn_sched_barrier(0)
#define WAITV(n) asm volatile("s_waitcnt vmcnt(" #n ")" ::: "memory")
#define RAWBAR() do { asm volatile("" ::: "memory"); __builtin_amdgcn_s_waitcnt(0xC07F); __builtin_amdgcn_s_barrier(); asm volatile("" ::: "memory"); } while (0)

constexpr size_t OFF_PQ  = 0;
constexpr size_t OFF_O2  = OFF_PQ + 2621440;
constexpr size_t OFF_KVP = OFF_O2 + 2621440;
constexpr size_t OFF_VB  = OFF_KVP + 860160;
constexpr int    VB_TS   = 10368;
constexpr size_t OFF_AP  = OFF_VB + (size_t)256 * VB_TS;
constexpr int    AP_TS   = 10240;
constexpr size_t OFF_WOF = OFF_AP + (size_t)256 * AP_TS;
constexpr size_t OFF_G   = OFF_WOF + 819200;

constexpr double kPi = 3.14159265358979323846;
constexpr double tcos(double x) {
  double x2 = x * x, term = 1.0, s = 1.0;
  for (int i = 1; i < 14; ++i) { term *= -x2 / ((2 * i - 1) * (2 * i)); s += term; }
  return s;
}
constexpr double cos64(int k) {
  k &= 63; if (k > 32) k = 64 - k;
  if (k == 16) return 0.0;
  return (k < 16) ? tcos(2 * kPi * k / 64) : -tcos(2 * kPi * (32 - k) / 64);
}
constexpr u16 tobf(double v) {
  u32 u = __builtin_bit_cast(u32, (float)v);
  return (u16)((u + 0x7fffu + ((u >> 16) & 1u)) >> 16);
}
struct C64 { u16 c[64]; };
constexpr C64 mkC64() { C64 t{}; for (int i = 0; i < 64; ++i) t.c[i] = tobf(cos64(i)); return t; }
constexpr C64 kC64 = mkC64();
constexpr u16 gsv(int qp, int n1) {
  int i = qp >> 1;
  if (qp == 1) return kC64.c[(32 * n1) & 63];
  return (qp & 1) ? kC64.c[(i * n1 + 48) & 63] : kC64.c[(i * n1) & 63];
}
constexpr int kperm(int s2, int g, int j) { return 32 * s2 + (j < 4 ? 4 * g + j : 12 + 4 * g + j); }
struct T16 { u16 v[4096]; };
struct T32 { u32 v[4096]; };
constexpr T16 mkGsA() { T16 t{};
  for (int f = 0; f < 8; ++f) for (int l = 0; l < 64; ++l) for (int j = 0; j < 8; ++j)
    t.v[(f * 64 + l) * 8 + j] = gsv(16 * (f >> 1) + (l & 15), kperm(f & 1, l >> 4, j));
  return t; }
constexpr T16 mkGsB() { T16 t{};
  for (int f = 0; f < 8; ++f) for (int l = 0; l < 64; ++l) for (int j = 0; j < 8; ++j)
    t.v[(f * 64 + l) * 8 + j] = gsv(kperm(f & 1, l >> 4, j), 16 * (f >> 1) + (l & 15));
  return t; }
constexpr T32 mkF64() { T32 t{};
  for (int f = 0; f < 16; ++f) for (int l = 0; l < 64; ++l) for (int j = 0; j < 4; ++j) {
    int u = 16 * (f >> 2) + (l & 15), v = 4 * (4 * (f & 3) + (l >> 4)) + j;
    t.v[(f * 64 + l) * 4 + j] = (u32)kC64.c[(u * v) & 63] | ((u32)kC64.c[(u * v + 16) & 63] << 16);
  }
  return t; }
__device__ __attribute__((aligned(128))) const T16 dGsA = mkGsA();
__device__ __attribute__((aligned(128))) const T16 dGsB = mkGsB();
__device__ __attribute__((aligned(128))) const T32 dF64 = mkF64();

DI f32x4 mfma(u32x4 a, u32x4 b, f32x4 c) {
  return __builtin_amdgcn_mfma_f32_16x16x32_bf16(__builtin_bit_cast(bf16x8, a), __builtin_bit_cast(bf16x8, b), c, 0, 0, 0);
}
DI u32 pk2(float lo, float hi) { f32x2 v = {lo, hi}; return __builtin_bit_cast(u32, __builtin_convertvector(v, bf16x2)); }
DI float lo2f(u32 p) { return __uint_as_float(p << 16); }
DI float hi2f(u32 p) { return __uint_as_float(p & 0xffff0000u); }
DI float bfr(float x) { return lo2f(pk2(x, x)); }
DI u32x4 pkfrag(f32x4 a, f32x4 b) { u32x4 r = {pk2(a[0], a[1]), pk2(a[2], a[3]), pk2(b[0], b[1]), pk2(b[2], b[3])}; return r; }
DI float ror8(float v) { return __int_as_float(__builtin_amdgcn_update_dpp(0, __float_as_int(v), 0x128, 0xf, 0xf, true)); }
DI u32x4 ldsr128(const char* smem, u32 off) { return *(const LDS_AS u32x4*)((const LDS_AS char*)smem + off); }
DI u32x2 ldstr(const char* smem, u32 off) {
  return __builtin_bit_cast(u32x2, __builtin_amdgcn_ds_read_tr16_b64_v4i16((LDS_AS s16x4*)((LDS_AS char*)smem + off)));
}
DI void st16wt(void* p, u32x4 v) { asm volatile("global_store_dwordx4 %0, %1, off sc1\n\ts_nop 1" :: "v"(p), "v"(v) : "memory"); }
DI void st4wt(void* p, u32 v) { asm volatile("global_store_dword %0, %1, off sc1" :: "v"(p), "v"(v) : "memory"); }
DI void l2touch(u32 lds, const void* p) {
  u32 keep;
  asm volatile("s_mov_b32 %0, m0\n\ts_mov_b32 m0, %1\n\ts_nop 0\n\tglobal_load_lds_dword %2, off\n\ts_mov_b32 m0, %0"
               : "=&s"(keep) : "s"(lds), "v"(p) : "memory");
}
DI void st16p(char* b, u32 o, f32x4 v) { *(f32x4*)(b + (size_t)o) = v; }
DI void st16s(const void* b, u32 o, u32x4 v) { asm volatile("global_store_dwordx4 %0, %1, %2 sc1\n\ts_nop 1" :: "v"(o), "v"(v), "s"(b) : "memory"); }
#define ST16SO(b, o, imm, v) asm volatile("global_store_dwordx4 %0, %1, %2 offset:" #imm " sc1\n\ts_nop 1" :: "v"(o), "v"(v), "s"(b) : "memory")
#define ST4SO(b, o, imm, v) asm volatile("global_store_dword %0, %1, %2 offset:%3 sc1" :: "v"(o), "v"(v), "s"(b), "i"(imm) : "memory")
#define ST4O(p, imm, v) asm volatile("global_store_dword %0, %1, off offset:%2 sc1" :: "v"(p), "v"(v), "i"(imm) : "memory")
DI void ldsw128(char* smem, u32 off, u32x4 v) { *(LDS_AS u32x4*)((LDS_AS char*)smem + off) = v; }
DI void ldsw64(char* smem, u32 off, u32x2 v) { *(LDS_AS u32x2*)((LDS_AS char*)smem + off) = v; }
DI u32x2 ldsr64(const char* smem, u32 off) { return *(const LDS_AS u32x2*)((const LDS_AS char*)smem + off); }
DI u32 ldsr16(const char* smem, u32 off) { return *(const LDS_AS u16*)((const LDS_AS char*)smem + off); }
template <int N> DI void phi4(u32x4 (&p)[N]) {
  f32x2 t[4 * N], e[4 * N];
#pragma unroll
  for (int i = 0; i < 4 * N; ++i) t[i] = f32x2{lo2f(p[i >> 2][i & 3]), hi2f(p[i >> 2][i & 3])};
  SB();
#pragma unroll
  for (int i = 0; i < 4 * N; ++i) e[i] = t[i] * 1.44269504088896f;
  SB();
#pragma unroll
  for (int i = 0; i < 4 * N; ++i) {
    e[i][0] = __builtin_amdgcn_fmed3f(__builtin_amdgcn_exp2f(e[i][0]), 0.f, 1.f); e[i][1] = __builtin_amdgcn_fmed3f(__builtin_amdgcn_exp2f(e[i][1]), 0.f, 1.f);
    t[i][0] = __builtin_fmaxf(t[i][0], 0.f); t[i][1] = __builtin_fmaxf(t[i][1], 0.f);
  }
  SB();
  float mn = 1.f;
#pragma unroll
  for (int i = 0; i < 4 * N; ++i) { mn = __builtin_fminf(__builtin_fminf(mn, e[i][0]), e[i][1]); e[i] += t[i]; }
  SB();
#pragma unroll
  for (int i = 0; i < 4 * N; ++i) p[i >> 2][i & 3] = pk2(e[i][0], e[i][1]);
  if (__builtin_amdgcn_ballot_w64(mn < 0.001f)) {
#pragma unroll
    for (int n = 0; n < N; ++n)
#pragma unroll
      for (int i = 0; i < 4; ++i) p[n][i] = pk2(lo2f(p[n][i]) + 1e-6f, hi2f(p[n][i]) + 1e-6f);
  }
}
template <int CTRL> DI float dppf(float v) { return __int_as_float(__builtin_amdgcn_update_dpp(0, __float_as_int(v), CTRL, 0xf, 0xf, true)); }
DI u32x4 onehot(u32 s, int d) { u32x4 r = {d == 0 ? s : 0u, d == 1 ? s : 0u, d == 2 ? s : 0u, d == 3 ? s : 0u}; return r; }

constexpr int K1_SLOT = 16384, K1_B = 65536, K1_AUX = 131072, K1_GS = K1_AUX + 272, K1_LDS = K1_GS + 8192;

struct Frags { u32x4 x[2][2], w[2][4]; };
#define PIN(v) asm volatile("" : "+v"(v))

template <bool QT>
DI void k1_gemm(char* smem, const u16* x, const u16* gate, char* ws, int m, int nt, int w, int l
                ) {
  const int g = l >> 4, l15 = l & 15, rg = w & 3, chh = w >> 2;
  {
    const int r0 = 8 * nt + w, lc = l < 40 ? l : 39;
    const u32x4* gx = (const u32x4*)(x + (32 * (r0 & 63) + 2 * m + (r0 >> 6)) * 640) + lc;
    const u32x4 gxv[2] = {gx[0], gx[40]}, gwv[2] = {((const u32x4*)gate)[lc], ((const u32x4*)gate)[lc + 40]};
    float g0 = 0.f, g1 = 0.f;
#pragma unroll
    for (int i = 0; i < 8; ++i) {
      const u32 xv = gxv[i >> 2][i & 3], wv = gwv[i >> 2][i & 3];
      u32 pr = pk2(lo2f(xv) * lo2f(wv), hi2f(xv) * hi2f(wv));
      g0 += lo2f(pr); g1 += hi2f(pr);
    }
    g0 = l < 40 ? g0 + g1 : 0.f;
    g0 += dppf<0xB1>(g0); g0 += dppf<0x4E>(g0); g0 += dppf<0x124>(g0); g0 += dppf<0x128>(g0);
    float tot = __uint_as_float(__builtin_amdgcn_readlane(__float_as_uint(g0), 0)) + __uint_as_float(__builtin_amdgcn_readlane(__float_as_uint(g0), 16))
              + __uint_as_float(__builtin_amdgcn_readlane(__float_as_uint(g0), 32)) + __uint_as_float(__builtin_amdgcn_readlane(__float_as_uint(g0), 48));
    float gt = bfr(__builtin_amdgcn_rcpf(1.f + __builtin_amdgcn_exp2f(bfr(tot) * -1.44269504088896f)));
    if (l == 0) ((float*)(ws + OFF_G))[128 * m + r0] = gt;
  }
  u32 xa[2], wa[4][2];
  xa[0] = rg * 4096 + l15 * 128 + ((g ^ ((l15 >> 1) & 7)) << 4); xa[1] = xa[0] ^ 64;
#pragma unroll
  for (int t2 = 0; t2 < 2; ++t2) {
    const int q4 = l15 >> 2, swz = (q4 << 2) | ((2 * g + t2) & 3);
    const u32 b0 = K1_B + (8 * g + 4 * t2 + q4) * 256 + (((((l >> 1) & 1) | ((QT ? 8 : 4) * chh)) ^ swz) << 4) + 8 * (l & 1);
#pragma unroll
    for (int i = 0; i < 4; ++i) { wa[i][t2] = b0 ^ ((QT ? i : (i & 1) + 4 * (i >> 1)) << 5); PIN(wa[i][t2]); }
  }
  f32x4 acc[2][4];
#pragma unroll
  for (int rt = 0; rt < 2; ++rt)
#pragma unroll
    for (int i = 0; i < 4; ++i) acc[rt][i] = f32x4{0.f, 0.f, 0.f, 0.f};
  Frags F[2];
  auto rd = [&](Frags& f, int C) {
    const int so = (C & 3) * K1_SLOT;
#pragma unroll
    for (int ks = 0; ks < 2; ++ks) {
#pragma unroll
      for (int rt = 0; rt < 2; ++rt) f.x[ks][rt] = ldsr128(smem, xa[ks] + so + rt * 2048);
#pragma unroll
      for (int i = 0; i < 4; ++i) {
        u32x2 a = ldstr(smem, wa[i][0] + so + ks * 8192), b = ldstr(smem, wa[i][1] + so + ks * 8192);
        f.w[ks][i] = u32x4{a[0], a[1], b[0], b[1]};
      }
    }
  };
  auto mm = [&](const Frags& f) {
#pragma unroll
    for (int ks = 0; ks < 2; ++ks)
#pragma unroll
      for (int i = 0; i < 4; ++i)
#pragma unroll
        for (int rt = 0; rt < 2; ++rt)
          acc[rt][i] = QT ? mfma(f.w[ks][i], f.x[ks][rt], acc[rt][i]) : mfma(f.x[ks][rt], f.w[ks][i], acc[rt][i]);
  };
  STAMP();
  RAWBAR();
  STAMP();
  rd(F[0], 0);
  SB();
  u32 eo[2], ra[2], rb[2];
  if (QT) {
    const int ck = 16 * (nt - 11) + 8 * chh + (l & 7), hp = (ck * 205) >> 10;
    eo[0] = (hp * 2048 + 128 * m + 32 * rg + (l >> 3)) * 80 + (ck - 5 * hp) * 16;
    ra[0] = 2 * K1_SLOT + chh * K1_B + rg * 4608 + l15 * 144 + g * 8;
    rb[0] = 2 * K1_SLOT + chh * K1_B + rg * 4608 + (l >> 3) * 144 + (l & 7) * 16;
    PIN(eo[0]); PIN(ra[0]); PIN(rb[0]);
  } else {
#pragma unroll
    for (int k = 0; k < 2; ++k) {
      int P = w + 8 * k;
      int pi = (0x103322211100ull >> (4 * P)) & 15, pj = (0x333232121010ull >> (4 * P)) & 15;
      int d = 16 * pi + 4 * g, e = 16 * pj + l15, hd = (d * 205) >> 12, he = (e * 205) >> 12;
      int h = 3 * nt + hd;
      bool ok = P < 12 && d < 60 && h < 32 && (e == 60 || (e < 60 && he == hd));
      int ee = e == 60 ? 20 : e - 20 * he;
      eo[k] = ok ? (((m * 32 + h) * 21 + ee) * 20 + (d - 20 * hd)) * 4 : ~0u;
      ra[k] = 2 * K1_SLOT + pi * 1024 + l * 16; rb[k] = 2 * K1_SLOT + (4 + pj) * 1024 + l * 16;
      PIN(eo[k]); PIN(ra[k]); PIN(rb[k]);
    }
  }
  SB();
#pragma unroll
  for (int C = 0; C < 10; ++C) {
    Frags& cur = F[C & 1]; Frags& nxt = F[(C + 1) & 1];
    if (C == 8) { mm(cur); SB(); }
    if (C < 9) RAWBAR();
    SB();
    if (C < 9) rd(nxt, C + 1);
    if (C != 8) mm(cur);
    if (C < 8) {
#pragma unroll
      for (int i = 0; i < 4; ++i) {
        __builtin_amdgcn_sched_group_barrier(0x008, 1, 0); __builtin_amdgcn_sched_group_barrier(0x100, 2, 0);
#pragma unroll
        for (int k = 0; k < 3; ++k) { __builtin_amdgcn_sched_group_barrier(0x008, 1, 0); __builtin_amdgcn_sched_group_barrier(0x100, 1, 0); }
      }
    }
    SB();
  }
  STAMP();
  if (QT) {
    u32x4 pa[4];
#pragma unroll
    for (int i = 0; i < 4; ++i) pa[i] = pkfrag(acc[0][i], acc[1][i]);
    SB();
    phi4(pa);
#pragma unroll
    for (int i = 0; i < 4; ++i) { ldsw64(smem, ra[0] + i * 32, u32x2{pa[i][0], pa[i][1]}); ldsw64(smem, ra[0] + 2304 + i * 32, u32x2{pa[i][2], pa[i][3]}); }
    u32x4 o[4];
#pragma unroll
    for (int i = 0; i < 4; ++i) o[i] = ldsr128(smem, rb[0] + i * 1152);
#pragma unroll
    for (int i = 0; i < 4; ++i) *(u32x4*)(ws + OFF_PQ + (size_t)(eo[0] + i * 640)) = o[i];
  } else {
    u32x4 v1 = pkfrag(acc[0][3], acc[1][3]);
    if (chh && l15 == 12) v1 = u32x4{0x3f803f80u, 0x3f803f80u, 0x3f803f80u, 0x3f803f80u};
    const u32 fo = 2 * K1_SLOT + (rg * 8 + 2 * chh) * 1024 + l * 16;
    ldsw128(smem, fo + 4096, pkfrag(acc[0][2], acc[1][2]));
    ldsw128(smem, fo + 5120, v1);
    u32x4 pa[2] = {pkfrag(acc[0][0], acc[1][0]), pkfrag(acc[0][1], acc[1][1])};
    phi4(pa);
    ldsw128(smem, fo, pa[0]);
    ldsw128(smem, fo + 1024, pa[1]);
    STAMP();
    __builtin_amdgcn_s_waitcnt(0xC07F);
    LDS_AS u32* cnt = (LDS_AS u32*)((LDS_AS char*)smem + K1_AUX);
    if (l == 0) __hip_atomic_fetch_add(cnt, 1u, __ATOMIC_RELAXED, __HIP_MEMORY_SCOPE_WORKGROUP);
    while (__builtin_amdgcn_readfirstlane(__hip_atomic_load(cnt, __ATOMIC_RELAXED, __HIP_MEMORY_SCOPE_WORKGROUP)) < 8u) {}
    asm volatile("" ::: "memory");
    STAMP();
    u32x4 fa[4], fb[4];
    f32x4 r0 = {0.f, 0.f, 0.f, 0.f};
#pragma unroll
    for (int r = 0; r < 4; ++r) { fa[r] = ldsr128(smem, ra[0] + r * 8192); fb[r] = ldsr128(smem, rb[0] + r * 8192); }
    if (w < 4) {
      u32x4 fc[4], fd[4];
      f32x4 r1 = r0;
#pragma unroll
      for (int r = 0; r < 4; ++r) { fc[r] = ldsr128(smem, ra[1] + r * 8192); fd[r] = ldsr128(smem, rb[1] + r * 8192); }
      SB();
#pragma unroll
      for (int r = 0; r < 4; ++r) { r0 = mfma(fa[r], fb[r], r0); r1 = mfma(fc[r], fd[r], r1); }
      if (eo[1] != ~0u) st16p(ws + OFF_KVP, eo[1], r1);
    } else {
      SB();
#pragma unroll
      for (int r = 0; r < 4; ++r) r0 = mfma(fa[r], fb[r], r0);
    }
    if (eo[0] != ~0u) st16p(ws + OFF_KVP, eo[0], r0);
  }
}

template <bool C4>
DI void k1_dft(const char* vb, const u32x4 (&gs)[8], const u32x4 (&xq)[4][3], u32 vo, u32 k01, u32 k2, float tc, float ts, int l) {
  constexpr int NB = 1;
  const u32 kv01[1] = {k01}, kv2[1] = {k2};
  const int g = l >> 4, h = (l >> 3) & 1, j = l & 7;
  const u32 hm = (j & 1) ? 0xffff0000u : 0xffffu;
  const f32x4 z4 = {0.f, 0.f, 0.f, 0.f};
  u32x4 kd[NB][3], tb[NB][2];
  f32x4 t[NB][4], y[NB][4];
#pragma unroll
  for (int u = 0; u < NB; ++u) {
    kd[u][0] = onehot(((j & 1) ? kv01[u] << 16 : kv01[u]) & hm, j >> 1); kd[u][1] = onehot(((j & 1) ? kv01[u] : kv01[u] >> 16) & hm, j >> 1);
    kd[u][2] = onehot(kv2[u] & hm, j >> 1);
  }
  SB();
#pragma unroll
  for (int s = 0; s < 3; ++s)
#pragma unroll
    for (int u = 0; u < NB; ++u)
#pragma unroll
      for (int rt = 0; rt < 4; ++rt) t[u][rt] = mfma(xq[rt][s], kd[u][s], s ? t[u][rt] : z4);
#pragma unroll
  for (int u = 0; u < NB; ++u) { tb[u][0] = pkfrag(t[u][0], t[u][1]); tb[u][1] = pkfrag(t[u][2], t[u][3]); }
#pragma unroll
  for (int k = 0; k < 2; ++k)
#pragma unroll
    for (int u = 0; u < NB; ++u)
#pragma unroll
      for (int mt = 0; mt < 4; ++mt) y[u][mt] = mfma(gs[2 * mt + k], tb[u][k], k ? y[u][mt] : z4);
  SB();
#pragma unroll
  for (int u = 0; u < NB; ++u) {
    float X[8], Y[8]; u32 pk[8];
    const u32 vu = vo;
    if (!C4) {
#pragma unroll
      for (int i = 0; i < 8; ++i) { X[i] = y[u][i >> 1][2 * (i & 1)] - ror8(y[u][i >> 1][2 * (i & 1) + 1]); Y[i] = y[u][i >> 1][2 * (i & 1) + 1] + ror8(y[u][i >> 1][2 * (i & 1)]); }
      float p0 = ror8(y[u][0][0]), p1 = ror8(y[u][0][1]);
      X[0] = g == 0 ? (h ? y[u][0][1] : y[u][0][0]) : X[0]; Y[0] = g == 0 ? (h ? p1 : p0) : Y[0];
      SB();
#pragma unroll
      for (int i = 0; i < 8; ++i) pk[i] = pk2(X[i], Y[i]);
      SB();
#pragma unroll
      for (int i = 0; i < 8; ++i) pk[i] = __builtin_amdgcn_alignbit(pk[i], pk[i], 16 * h);
    } else {
      const float o1 = y[u][0][1];
      if (g == 0) ST4SO(vb, vu, 2048, pk2(o1 * tc, o1 * ts));
      y[u][0][1] = g == 0 ? 0.f : o1;
#pragma unroll
      for (int i = 0; i < 8; ++i) {
        float o0 = y[u][i >> 1][2 * (i & 1)], o2 = y[u][i >> 1][2 * (i & 1) + 1];
        X[i] = o0 * tc - o2 * ts; Y[i] = o0 * ts + o2 * tc;
      }
      SB();
#pragma unroll
      for (int i = 0; i < 8; ++i) pk[i] = pk2(X[i], Y[i]);
    }
    SB();
#pragma unroll
    for (int i = 0; i < 8; ++i) ST4SO(vb, vu, i * 256, pk[i]);
  }
}

__global__ __launch_bounds__(1024) __attribute__((amdgpu_waves_per_eu(4, 4)))
void k1(const u16* __restrict__ x, const u16* __restrict__ wqkv, const u16* __restrict__ wr, const u16* __restrict__ wi,
        const u16* __restrict__ gate, const u16* __restrict__ wo, char* __restrict__ ws) {
  extern __shared__ __attribute__((aligned(16))) char smem[];
  const int tid = threadIdx.x, l = tid & 63, w = __builtin_amdgcn_readfirstlane(tid >> 6);
  const int b = blockIdx.x, xcd = b & 7, bi = b >> 3;
  const int m = (xcd >> 1) * 4 + (bi & 3), nt = (xcd & 1) * 8 + (bi >> 2);
  asm volatile("" :: "s"(x), "s"(wqkv), "s"(wr), "s"(wi), "s"(gate), "s"(wo), "s"(ws));
  STAMP_DECL; STAMP();
#define TSARG
  if (w >= 8) {
    const int v = w - 8, u = v >> 1;
    int cn = ((nt & 7) * 5 >> 2) + ((m & 3) * 5 >> 1); if (cn >= 10) cn -= 10;
    const int cq = nt >= 11, cbase = cq ? 128 * (nt - 11) : 640 + 60 * nt, cs = cq ? 64 : 640, cmax = nt == 10 ? 4 : 7;
    u32 da[4];
#pragma unroll
    for (int e = 0; e < 4; ++e) {
      const int cbk = (l & 15) ^ (((l >> 4) << 2) | e);
      da[e] = ((v & 1) ? (4 * e + (l >> 4)) * 3840 + ((cbk >> 3) * cs + 8 * min(cbk & 7, cmax)) * 2
                       : (8 * e + (l >> 3)) * 40960 + (((l & 7) ^ ((4 * e + (l >> 4)) & 7)) << 4)) - 1024 * e;
    }
    const char* sa = (v & 1) ? (const char*)wqkv + u * (16 * 3840) + cbase * 2 : (const char*)x + ((u & 1) * 1024 + 2 * m + (u >> 1)) * 1280;
    const int cstep = (v & 1) ? 64 * 3840 : 128;
    const u32 dl = LDSADDR(smem) + u * 4096 + (v & 1) * K1_B;
    int cc = cn;
    auto issue = [&](int slot) {
      dma4(dl + slot * K1_SLOT, sa + cn * cstep, da[0], da[1], da[2], da[3]);
      cn = cn == 9 ? 0 : cn + 1;
    };
    __builtin_amdgcn_s_setprio(2);
    issue(0); issue(1);
    STAMP();
    if (l == 0 && v == 0) *(LDS_AS u32*)((LDS_AS char*)smem + K1_AUX) = 0u;
    const int n2s = nt >> 3, bg = nt & 7, g = l >> 4;
    const u32 ca = n2s * 8192 + (l & 15) * 128 + ((bg ^ ((l >> 1) & 7)) << 4);
    u32x4 xq[4][3];
#pragma unroll
    for (int rt = 0; rt < 4; ++rt) xq[rt][0] = xq[rt][1] = xq[rt][2] = u32x4{0u, 0u, 0u, 0u};
    auto step = [&](int C) {
      RAWBAR();
      if (C < 7) issue((C + 3) & 3);
      {
        const u32 o = LDSADDR(smem) + ca + ((C + 1) & 3) * K1_SLOT;
        const unsigned long long mk = (v < 5 ? 0xffffull : 0ull) << (16 * (cc & 3));
        asm volatile("s_mov_b64 exec, %13\n\ts_cmp_lt_u32 %14, 4\n\ts_cbranch_scc0 1f\n\t"
                     "ds_read_b128 %0, %12\n\tds_read_b128 %1, %12 offset:2048\n\tds_read_b128 %2, %12 offset:4096\n\tds_read_b128 %3, %12 offset:6144\n\ts_branch 3f\n"
                     "1:\n\ts_cmp_lt_u32 %14, 8\n\ts_cbranch_scc0 2f\n\t"
                     "ds_read_b128 %4, %12\n\tds_read_b128 %5, %12 offset:2048\n\tds_read_b128 %6, %12 offset:4096\n\tds_read_b128 %7, %12 offset:6144\n\ts_branch 3f\n"
                     "2:\n\tds_read_b128 %8, %12\n\tds_read_b128 %9, %12 offset:2048\n\tds_read_b128 %10, %12 offset:4096\n\tds_read_b128 %11, %12 offset:6144\n"
                     "3:\n\ts_mov_b64 exec, -1"
                     : "+v"(xq[0][0]), "+v"(xq[1][0]), "+v"(xq[2][0]), "+v"(xq[3][0]), "+v"(xq[0][1]), "+v"(xq[1][1]), "+v"(xq[2][1]), "+v"(xq[3][1]),
                       "+v"(xq[0][2]), "+v"(xq[1][2]), "+v"(xq[2][2]), "+v"(xq[3][2]) : "v"(o), "s"(mk), "s"(cc) : "memory", "scc");
      }
      cc = cc == 9 ? 0 : cc + 1;
    };
#pragma unroll 1
    for (int C = -1; C < 2; ++C) { WAITV(4); step(C); }
    dma1(LDSADDR(smem) + K1_GS + v * 1024, (const char*)dGsA.v + v * 1024, l * 16);
    const int h = (l >> 3) & 1, bb = 8 * bg + (l & 7);
    u32 kv01, kv2;
    {
      float c3[3];
#pragma unroll
      for (int s = 0; s < 3; ++s) c3[s] = __builtin_amdgcn_cosf((float)((v + 1) * (64 * (4 * s + g) + bb)) * (1.f / 640.f) - 0.25f * h);
      kv01 = pk2(c3[0], c3[1]); kv2 = pk2(c3[2], c3[2]);
      if (v == 4) kv01 = kv2 = (h & g) ? 0xbf80bf80u : 0x3f803f80u;
    }
    float tc = h ? __builtin_amdgcn_cosf(bb * (1.f / 128.f)) : 1.f, ts = h ? __builtin_amdgcn_sinf(bb * (1.f / 128.f)) : 0.f;
    u32 vo = ((2 * m + n2s) * 8 + bg) * VB_TS + l * 4;
    PIN(kv01); PIN(kv2); PIN(tc); PIN(ts); PIN(vo);
    WAITV(5); step(2);
#pragma unroll 1
    for (int C = 3; C < 7; ++C) { WAITV(4); step(C); }
    STAMP();
    {
      const u32 dump = LDSADDR(smem) + K1_AUX + 16;
      const int ci = 8 * ((b & 7) >> 1) + 2 * (b >> 6) + (b & 1), q = ((b >> 5) & 1) ? (ci ? 64 - ci : 32) : ci;
      const u32 wo2 = ((q + 64 * (8 * ((b >> 3) & 3) + (l >> 3))) * 642 + min((l & 7) * 128, 638)) & ~3u;
      const char* pc; int d0, d1;
      asm volatile("s_getpc_b64 %0\n1:\n\ts_mov_b32 %1, _Z2k1PKtS0_S0_S0_S0_S0_Pc-1b\n\ts_mov_b32 %2, _Z4kendv-1b" : "=s"(pc), "=s"(d0), "=s"(d1));
      const char* ca2 = l < 12 ? pc + d0 + (bi * 12 + l) * 128 : (const char*)dF64.v + (bi * 4 + (l & 3)) * 128;
      if (ca2 >= pc + d1 && l < 12) ca2 = pc;
      const int id = 25 * b + min((l & 7) * 4, 24), ko = (id * 52429) >> 22;
      const char* wo3 = (const char*)wo + (8 * ko + (l >> 3)) * 1280 + (id - 80 * ko) * 16;
      l2touch(dump, v == 6 ? (const char*)wr + wo2 : (v == 7 ? (const char*)wi + wo2 : (v == 5 ? wo3 : (v == 4 ? ca2 : pc))));
    }
    WAITV(5); step(7);
    WAITV(1); step(8);
    u32x4 gs[8];
#pragma unroll
    for (int f = 0; f < 8; ++f) gs[f] = ldsr128(smem, K1_GS + f * 1024 + l * 16);
    asm volatile("s_waitcnt lgkmcnt(0)" : "+v"(xq[0][0]), "+v"(xq[1][0]), "+v"(xq[2][0]), "+v"(xq[3][0]), "+v"(xq[0][1]), "+v"(xq[1][1]), "+v"(xq[2][1]), "+v"(xq[3][1]),
                 "+v"(xq[0][2]), "+v"(xq[1][2]), "+v"(xq[2][2]), "+v"(xq[3][2]) :: "memory");
    STAMP();
    if (v == 5) {
      const int id = 25 * b + min(l, 24), ko = (id * 52429) >> 22, cb = id - 80 * ko, a = cb >> 3, hp = (ko * 205) >> 10, wq = ko - 5 * hp;
#pragma unroll
      for (int i = 0; i < 8; ++i) gs[i] = *(const u32x4*)((const char*)wo + ko * 10240 + cb * 16 + i * 1280);
      u32x4* dst = (u32x4*)(ws + OFF_WOF) + ((((cb & 7) * 5 + (a >> 1)) * 20 + (hp >> 2) * 5 + wq) * 64 + (hp & 3) * 16 + (a & 1) * 8);
#pragma unroll
      for (int jj = 0; jj < 8; ++jj) {
        u32x4 o2;
#pragma unroll
        for (int d = 0; d < 4; ++d) o2[d] = __builtin_amdgcn_perm(gs[2 * d + 1][jj >> 1], gs[2 * d][jj >> 1], (jj & 1) ? 0x07060302u : 0x05040100u);
        dst[jj] = o2;
      }
    }
    asm volatile("" : "+v"(gs[0]), "+v"(gs[1]), "+v"(gs[2]), "+v"(gs[3]), "+v"(gs[4]), "+v"(gs[5]), "+v"(gs[6]), "+v"(gs[7]));
    if (v < 4) k1_dft<false>(ws + OFF_VB, gs, xq, vo + v * 2048, kv01, kv2, tc, ts, l);
    else if (v == 4) k1_dft<true>(ws + OFF_VB, gs, xq, vo + 8192, kv01, kv2, tc, ts, l);
  } else if (nt >= 11) k1_gemm<true>(smem, x, gate, ws, m, nt, w, l TSARG);
  else k1_gemm<false>(smem, x, gate, ws, m, nt, w, l TSARG);
  STAMP();
  STAMP_FLUSH(0);
}

constexpr int K2_V = 0, K2_A = 16384, K2_U = 32768, K2_US = 272, K2_PF = K2_U + 2 * 32 * K2_US, K2_W = K2_PF + 272, WROW = 656, K2_LDS = K2_W + 128 * WROW;
constexpr int K2_NF = 168;
constexpr int K2_NO = 64;

__global__ __launch_bounds__(1024) __attribute__((amdgpu_waves_per_eu(4, 4)))
void k2(const u16* __restrict__ wr, const u16* __restrict__ wi, char* __restrict__ ws) {
  extern __shared__ __attribute__((aligned(16))) char smem[];
  const int tid = threadIdx.x, l = tid & 63, w8 = __builtin_amdgcn_readfirstlane(tid >> 6);
  const int g = l >> 4, l15 = l & 15;
  const int b = blockIdx.x;
  const f32x4 z4 = {0.f, 0.f, 0.f, 0.f};
  asm volatile("" :: "s"(wr), "s"(wi), "s"(ws));
  auto prefetch = [&]() {
    const int i = (b >> 3) * 224 + tid;
    if (tid < 224 && i < 6400) l2touch(LDSADDR(smem) + K2_PF, ws + OFF_WOF + (size_t)i * 128);
    if (tid >= 448 && tid < 451 && (b >> 3) * 3 + tid - 448 < 64) l2touch(LDSADDR(smem) + K2_PF, (const char*)dGsB.v + ((b >> 3) * 3 + tid - 448) * 128);
  };
  STAMP_DECL; STAMP();
  if (b < K2_NF) {
    const int sidx = (b >> 5) * 32 + (b & 7) * 4 + ((b >> 3) & 3);
    if (sidx > 160) { prefetch(); return; }
    const int c = sidx >> 5 > 4 ? 4 : sidx >> 5;
    const int ci = sidx == 160 ? 32 : ((sidx >> 3) & 3) * 8 + (sidx & 3) * 2 + ((sidx >> 2) & 1);
    const int qa = ci, qb = (c < 4 && ci == 0) ? 32 : (64 - ci) & 63;
    LDS_AS u32* cnt = (LDS_AS u32*)((LDS_AS char*)smem + K2_PF + 256);
    if (tid == 0) *cnt = 0u;
    RAWBAR();
    if (w8 >= 8) {
      const int v = w8 - 8;
      u32 vo[11];
#pragma unroll
      for (int i = 0; i < 11; ++i) { const int P = 64 * i + l; vo[i] = (P - 64 * (i & 3)) * 16 + ((P * 1599) >> 16) * (41088 - 656); }
      const char* wsrc = (const char*)((v >> 2) ? wi : wr) + (((((v >> 1) & 1) ? qb : qa) * 642) & ~15) + (v & 1) * (16 * 41088);
      const u32 L = LDSADDR(smem) + K2_W + v * 16 * WROW;
      {
      const int half = v >> 2, w4 = v & 3, q = c == 4 ? ci : (half == 0 ? ci : (ci == 0 ? 32 : 64 - ci));
    {
      const int ri = 16 * (w4 & 1) + l15;
      const int B = w4 < 2 ? q + 64 * ri : 64 * ri, A = (w4 < 2 ? 0 : ri * q) + B * 4 * g;
      float cs[8], sn[8];
#pragma unroll
      for (int j = 0; j < 8; ++j) {
        float rev = (float)((A + B * (j < 4 ? j : 12 + j)) & 2047) * (1.f / 2048.f);
        cs[j] = __builtin_amdgcn_cosf(rev); sn[j] = __builtin_amdgcn_sinf(rev);
      }
      ldsw128(smem, K2_A + v * 2048 + l * 16, u32x4{pk2(cs[0], cs[1]), pk2(cs[2], cs[3]), pk2(cs[4], cs[5]), pk2(cs[6], cs[7])});
      ldsw128(smem, K2_A + v * 2048 + 1024 + l * 16, u32x4{pk2(sn[0], sn[1]), pk2(sn[2], sn[3]), pk2(sn[4], sn[5]), pk2(sn[6], sn[7])});
    }
      __builtin_amdgcn_s_waitcnt(0xC07F);
      if (l == 0) __hip_atomic_fetch_add(cnt, 1u, __ATOMIC_RELAXED, __HIP_MEMORY_SCOPE_WORKGROUP);
      }
      dma4(L, wsrc, vo[0], vo[1], vo[2], vo[3]); dma4(L + 4096, wsrc, vo[4], vo[5], vo[6], vo[7]); dma2(L + 8192, wsrc, vo[8], vo[9]);
      if (l < 16) dma1(L + 10240, wsrc + 2048, vo[10]);
      WAITV(0);
      STAMP_FLUSH(1);
      return;
    }
    const int half = w8 >> 2, w4 = w8 & 3;
    {
      int kc0 = l15 ^ ((8 * w4 + g) & 15), kc1 = kc0 ^ 4;
      u32 v0 = ((8 * w4 + g) * 8 + (kc0 >> 1)) * VB_TS + (kc0 & 1) * 16, v1 = ((8 * w4 + 4 + g) * 8 + (kc1 >> 1)) * VB_TS + (kc1 & 1) * 16 - 1024;
      dma2(LDSADDR(smem) + K2_V + w8 * 2048, ws + OFF_VB + sidx * 64 + half * 32, v0, v1);
    }
    u32x4 tr[4], ti[4];
    dma2(LDSADDR(smem) + K2_U + w8 * 2048, (const char*)dF64.v + w8 * 2048, l * 16, l * 16);
    const int q = c == 4 ? ci : (half == 0 ? ci : (ci == 0 ? 32 : 64 - ci));
    u32 va = K2_V + half * 8192 + l15 * 256 + ((g ^ (l15 & 3)) << 4) + ((l15 >> 2) << 6), aa = K2_A + half * 8192 + l * 16;
    PIN(va); PIN(aa);
    SB();
    const float sc = 2.f / (2048.f * 640.f), s2 = (c == 4 && (ci & 31) == 0) ? 0.5f * sc : sc;
    f32x4 tc = z4, ts = z4;
    if (c == 4) {
#pragma unroll
      for (int i = 0; i < 4; ++i) {
        float rev = (float)(16 * w4 + 4 * g + i) * (1.f / 128.f);
        tc[i] = __builtin_amdgcn_cosf(rev) * s2; ts[i] = __builtin_amdgcn_sinf(rev) * s2;
      }
    }
    const int so = sidx == 160 ? 128 : sidx;
    char* dst = ws + OFF_AP + (size_t)((16 * half + l15) * 8 + 2 * w4 + (g >> 1)) * AP_TS + so * 64 + (g & 1) * 32;
    u32 uw = K2_U + half * (32 * K2_US) + 4 * g * K2_US + (16 * w4 + l15) * 4, ur0 = K2_U + (16 * half + l15) * K2_US + g * 16;
    asm volatile("" : "+v"(tc), "+v"(ts), "+v"(dst), "+v"(uw), "+v"(ur0));
    const int r = c == 4 ? 5 * half : c + 1, kd = r + 10 * (16 * w4 + l15);
    const bool mir = w4 >= 2;
    u32 wg[2], wg0[2];
#pragma unroll
    for (int k = 0; k < 2; ++k) {
      const bool mr = mir != (k == 1);
      const int qs = mr ? (64 - q) & 63 : q, col = k ? (mir ? 320 : 0) : (mir ? 640 - kd : kd);
      wg[k] = K2_W + (qs != qa) * 32 * WROW + ((2 * qs) & 15) + 2 * col + (mr ? ((q == 0 ? 32 : 31) - 19 - 4 * g) * WROW : 4 * g * WROW);
      wg0[k] = (mr && q == 0 && g == 0) ? wg[k] - 13 * WROW : wg[k] + (mr ? 19 * WROW : 0);
      PIN(wg[k]); PIN(wg0[k]);
    }
    WAITV(0);
    STAMP();
    __builtin_amdgcn_s_waitcnt(0xC07F);
    if (l == 0) __hip_atomic_fetch_add(cnt, 1u, __ATOMIC_RELAXED, __HIP_MEMORY_SCOPE_WORKGROUP);
    while (__builtin_amdgcn_readfirstlane(__hip_atomic_load(cnt, __ATOMIC_RELAXED, __HIP_MEMORY_SCOPE_WORKGROUP)) < 16u) {}
    asm volatile("" ::: "memory");
    STAMP();
    u32x4 vf[2][4], cf[4], sf[4];
#pragma unroll
    for (int ks = 0; ks < 4; ++ks) tr[ks] = ldsr128(smem, K2_U + w4 * 4096 + ks * 1024 + l * 16);
#pragma unroll
    for (int ks = 0; ks < 4; ++ks)
#pragma unroll
      for (int mt = 0; mt < 2; ++mt) vf[mt][ks] = ldsr128(smem, (va ^ (ks << 6)) + mt * 4096);
#pragma unroll
    for (int i = 0; i < 4; ++i) {
      cf[i] = ldsr128(smem, aa + i * 2048); sf[i] = ldsr128(smem, aa + i * 2048 + 1024);
    }
    SB();
    f32x4 Tr[2] = {z4, z4}, Ti[2] = {z4, z4};
#pragma unroll
    for (int ks = 0; ks < 4; ++ks) {
#pragma unroll
      for (int mt = 0; mt < 2; ++mt) Tr[mt] = mfma(vf[mt][ks], tr[ks], Tr[mt]);
#pragma unroll
      for (int i = 0; i < 4; ++i) ti[ks][i] = __builtin_amdgcn_alignbit(tr[ks][i], tr[ks][i], 16) ^ 0x8000u;
      SB();
    }
#pragma unroll
    for (int ks = 0; ks < 4; ++ks)
#pragma unroll
      for (int mt = 0; mt < 2; ++mt) Ti[mt] = mfma(vf[mt][ks], ti[ks], Ti[mt]);
    SB();
    u32x4 br = pkfrag(Tr[0], Tr[1]), bi = pkfrag(Ti[0], Ti[1]), ni = bi ^ 0x80008000u;
    f32x4 zr[2], zn[2];
#pragma unroll
    for (int mt = 0; mt < 2; ++mt) { zr[mt] = mfma(cf[mt], br, z4); zn[mt] = mfma(cf[mt], bi, z4); }
#pragma unroll
    for (int mt = 0; mt < 2; ++mt) { zr[mt] = mfma(sf[mt], ni, zr[mt]); zn[mt] = mfma(sf[mt], br, zn[mt]); }
    SB();
    __builtin_amdgcn_s_barrier();
    asm volatile("" ::: "memory");
    STAMP();
    f32x4 wa[2], wb[2];
    {
      u32 ra[8], rb[8];
      auto gather = [&](int s, bool mr) {
#pragma unroll
        for (int k = 0; k < 8; ++k) {
          const int dk = 16 * (k >> 2) + (k & 3);
          const u32 o = k == 0 ? wg0[s] : wg[s] + (mr ? 19 - dk : dk) * WROW;
          ra[k] = ldsr16(smem, o); rb[k] = ldsr16(smem, o + 64 * WROW);
        }
      };
      if (mir) gather(0, true); else gather(0, false);
      prefetch();
      const u32 sg = mir ? 0x80000000u : 0u;
#pragma unroll
      for (int k = 0; k < 8; ++k) { wa[k >> 2][k & 3] = lo2f(ra[k]); wb[k >> 2][k & 3] = __uint_as_float((rb[k] << 16) ^ sg); }
      if (r == 0 && (w4 & 1) == 0) {
        if (mir) gather(1, false); else gather(1, true);
#pragma unroll
        for (int k = 0; k < 8; ++k) {
          wa[k >> 2][k & 3] = l15 == 0 ? 0.5f * (wa[k >> 2][k & 3] + lo2f(ra[k])) : wa[k >> 2][k & 3];
          wb[k >> 2][k & 3] = l15 == 0 ? 0.5f * (wb[k >> 2][k & 3] - __uint_as_float((rb[k] << 16) ^ sg)) : wb[k >> 2][k & 3];
        }
      }
    }
    f32x4 Yr[2], Yi[2];
#pragma unroll
    for (int mt = 0; mt < 2; ++mt) { Yr[mt] = zr[mt] * wa[mt]; Yi[mt] = zr[mt] * wb[mt]; }
    SB();
#pragma unroll
    for (int mt = 0; mt < 2; ++mt) { Yr[mt] += zn[mt] * wb[mt]; Yi[mt] -= zn[mt] * wa[mt]; }
    SB();
    br = pkfrag(Yr[0], Yr[1]); bi = pkfrag(Yi[0], Yi[1]); ni = bi ^ 0x80008000u;
    f32x4 ur[2], ui[2];
#pragma unroll
    for (int mt = 0; mt < 2; ++mt) { ur[mt] = mfma(cf[2 + mt], br, z4); ui[mt] = mfma(cf[2 + mt], bi, z4); }
#pragma unroll
    for (int mt = 0; mt < 2; ++mt) { ur[mt] = mfma(sf[2 + mt], ni, ur[mt]); ui[mt] = mfma(sf[2 + mt], br, ui[mt]); }
    SB();
#pragma unroll
    for (int mt = 0; mt < 2; ++mt)
#pragma unroll
      for (int i = 0; i < 4; ++i) *(LDS_AS u32*)((LDS_AS char*)smem + uw + (16 * mt + i) * K2_US) = pk2(ur[mt][i], ui[mt][i]);
    STAMP();
    RAWBAR();
    STAMP();
    u32x4 uf[2][4];
#pragma unroll
    for (int ks = 0; ks < 4; ++ks)
#pragma unroll
      for (int cl = 0; cl < 2; ++cl) uf[cl][ks] = ldsr128(smem, ur0 + cl * (32 * K2_US) + ks * 64);
    SB();
    f32x4 Rr[2] = {z4, z4}, Ri[2] = {z4, z4};
#pragma unroll
    for (int ks = 0; ks < 4; ++ks)
#pragma unroll
      for (int cl = 0; cl < 2; ++cl) { Rr[cl] = mfma(tr[ks], uf[cl][ks], Rr[cl]); Ri[cl] = mfma(ti[ks], uf[cl][ks], Ri[cl]); }
    f32x4 e00, e01, e10, e11;
    if (c < 4) {
      f32x4 ar = Rr[0] * sc, ai = Ri[0] * -sc;
      if (ci > 0) { e00 = ar + Rr[1] * sc; e01 = ai - Ri[1] * sc; e10 = ai + Ri[1] * sc; e11 = Rr[1] * sc - ar; }
      else { e00 = ar; e01 = ai; e10 = Rr[1] * sc; e11 = Ri[1] * -sc; }
    } else {
      e00 = Rr[0] * s2; e01 = Rr[1] * tc - Ri[1] * ts;
      if (ci & 31) { e10 = Ri[0] * -s2; e11 = -(Rr[1] * ts + Ri[1] * tc); }
      else { e10 = e00; e11 = e01; }
    }
    if (sidx != 160) *(u32x4*)dst = pkfrag(e00, e01);
    if (sidx != 128) *(u32x4*)(dst + 16) = pkfrag(e10, e11);
    STAMP();
  } else {
    if (w8 >= 8) return;
    const int ob = b - K2_NF, hp = ob & 15, n2 = 8 * (ob >> 4) + w8;
    u32x4 s[8];
    const int rh = tid >= 256, rp = tid & 255;
    if (rp < 210) {
      const char* kb = ws + OFF_KVP + hp * 3360;
      const u32 ko = rp * 16 + rh * (8 * 53760);
#pragma unroll
      for (int mm = 0; mm < 8; ++mm) asm volatile("global_load_dwordx4 %0, %1, %2" : "=v"(s[mm]) : "v"(ko), "s"(kb + mm * 53760) : "memory");
    }
    const char* src = ws + OFF_PQ + ((size_t)hp * 2048 + 64 * n2) * 80;
    const u32 pl = 8192 + w8 * 5136;
    dma4(LDSADDR(smem) + pl, src, l * 16, l * 16, l * 16, l * 16);
    dma1(LDSADDR(smem) + pl + 4096, src + 4096, l * 16);
    const int gc = g > 2 ? 2 : g;
    const u32 m0 = g < 3 ? ~0u : 0u, m1 = g < 2 ? ~0u : 0u;
    u32 oo = (n2 * 16 + hp) * 5120 + l * 16;
    u32 a0 = l15 * 48 + gc * 16, a1 = (16 + (l15 & 3)) * 48 + gc * 16, a2 = 960 + gc * 16;
    u32 pr = pl + l15 * 80 + 16 * gc, pw = pl + l15 * 80 + 8 * g, pw2 = pl + l15 * 80 + 32 + 4 * (g & 1), pc = pl + l * 16;
    const int u_ = rp >= 105, in = rp - 105 * u_, e_ = (in * 205) >> 10;
    u32 kw = u_ * 1024 + e_ * 48 + (in - 5 * e_) * 8;
    PIN(oo); PIN(a0); PIN(a1); PIN(a2); PIN(pr); PIN(pw); PIN(pw2); PIN(pc); PIN(kw);
    asm volatile("s_waitcnt vmcnt(5)" : "+v"(s[0]), "+v"(s[1]), "+v"(s[2]), "+v"(s[3]), "+v"(s[4]), "+v"(s[5]), "+v"(s[6]), "+v"(s[7]) :: "memory");
    f32x4 sum = ((__builtin_bit_cast(f32x4, s[0]) + __builtin_bit_cast(f32x4, s[1])) + (__builtin_bit_cast(f32x4, s[2]) + __builtin_bit_cast(f32x4, s[3])))
              + ((__builtin_bit_cast(f32x4, s[4]) + __builtin_bit_cast(f32x4, s[5])) + (__builtin_bit_cast(f32x4, s[6]) + __builtin_bit_cast(f32x4, s[7])));
    if (rh) *(LDS_AS f32x4*)((LDS_AS char*)smem + 2048 + rp * 16) = sum;
    RAWBAR();
    if (!rh && rp < 210) {
      sum += *(const LDS_AS f32x4*)((const LDS_AS char*)smem + 2048 + rp * 16);
      ldsw64(smem, kw, u32x2{pk2(sum[0], sum[1]), pk2(sum[2], sum[3])});
    }
    STAMP();
    WAITV(0);
    prefetch();
    u32x4 af[2][3], pf[4][2];
#pragma unroll
    for (int rt = 0; rt < 4; ++rt)
#pragma unroll
      for (int u = 0; u < 2; ++u) {
        u32x2 a = ldsr64(smem, pr + rt * 1280 + 40 * u), bq = ldsr64(smem, pr + rt * 1280 + 40 * u + 8);
        pf[rt][u] = u32x4{a[0], a[1], bq[0], bq[1]};
      }
    RAWBAR();
    STAMP();
#pragma unroll
    for (int u = 0; u < 2; ++u) { af[u][2] = ldsr128(smem, a2 + u * 1024); af[u][0] = ldsr128(smem, a0 + u * 1024); af[u][1] = ldsr128(smem, a1 + u * 1024); }
    SB();
    pf[3][1] &= u32x4{~0u, ~0u, m1, m1};
#pragma unroll
    for (int u = 0; u < 2; ++u)
#pragma unroll
      for (int tI = 0; tI < 3; ++tI) af[u][tI] &= u32x4{m0, m0, m1, m1};
    f32x4 n0[4][2], n1[4][2], dn[4][2];
#pragma unroll
    for (int u = 0; u < 2; ++u)
#pragma unroll
      for (int rt = 0; rt < 4; ++rt) dn[rt][u] = mfma(af[u][2], pf[rt][u], z4);
#pragma unroll
    for (int u = 0; u < 2; ++u)
#pragma unroll
      for (int rt = 0; rt < 4; ++rt) { n0[rt][u] = mfma(af[u][0], pf[rt][u], z4); n1[rt][u] = mfma(af[u][1], pf[rt][u], z4); }
    SB();
    float rd[8]; u32 b0[8], b1[8], b2[8];
#pragma unroll
    for (int k = 0; k < 8; ++k) rd[k] = bfr(dn[k & 3][k >> 2][0]) + 1e-6f;
    SB();
#pragma unroll
    for (int k = 0; k < 8; ++k) rd[k] = __builtin_amdgcn_rcpf(bfr(rd[k]));
    SB();
#pragma unroll
    for (int k = 0; k < 8; ++k) {
      const f32x4 a = n0[k & 3][k >> 2], c2 = n1[k & 3][k >> 2];
      b0[k] = pk2(a[0], a[1]); b1[k] = pk2(a[2], a[3]); b2[k] = (g & 1) ? pk2(c2[2], c2[3]) : pk2(c2[0], c2[1]);
    }
    SB();
#pragma unroll
    for (int k = 0; k < 8; ++k) {
      f32x2 x0 = f32x2{lo2f(b0[k]), hi2f(b0[k])} * rd[k], x1 = f32x2{lo2f(b1[k]), hi2f(b1[k])} * rd[k], x2 = f32x2{lo2f(b2[k]), hi2f(b2[k])} * rd[k];
      b0[k] = pk2(x0[0], x0[1]); b1[k] = pk2(x1[0], x1[1]); b2[k] = pk2(x2[0], x2[1]);
    }
#pragma unroll
    for (int k = 0; k < 8; ++k) {
      ldsw64(smem, pw + (k & 3) * 1280 + 40 * (k >> 2), u32x2{b0[k], b1[k]});
      *(LDS_AS u32*)((LDS_AS char*)smem + pw2 + (k & 3) * 1280 + 40 * (k >> 2)) = b2[k];
    }
    u32x4 o[5];
#pragma unroll
    for (int i = 0; i < 5; ++i) o[i] = ldsr128(smem, pc + i * 1024);
    ST16SO(ws + OFF_O2, oo, 0, o[0]); ST16SO(ws + OFF_O2, oo, 1024, o[1]); ST16SO(ws + OFF_O2, oo, 2048, o[2]); ST16SO(ws + OFF_O2, oo, 3072, o[3]);
    ST16SO(ws + OFF_O2 + 4096, oo, 0, o[4]);
    STAMP();
  }
  STAMP_FLUSH(1);
}

constexpr int K3_W = 81920, K3_AP = 61440, K3_GS = K3_AP + 10240, K3_CNT = K3_W + 3 * 25600, K3_LDS = K3_CNT + 16;

__global__ __launch_bounds__(1024) __attribute__((amdgpu_waves_per_eu(4, 4)))
void k3(const char* __restrict__ ws, u16* __restrict__ out) {
  extern __shared__ __attribute__((aligned(16))) char smem[];
  const int tid = threadIdx.x, l = tid & 63, w = __builtin_amdgcn_readfirstlane(tid >> 6);
  const int g = l >> 4, l15 = l & 15;
  const int b = blockIdx.x, n2 = 4 * (b & 7) + ((b >> 3) & 3), bg = b >> 5;
  const f32x4 z4 = {0.f, 0.f, 0.f, 0.f};
  asm volatile("" :: "s"(ws), "s"(out));
  STAMP_DECL; STAMP();
  if (w >= 10) {
    const int v = w - 10, rot = (bg + (b >> 3)) & 3;
    const u32 lo = l * 16, L = LDSADDR(smem);
    __builtin_amdgcn_s_setprio(2);
    {
      const char* ap = ws + OFF_AP + (size_t)(n2 * 8 + bg) * AP_TS;
      if (v < 5) dma2(L + K3_AP + v * 2048, ap + v * 2048, lo, lo);
      if (v < 5) dma1(L + K3_GS + v * 1024, (const char*)dGsB.v + v * 1024, lo);
      else { dma2(L + K3_GS + 5120, (const char*)dGsB.v + 5120, lo, lo); dma1(L + K3_GS + 7168, (const char*)dGsB.v + 7168, lo); }
    }
    const char* op = ws + OFF_O2 + (size_t)n2 * 81920 + v * 4096;
    const char* wp = ws + OFF_WOF + (size_t)bg * 102400;
    auto issue = [&](int Q) {
      const int Qd = (Q + rot) & 3;
      const u32 lw = L + K3_W + (Q == 3 ? 0 : Q) * 25600;
      if (v < 5) {
        dma4(L + Q * 20480 + v * 4096, op + Qd * 20480, lo, lo, lo, lo);
        dma4(lw + v * 5120, wp + v * 20480 + Qd * 5120, lo, lo, lo, lo);
      } else {
#pragma unroll
        for (int c = 0; c < 5; ++c) dma1(lw + c * 5120 + 4096, wp + c * 20480 + Qd * 5120 + 4096, lo);
      }
    };
#define K3W() do { if (v < 5) WAITV(8); else WAITV(5); __builtin_amdgcn_s_barrier(); } while (0)
    issue(0);
    if (tid == 640) *(LDS_AS u32*)((LDS_AS char*)smem + K3_CNT) = 0u;
    __builtin_amdgcn_s_waitcnt(0xC07F);
    K3W();
    issue(1);
    K3W();
    issue(2);
    K3W();
    issue(3);
    K3W();
    WAITV(0);
    if (l == 0) __hip_atomic_fetch_add((LDS_AS u32*)((LDS_AS char*)smem + K3_CNT), 1u, __ATOMIC_RELAXED, __HIP_MEMORY_SCOPE_WORKGROUP);
    __builtin_amdgcn_s_waitcnt(0xC07F);
  } else {
    const int ga = w < 5, ct = ga ? w : w - 5;
    const u32 oa = g * 5120 + l15 * 80, wa = K3_W + ct * 5120 + l * 16;
    f32x4 acc[4] = {z4, z4, z4, z4};
    auto quarter = [&](int Q) {
      u32x4 of[5][4], wf[5];
#pragma unroll
      for (int wv = 0; wv < 5; ++wv) {
        wf[wv] = ldsr128(smem, wa + (Q == 3 ? 0 : Q) * 25600 + wv * 1024);
        if (wv < 3) {
#pragma unroll
          for (int rt = 0; rt < 4; ++rt) of[wv][rt] = ldsr128(smem, oa + Q * 20480 + rt * 1280 + wv * 16);
        }
      }
      SB();
#pragma unroll
      for (int wv = 0; wv < 2; ++wv)
#pragma unroll
        for (int rt = 0; rt < 4; ++rt) {
          acc[rt] = mfma(wf[wv], of[wv][rt], acc[rt]);
          SB();
          of[3 + wv][rt] = ldsr128(smem, oa + Q * 20480 + rt * 1280 + (3 + wv) * 16);
          SB();
        }
#pragma unroll
      for (int wv = 2; wv < 5; ++wv)
#pragma unroll
        for (int rt = 0; rt < 4; ++rt) acc[rt] = mfma(wf[wv], of[wv][rt], acc[rt]);
      SB();
    };
    if (!ga) {
      __builtin_amdgcn_s_barrier();
      __builtin_amdgcn_s_barrier();
      STAMP();
      quarter(0);
      RAWBAR();
      __builtin_amdgcn_s_barrier();
      STAMP();
      quarter(2);
#pragma unroll
      for (int rt = 0; rt < 4; ++rt) *(LDS_AS f32x4*)((LDS_AS char*)smem + (ct * 4 + rt) * 1024 + l * 16) = acc[rt];
      __builtin_amdgcn_s_waitcnt(0xC07F);
      STAMP();
    } else {
      u32 gv[4];
      const char* gp = ws + OFF_G + (64 * n2 + l15) * 4;
      GLD1(gv[0], gp, 0); GLD1(gv[1], gp, 64); GLD1(gv[2], gp, 128); GLD1(gv[3], gp, 192);
      u32x4 kd[3];
      const int a = 2 * ct + (l15 >> 3), j = l15 & 7, d = 64 * a + 8 * bg + j;
#pragma unroll
      for (int s = 0; s < 3; ++s) {
        int kc = 4 * s + g, c = kc >> 1;
        float rev = c < 4 ? (float)((c + 1) * d) * (1.f / 640.f) : 0.f;
        float v0 = __builtin_amdgcn_cosf(rev), v1 = c < 4 ? __builtin_amdgcn_sinf(rev) : ((a & 1) ? -1.f : 1.f);
        bool on = kc < 10 && (kc & 1) == (j >> 2);
        u32 pp = on ? pk2(v0, v1) : 0u;
        u32 p0 = (pp & 0xffffu) << ((j & 1) * 16), p1 = (pp >> 16) << ((j & 1) * 16);
        kd[s] = (j & 2) ? u32x4{0u, p0, 0u, p1} : u32x4{p0, 0u, p1, 0u};
        PIN(kd[s]);
      }
      u32 ao = K3_AP + ((l15 >> 1) & 1) * 256 + (l15 >> 2) * 64 + (l15 & 1) * 16 + (g & 1) * 32, ao1 = ao + (g >> 1) * 2048;
      u32 oo = ((32 * l15 + n2) * 640 + 64 * (2 * ct + (g >> 1)) + 8 * bg + 4 * (g & 1)) * 2;
      PIN(ao); PIN(ao1); PIN(oo);
      STAMP();
      __builtin_amdgcn_s_barrier();
      asm volatile("" ::: "memory");
      u32x4 af[4][3], gs[8];
#pragma unroll
      for (int rt = 0; rt < 4; ++rt) { af[rt][0] = ldsr128(smem, ao1 + rt * 512); af[rt][1] = ldsr128(smem, ao1 + 4096 + rt * 512); af[rt][2] = ldsr128(smem, ao + 8192 + rt * 512); }
#pragma unroll
      for (int f = 0; f < 8; ++f) gs[f] = ldsr128(smem, K3_GS + f * 1024 + l * 16);
      SB();
      f32x4 P[4];
#pragma unroll
      for (int s = 0; s < 3; ++s)
#pragma unroll
        for (int rt = 0; rt < 4; ++rt) P[rt] = mfma(af[rt][s], kd[s], s ? P[rt] : z4);
      u32x4 p0 = pkfrag(P[0], P[1]), p1 = pkfrag(P[2], P[3]);
      SB();
      RAWBAR();
      f32x4 yb[4];
#pragma unroll
      for (int nt = 0; nt < 4; ++nt) yb[nt] = mfma(p0, gs[2 * nt], z4);
#pragma unroll
      for (int nt = 0; nt < 4; ++nt) yb[nt] = mfma(p1, gs[2 * nt + 1], yb[nt]);
      asm volatile("s_waitcnt vmcnt(0)" : "+v"(gv[0]), "+v"(gv[1]), "+v"(gv[2]), "+v"(gv[3]) :: "memory");
      u32 yp[4][2];
#pragma unroll
      for (int nt = 0; nt < 4; ++nt) {
        const float hh = bfr(1.f - __uint_as_float(gv[nt]));
        u32 b0 = pk2(yb[nt][0], yb[nt][1]), b1 = pk2(yb[nt][2], yb[nt][3]);
        yp[nt][0] = pk2(hh * lo2f(b0), hh * hi2f(b0)); yp[nt][1] = pk2(hh * lo2f(b1), hh * hi2f(b1));
        PIN(yp[nt][0]); PIN(yp[nt][1]);
      }
      STAMP();
      __builtin_amdgcn_s_barrier();
      asm volatile("" ::: "memory");
      STAMP();
      quarter(1);
      __builtin_amdgcn_s_barrier();
      while (__builtin_amdgcn_readfirstlane(__hip_atomic_load((LDS_AS u32*)((LDS_AS char*)smem + K3_CNT), __ATOMIC_RELAXED, __HIP_MEMORY_SCOPE_WORKGROUP)) < 6u) __builtin_amdgcn_s_sleep(1);
      asm volatile("" ::: "memory");
      STAMP();
      quarter(3);
      __builtin_amdgcn_s_barrier();
      asm volatile("" ::: "memory");
      f32x4 pb[4];
#pragma unroll
      for (int rt = 0; rt < 4; ++rt) pb[rt] = *(const LDS_AS f32x4*)((const LDS_AS char*)smem + (ct * 4 + rt) * 1024 + l * 16);
      STAMP();
      u32 a0[4], a1[4];
#pragma unroll
      for (int rt = 0; rt < 4; ++rt) { f32x4 t = acc[rt] + pb[rt]; a0[rt] = pk2(t[0], t[1]); a1[rt] = pk2(t[2], t[3]); }
      SB();
#pragma unroll
      for (int rt = 0; rt < 4; ++rt) {
        const float gg = __uint_as_float(gv[rt]);
        a0[rt] = pk2(gg * lo2f(a0[rt]), gg * hi2f(a0[rt])); a1[rt] = pk2(gg * lo2f(a1[rt]), gg * hi2f(a1[rt]));
      }
      SB();
#pragma unroll
      for (int rt = 0; rt < 4; ++rt) {
        u32x2 o = {pk2(lo2f(a0[rt]) + lo2f(yp[rt][0]), hi2f(a0[rt]) + hi2f(yp[rt][0])), pk2(lo2f(a1[rt]) + lo2f(yp[rt][1]), hi2f(a1[rt]) + hi2f(yp[rt][1]))};
        *(u32x2*)((char*)out + (size_t)(oo + rt * (512 * 1280))) = o;
      }
      STAMP();
    }
  }
  STAMP_FLUSH(2);
}

__global__ void kend() {}

extern "C" void kernel_launch(void* const* d_in, const int* in_sizes, int n_in, void* d_out, int out_size, void* d_ws,
                              size_t ws_size, hipStream_t stream) {
  const u16* x = (const u16*)d_in[0];
  const u16* wqkv = (const u16*)d_in[1];
  const u16* wo = (const u16*)d_in[2];
  const u16* wr = (const u16*)d_in[4];
  const u16* wi = (const u16*)d_in[5];
  const u16* gate = (const u16*)d_in[6];
  char* ws = (char*)d_ws;
  (void)hipFuncSetAttribute((const void*)k1, hipFuncAttributeMaxDynamicSharedMemorySize, K1_LDS);
  (void)hipFuncSetAttribute((const void*)k2, hipFuncAttributeMaxDynamicSharedMemorySize, K2_LDS);
  (void)hipFuncSetAttribute((const void*)k3, hipFuncAttributeMaxDynamicSharedMemorySize, K3_LDS);
  k1<<<256, 1024, K1_LDS, stream>>>(x, wqkv, wr, wi, gate, wo, ws);
  k2<<<K2_NF + K2_NO, 1024, K2_LDS, stream>>>(wr, wi, ws);
  k3<<<256, 1024, K3_LDS, stream>>>(ws, (u16*)d_out);
}
```

```cpp
#include <hip/hip_runtime.h>
#include <stdint.h>

typedef unsigned short u16;
typedef unsigned int u32;
typedef short s16x4 __attribute__((ext_vector_type(4)));
typedef short s16x8 __attribute__((ext_vector_type(8)));
typedef __bf16 bf16x8 __attribute__((ext_vector_type(8)));
typedef __bf16 bf16x2 __attribute__((ext_vector_type(2)));
typedef float f32x4 __attribute__((ext_vector_type(4)));
typedef float f32x2 __attribute__((ext_vector_type(2)));
typedef u32 u32x4 __attribute__((ext_vector_type(4)));
typedef u32 u32x2 __attribute__((ext_vector_type(2)));

#define LDS_AS __attribute__((address_space(3)))
#define GLB_AS __attribute__((address_space(1)))
#define DI __device__ __forceinline__
#define STAMP_DECL
#define STAMP()
#define STAMP_FLUSH(K)
__device__ __forceinline__ void dma1(u32 lds, const void* base, u32 v0) {
  u32 keep;
  asm volatile("s_mov_b32 %0, m0\n\ts_mov_b32 m0, %1\n\ts_nop 4\n\tglobal_load_lds_dwordx4 %2, %3\n\ts_mov_b32 m0, %0"
               : "=&s"(keep) : "s"(lds), "v"(v0), "s"(base) : "memory");
}
__device__ __forceinline__ void dma4(u32 lds, const void* base, u32 v0, u32 v1, u32 v2, u32 v3) {
  u32 keep;
  asm volatile("s_mov_b32 %0, m0\n\ts_mov_b32 m0, %1\n\ts_nop 4\n\tglobal_load_lds_dwordx4 %2, %6\n\t"
               "global_load_lds_dwordx4 %3, %6 offset:1024\n\tglobal_load_lds_dwordx4 %4, %6 offset:2048\n\t"
               "global_load_lds_dwordx4 %5, %6 offset:3072\n\ts_mov_b32 m0, %0"
               : "=&s"(keep) : "s"(lds), "v"(v0), "v"(v1), "v"(v2), "v"(v3), "s"(base) : "memory");
}
__device__ __forceinline__ void dma2(u32 lds, const void* base, u32 v0, u32 v1) {
  u32 keep;
  asm volatile("s_mov_b32 %0, m0\n\ts_mov_b32 m0, %1\n\ts_nop 4\n\tglobal_load_lds_dwordx4 %2, %4\n\t"
               "global_load_lds_dwordx4 %3, %4 offset:1024\n\ts_mov_b32 m0, %0"
               : "=&s"(keep) : "s"(lds), "v"(v0), "v"(v1), "s"(base) : "memory");
}
__device__ __forceinline__ void dma1v(u32 lds, const void* src) {
  u32 keep;
  asm volatile("s_mov_b32 %0, m0\n\ts_mov_b32 m0, %1\n\ts_nop 0\n\tglobal_load_lds_dwordx4 %2, off\n\ts_mov_b32 m0, %0"
               : "=&s"(keep) : "s"(lds), "v"(src) : "memory");
}
#define GLD(dst, p, imm) asm volatile("global_load_dwordx4 %0, %1, off offset:" #imm : "=v"(dst) : "v"(p) : "memory")
#define GLD1(dst, p, imm) asm volatile("global_load_dword %0, %1, off offset:" #imm : "=v"(dst) : "v"(p) : "memory")
#define GLDH(dst, p) asm volatile("global_load_ushort %0, %1, off" : "=v"(dst) : "v"(p) : "memory")
#define LDSADDR(p) ((u32)(uintptr_t)(LDS_AS char*)(p))
#define SB() __builtin_amdgcn_sched_barrier(0)
#define WAITV(n) asm volatile("s_waitcnt vmcnt(" #n ")" ::: "memory")
#define RAWBAR() do { asm volatile("" ::: "memory"); __builtin_amdgcn_s_waitcnt(0xC07F); __builtin_amdgcn_s_barrier(); asm volatile("" ::: "memory"); } while (0)

constexpr size_t OFF_PQ  = 0;
constexpr size_t OFF_O2  = OFF_PQ + 2621440;
constexpr size_t OFF_KVP = OFF_O2 + 2621440;
constexpr size_t OFF_VB  = OFF_KVP + 860160;
constexpr int    VB_TS   = 10368;
constexpr size_t OFF_AP  = OFF_VB + (size_t)256 * VB_TS;
constexpr int    AP_TS   = 10240;
constexpr size_t OFF_WOF = OFF_AP + (size_t)256 * AP_TS;
constexpr size_t OFF_G   = OFF_WOF + 819200;

constexpr double kPi = 3.14159265358979323846;
constexpr double tcos(double x) {
  double x2 = x * x, term = 1.0, s = 1.0;
  for (int i = 1; i < 14; ++i) { term *= -x2 / ((2 * i - 1) * (2 * i)); s += term; }
  return s;
}
constexpr double cos64(int k) {
  k &= 63; if (k > 32) k = 64 - k;
  if (k == 16) return 0.0;
  return (k < 16) ? tcos(2 * kPi * k / 64) : -tcos(2 * kPi * (32 - k) / 64);
}
constexpr u16 tobf(double v) {
  u32 u = __builtin_bit_cast(u32, (float)v);
  return (u16)((u + 0x7fffu + ((u >> 16) & 1u)) >> 16);
}
struct C64 { u16 c[64]; };
constexpr C64 mkC64() { C64 t{}; for (int i = 0; i < 64; ++i) t.c[i] = tobf(cos64(i)); return t; }
constexpr C64 kC64 = mkC64();
constexpr u16 gsv(int qp, int n1) {
  int i = qp >> 1;
  if (qp == 1) return kC64.c[(32 * n1) & 63];
  return (qp & 1) ? kC64.c[(i * n1 + 48) & 63] : kC64.c[(i * n1) & 63];
}
constexpr int kperm(int s2, int g, int j) { return 32 * s2 + (j < 4 ? 4 * g + j : 12 + 4 * g + j); }
struct T16 { u16 v[4096]; };
struct T32 { u32 v[4096]; };
constexpr T16 mkGsA() { T16 t{};
  for (int f = 0; f < 8; ++f) for (int l = 0; l < 64; ++l) for (int j = 0; j < 8; ++j)
    t.v[(f * 64 + l) * 8 + j] = gsv(16 * (f >> 1) + (l & 15), kperm(f & 1, l >> 4, j));
  return t; }
constexpr T16 mkGsB() { T16 t{};
  for (int f = 0; f < 8; ++f) for (int l = 0; l < 64; ++l) for (int j = 0; j < 8; ++j)
    t.v[(f * 64 + l) * 8 + j] = gsv(kperm(f & 1, l >> 4, j), 16 * (f >> 1) + (l & 15));
  return t; }
constexpr T32 mkF64() { T32 t{};
  for (int f = 0; f < 16; ++f) for (int l = 0; l < 64; ++l) for (int j = 0; j < 4; ++j) {
    int u = 16 * (f >> 2) + (l & 15), v = 4 * (4 * (f & 3) + (l >> 4)) + j;
    t.v[(f * 64 + l) * 4 + j] = (u32)kC64.c[(u * v) & 63] | ((u32)kC64.c[(u * v + 16) & 63] << 16);
  }
  return t; }
__device__ __attribute__((aligned(128))) const T16 dGsA = mkGsA();
__device__ __attribute__((aligned(128))) const T16 dGsB = mkGsB();
__device__ __attribute__((aligned(128))) const T32 dF64 = mkF64();

DI f32x4 mfma(u32x4 a, u32x4 b, f32x4 c) {
  return __builtin_amdgcn_mfma_f32_16x16x32_bf16(__builtin_bit_cast(bf16x8, a), __builtin_bit_cast(bf16x8, b), c, 0, 0, 0);
}
DI u32 pk2(float lo, float hi) { f32x2 v = {lo, hi}; return __builtin_bit_cast(u32, __builtin_convertvector(v, bf16x2)); }
DI float lo2f(u32 p) { return __uint_as_float(p << 16); }
DI float hi2f(u32 p) { return __uint_as_float(p & 0xffff0000u); }
DI float bfr(float x) { return lo2f(pk2(x, x)); }
DI u32x4 pkfrag(f32x4 a, f32x4 b) { u32x4 r = {pk2(a[0], a[1]), pk2(a[2], a[3]), pk2(b[0], b[1]), pk2(b[2], b[3])}; return r; }
DI float ror8(float v) { return __int_as_float(__builtin_amdgcn_update_dpp(0, __float_as_int(v), 0x128, 0xf, 0xf, true)); }
DI u32x4 ldsr128(const char* smem, u32 off) { return *(const LDS_AS u32x4*)((const LDS_AS char*)smem + off); }
DI u32x2 ldstr(const char* smem, u32 off) {
  return __builtin_bit_cast(u32x2, __builtin_amdgcn_ds_read_tr16_b64_v4i16((LDS_AS s16x4*)((LDS_AS char*)smem + off)));
}
DI void st16wt(void* p, u32x4 v) { asm volatile("global_store_dwordx4 %0, %1, off sc1\n\ts_nop 1" :: "v"(p), "v"(v) : "memory"); }
DI void st4wt(void* p, u32 v) { asm volatile("global_store_dword %0, %1, off sc1" :: "v"(p), "v"(v) : "memory"); }
DI void l2touch(u32 lds, const void* p) {
  u32 keep;
  asm volatile("s_mov_b32 %0, m0\n\ts_mov_b32 m0, %1\n\ts_nop 0\n\tglobal_load_lds_dword %2, off\n\ts_mov_b32 m0, %0"
               : "=&s"(keep) : "s"(lds), "v"(p) : "memory");
}
DI void st16p(char* b, u32 o, f32x4 v) { *(f32x4*)(b + (size_t)o) = v; }
DI void st16s(const void* b, u32 o, u32x4 v) { asm volatile("global_store_dwordx4 %0, %1, %2 sc1\n\ts_nop 1" :: "v"(o), "v"(v), "s"(b) : "memory"); }
#define ST16SO(b, o, imm, v) asm volatile("global_store_dwordx4 %0, %1, %2 offset:" #imm " sc1\n\ts_nop 1" :: "v"(o), "v"(v), "s"(b) : "memory")
#define ST4SO(b, o, imm, v) asm volatile("global_store_dword %0, %1, %2 offset:%3 sc1" :: "v"(o), "v"(v), "s"(b), "i"(imm) : "memory")
#define ST4O(p, imm, v) asm volatile("global_store_dword %0, %1, off offset:%2 sc1" :: "v"(p), "v"(v), "i"(imm) : "memory")
DI void ldsw128(char* smem, u32 off, u32x4 v) { *(LDS_AS u32x4*)((LDS_AS char*)smem + off) = v; }
DI void ldsw64(char* smem, u32 off, u32x2 v) { *(LDS_AS u32x2*)((LDS_AS char*)smem + off) = v; }
DI u32x2 ldsr64(const char* smem, u32 off) { return *(const LDS_AS u32x2*)((const LDS_AS char*)smem + off); }
DI u32 ldsr16(const char* smem, u32 off) { return *(const LDS_AS u16*)((const LDS_AS char*)smem + off); }
template <int N> DI void phi4(u32x4 (&p)[N]) {
  f32x2 t[4 * N], e[4 * N];
#pragma unroll
  for (int i = 0; i < 4 * N; ++i) t[i] = f32x2{lo2f(p[i >> 2][i & 3]), hi2f(p[i >> 2][i & 3])};
  SB();
#pragma unroll
  for (int i = 0; i < 4 * N; ++i) e[i] = t[i] * 1.44269504088896f;
  SB();
#pragma unroll
  for (int i = 0; i < 4 * N; ++i) {
    e[i][0] = __builtin_amdgcn_fmed3f(__builtin_amdgcn_exp2f(e[i][0]), 0.f, 1.f); e[i][1] = __builtin_amdgcn_fmed3f(__builtin_amdgcn_exp2f(e[i][1]), 0.f, 1.f);
    t[i][0] = __builtin_fmaxf(t[i][0], 0.f); t[i][1] = __builtin_fmaxf(t[i][1], 0.f);
  }
  SB();
  float mn = 1.f;
#pragma unroll
  for (int i = 0; i < 4 * N; ++i) { mn = __builtin_fminf(__builtin_fminf(mn, e[i][0]), e[i][1]); e[i] += t[i]; }
  SB();
#pragma unroll
  for (int i = 0; i < 4 * N; ++i) p[i >> 2][i & 3] = pk2(e[i][0], e[i][1]);
  if (__builtin_amdgcn_ballot_w64(mn < 0.001f)) {
#pragma unroll
    for (int n = 0; n < N; ++n)
#pragma unroll
      for (int i = 0; i < 4; ++i) p[n][i] = pk2(lo2f(p[n][i]) + 1e-6f, hi2f(p[n][i]) + 1e-6f);
  }
}
template <int CTRL> DI float dppf(float v) { return __int_as_float(__builtin_amdgcn_update_dpp(0, __float_as_int(v), CTRL, 0xf, 0xf, true)); }
DI u32x4 onehot(u32 s, int d) { u32x4 r = {d == 0 ? s : 0u, d == 1 ? s : 0u, d == 2 ? s : 0u, d == 3 ? s : 0u}; return r; }

constexpr int K1_SLOT = 16384, K1_B = 65536, K1_AUX = 131072, K1_GS = K1_AUX + 272, K1_LDS = K1_GS + 8192;

struct Frags { u32x4 x[2][2], w[2][4]; };
#define PIN(v) asm volatile("" : "+v"(v))

template <bool QT>
DI void k1_gemm(char* smem, const u16* x, const u16* gate, char* ws, int m, int nt, int w, int l
                ) {
  const int g = l >> 4, l15 = l & 15, rg = w & 3, chh = w >> 2;
  {
    const int r0 = 8 * nt + w, lc = l < 40 ? l : 39;
    const u32x4* gx = (const u32x4*)(x + (32 * (r0 & 63) + 2 * m + (r0 >> 6)) * 640) + lc;
    const u32x4 gxv[2] = {gx[0], gx[40]}, gwv[2] = {((const u32x4*)gate)[lc], ((const u32x4*)gate)[lc + 40]};
    float g0 = 0.f, g1 = 0.f;
#pragma unroll
    for (int i = 0; i < 8; ++i) {
      const u32 xv = gxv[i >> 2][i & 3], wv = gwv[i >> 2][i & 3];
      u32 pr = pk2(lo2f(xv) * lo2f(wv), hi2f(xv) * hi2f(wv));
      g0 += lo2f(pr); g1 += hi2f(pr);
    }
    g0 = l < 40 ? g0 + g1 : 0.f;
    g0 += dppf<0xB1>(g0); g0 += dppf<0x4E>(g0); g0 += dppf<0x124>(g0); g0 += dppf<0x128>(g0);
    float tot = __uint_as_float(__builtin_amdgcn_readlane(__float_as_uint(g0), 0)) + __uint_as_float(__builtin_amdgcn_readlane(__float_as_uint(g0), 16))
              + __uint_as_float(__builtin_amdgcn_readlane(__float_as_uint(g0), 32)) + __uint_as_float(__builtin_amdgcn_readlane(__float_as_uint(g0), 48));
    float gt = bfr(__builtin_amdgcn_rcpf(1.f + __builtin_amdgcn_exp2f(bfr(tot) * -1.44269504088896f)));
    if (l == 0) ((float*)(ws + OFF_G))[128 * m + r0] = gt;
  }
  u32 xa[2], wa[4][2];
  xa[0] = rg * 4096 + l15 * 128 + ((g ^ ((l15 >> 1) & 7)) << 4); xa[1] = xa[0] ^ 64;
#pragma unroll
  for (int t2 = 0; t2 < 2; ++t2) {
    const int q4 = l15 >> 2, swz = (q4 << 2) | ((2 * g + t2) & 3);
    const u32 b0 = K1_B + (8 * g + 4 * t2 + q4) * 256 + (((((l >> 1) & 1) | ((QT ? 8 : 4) * chh)) ^ swz) << 4) + 8 * (l & 1);
#pragma unroll
    for (int i = 0; i < 4; ++i) { wa[i][t2] = b0 ^ ((QT ? i : (i & 1) + 4 * (i >> 1)) << 5); PIN(wa[i][t2]); }
  }
  f32x4 acc[2][4];
#pragma unroll
  for (int rt = 0; rt < 2; ++rt)
#pragma unroll
    for (int i = 0; i < 4; ++i) acc[rt][i] = f32x4{0.f, 0.f, 0.f, 0.f};
  Frags F[2];
  auto rd = [&](Frags& f, int C) {
    const int so = (C & 3) * K1_SLOT;
#pragma unroll
    for (int ks = 0; ks < 2; ++ks) {
#pragma unroll
      for (int rt = 0; rt < 2; ++rt) f.x[ks][rt] = ldsr128(smem, xa[ks] + so + rt * 2048);
#pragma unroll
      for (int i = 0; i < 4; ++i) {
        u32x2 a = ldstr(smem, wa[i][0] + so + ks * 8192), b = ldstr(smem, wa[i][1] + so + ks * 8192);
        f.w[ks][i] = u32x4{a[0], a[1], b[0], b[1]};
      }
    }
  };
  auto mm = [&](const Frags& f) {
#pragma unroll
    for (int ks = 0; ks < 2; ++ks)
#pragma unroll
      for (int i = 0; i < 4; ++i)
#pragma unroll
        for (int rt = 0; rt < 2; ++rt)
          acc[rt][i] = QT ? mfma(f.w[ks][i], f.x[ks][rt], acc[rt][i]) : mfma(f.x[ks][rt], f.w[ks][i], acc[rt][i]);
  };
  STAMP();
  RAWBAR();
  STAMP();
  rd(F[0], 0);
  SB();
  u32 eo[2], ra[2], rb[2];
  if (QT) {
    const int ck = 16 * (nt - 11) + 8 * chh + (l & 7), hp = (ck * 205) >> 10;
    eo[0] = (hp * 2048 + 128 * m + 32 * rg + (l >> 3)) * 80 + (ck - 5 * hp) * 16;
    ra[0] = 2 * K1_SLOT + chh * K1_B + rg * 4608 + l15 * 144 + g * 8;
    rb[0] = 2 * K1_SLOT + chh * K1_B + rg * 4608 + (l >> 3) * 144 + (l & 7) * 16;
    PIN(eo[0]); PIN(ra[0]); PIN(rb[0]);
  } else {
#pragma unroll
    for (int k = 0; k < 2; ++k) {
      int P = w + 8 * k;
      int pi = (0x103322211100ull >> (4 * P)) & 15, pj = (0x333232121010ull >> (4 * P)) & 15;
      int d = 16 * pi + 4 * g, e = 16 * pj + l15, hd = (d * 205) >> 12, he = (e * 205) >> 12;
      int h = 3 * nt + hd;
      bool ok = P < 12 && d < 60 && h < 32 && (e == 60 || (e < 60 && he == hd));
      int ee = e == 60 ? 20 : e - 20 * he;
      eo[k] = ok ? (((m * 32 + h) * 21 + ee) * 20 + (d - 20 * hd)) * 4 : ~0u;
      ra[k] = 2 * K1_SLOT + pi * 1024 + l * 16; rb[k] = 2 * K1_SLOT + (4 + pj) * 1024 + l * 16;
      PIN(eo[k]); PIN(ra[k]); PIN(rb[k]);
    }
  }
  SB();
#pragma unroll
  for (int C = 0; C < 10; ++C) {
    Frags& cur = F[C & 1]; Frags& nxt = F[(C + 1) & 1];
    if (C == 8) { mm(cur); SB(); }
    if (C < 9) RAWBAR();
    SB();
    if (C < 9) rd(nxt, C + 1);
    if (C != 8) mm(cur);
    if (C < 8) {
#pragma unroll
      for (int i = 0; i < 4; ++i) {
        __builtin_amdgcn_sched_group_barrier(0x008, 1, 0); __builtin_amdgcn_sched_group_barrier(0x100, 2, 0);
#pragma unroll
        for (int k = 0; k < 3; ++k) { __builtin_amdgcn_sched_group_barrier(0x008, 1, 0); __builtin_amdgcn_sched_group_barrier(0x100, 1, 0); }
      }
    }
    SB();
  }
  STAMP();
  if (QT) {
    u32x4 pa[4];
#pragma unroll
    for (int i = 0; i < 4; ++i) pa[i] = pkfrag(acc[0][i], acc[1][i]);
    SB();
    phi4(pa);
#pragma unroll
    for (int i = 0; i < 4; ++i) { ldsw64(smem, ra[0] + i * 32, u32x2{pa[i][0], pa[i][1]}); ldsw64(smem, ra[0] + 2304 + i * 32, u32x2{pa[i][2], pa[i][3]}); }
    u32x4 o[4];
#pragma unroll
    for (int i = 0; i < 4; ++i) o[i] = ldsr128(smem, rb[0] + i * 1152);
#pragma unroll
    for (int i = 0; i < 4; ++i) *(u32x4*)(ws + OFF_PQ + (size_t)(eo[0] + i * 640)) = o[i];
  } else {
    u32x4 v1 = pkfrag(acc[0][3], acc[1][3]);
    if (chh && l15 == 12) v1 = u32x4{0x3f803f80u, 0x3f803f80u, 0x3f803f80u, 0x3f803f80u};
    const u32 fo = 2 * K1_SLOT + (rg * 8 + 2 * chh) * 1024 + l * 16;
    ldsw128(smem, fo + 4096, pkfrag(acc[0][2], acc[1][2]));
    ldsw128(smem, fo + 5120, v1);
    u32x4 pa[2] = {pkfrag(acc[0][0], acc[1][0]), pkfrag(acc[0][1], acc[1][1])};
    phi4(pa);
    ldsw128(smem, fo, pa[0]);
    ldsw128(smem, fo + 1024, pa[1]);
    STAMP();
    __builtin_amdgcn_s_waitcnt(0xC07F);
    LDS_AS u32* cnt = (LDS_AS u32*)((LDS_AS char*)smem + K1_AUX);
    if (l == 0) __hip_atomic_fetch_add(cnt, 1u, __ATOMIC_RELAXED, __HIP_MEMORY_SCOPE_WORKGROUP);
    while (__builtin_amdgcn_readfirstlane(__hip_atomic_load(cnt, __ATOMIC_RELAXED, __HIP_MEMORY_SCOPE_WORKGROUP)) < 8u) {}
    asm volatile("" ::: "memory");
    STAMP();
    u32x4 fa[4], fb[4];
    f32x4 r0 = {0.f, 0.f, 0.f, 0.f};
#pragma unroll
    for (int r = 0; r < 4; ++r) { fa[r] = ldsr128(smem, ra[0] + r * 8192); fb[r] = ldsr128(smem, rb[0] + r * 8192); }
    if (w < 4) {
      u32x4 fc[4], fd[4];
      f32x4 r1 = r0;
#pragma unroll
      for (int r = 0; r < 4; ++r) { fc[r] = ldsr128(smem, ra[1] + r * 8192); fd[r] = ldsr128(smem, rb[1] + r * 8192); }
      SB();
#pragma unroll
      for (int r = 0; r < 4; ++r) { r0 = mfma(fa[r], fb[r], r0); r1 = mfma(fc[r], fd[r], r1); }
      if (eo[1] != ~0u) st16p(ws + OFF_KVP, eo[1], r1);
    } else {
      SB();
#pragma unroll
      for (int r = 0; r < 4; ++r) r0 = mfma(fa[r], fb[r], r0);
    }
    if (eo[0] != ~0u) st16p(ws + OFF_KVP, eo[0], r0);
  }
}

template <bool C4>
DI void k1_dft(const char* vb, const u32x4 (&gs)[8], const u32x4 (&xq)[4][3], u32 vo, u32 k01, u32 k2, float tc, float ts, int l) {
  constexpr int NB = 1;
  const u32 kv01[1] = {k01}, kv2[1] = {k2};
  const int g = l >> 4, h = (l >> 3) & 1, j = l & 7;
  const u32 hm = (j & 1) ? 0xffff0000u : 0xffffu;
  const f32x4 z4 = {0.f, 0.f, 0.f, 0.f};
  u32x4 kd[NB][3], tb[NB][2];
  f32x4 t[NB][4], y[NB][4];
#pragma unroll
  for (int u = 0; u < NB; ++u) {
    kd[u][0] = onehot(((j & 1) ? kv01[u] << 16 : kv01[u]) & hm, j >> 1); kd[u][1] = onehot(((j & 1) ? kv01[u] : kv01[u] >> 16) & hm, j >> 1);
    kd[u][2] = onehot(kv2[u] & hm, j >> 1);
  }
  SB();
#pragma unroll
  for (int s = 0; s < 3; ++s)
#pragma unroll
    for (int u = 0; u < NB; ++u)
#pragma unroll
      for (int rt = 0; rt < 4; ++rt) t[u][rt] = mfma(xq[rt][s], kd[u][s], s ? t[u][rt] : z4);
#pragma unroll
  for (int u = 0; u < NB; ++u) { tb[u][0] = pkfrag(t[u][0], t[u][1]); tb[u][1] = pkfrag(t[u][2], t[u][3]); }
#pragma unroll
  for (int k = 0; k < 2; ++k)
#pragma unroll
    for (int u = 0; u < NB; ++u)
#pragma unroll
      for (int mt = 0; mt < 4; ++mt) y[u][mt] = mfma(gs[2 * mt + k], tb[u][k], k ? y[u][mt] : z4);
  SB();
#pragma unroll
  for (int u = 0; u < NB; ++u) {
    float X[8], Y[8]; u32 pk[8];
    const u32 vu = vo;
    if (!C4) {
#pragma unroll
      for (int i = 0; i < 8; ++i) { X[i] = y[u][i >> 1][2 * (i & 1)] - ror8(y[u][i >> 1][2 * (i & 1) + 1]); Y[i] = y[u][i >> 1][2 * (i & 1) + 1] + ror8(y[u][i >> 1][2 * (i & 1)]); }
      float p0 = ror8(y[u][0][0]), p1 = ror8(y[u][0][1]);
      X[0] = g == 0 ? (h ? y[u][0][1] : y[u][0][0]) : X[0]; Y[0] = g == 0 ? (h ? p1 : p0) : Y[0];
      SB();
#pragma unroll
      for (int i = 0; i < 8; ++i) pk[i] = pk2(X[i], Y[i]);
      SB();
#pragma unroll
      for (int i = 0; i < 8; ++i) pk[i] = __builtin_amdgcn_alignbit(pk[i], pk[i], 16 * h);
    } else {
      const float o1 = y[u][0][1];
      if (g == 0) ST4SO(vb, vu, 2048, pk2(o1 * tc, o1 * ts));
      y[u][0][1] = g == 0 ? 0.f : o1;
#pragma unroll
      for (int i = 0; i < 8; ++i) {
        float o0 = y[u][i >> 1][2 * (i & 1)], o2 = y[u][i >> 1][2 * (i & 1) + 1];
        X[i] = o0 * tc - o2 * ts; Y[i] = o0 * ts + o2 * tc;
      }
      SB();
#pragma unroll
      for (int i = 0; i < 8; ++i) pk[i] = pk2(X[i], Y[i]);
    }
    SB();
#pragma unroll
    for (int i = 0; i < 8; ++i) ST4SO(vb, vu, i * 256, pk[i]);
  }
}

__global__ __launch_bounds__(1024) __attribute__((amdgpu_waves_per_eu(4, 4)))
void k1(const u16* __restrict__ x, const u16* __restrict__ wqkv, const u16* __restrict__ wr, const u16* __restrict__ wi,
        const u16* __restrict__ gate, const u16* __restrict__ wo, char* __restrict__ ws, u16* __restrict__ out) {
  extern __shared__ __attribute__((aligned(16))) char smem[];
  const int tid = threadIdx.x, l = tid & 63, w = __builtin_amdgcn_readfirstlane(tid >> 6);
  const int b = blockIdx.x, xcd = b & 7, bi = b >> 3;
  const int m = (xcd >> 1) * 4 + (bi & 3), nt = (xcd & 1) * 8 + (bi >> 2);
  asm volatile("" :: "s"(x), "s"(wqkv), "s"(wr), "s"(wi), "s"(gate), "s"(wo), "s"(ws), "s"(out));
  STAMP_DECL; STAMP();
#define TSARG
  if (w == 7) {
    const char* pc; int d0, d1;
    asm volatile("s_getpc_b64 %0\n1:\n\ts_mov_b32 %1, _Z2k1PKtS0_S0_S0_S0_S0_PcPt-1b\n\ts_mov_b32 %2, _Z4kendv-1b" : "=s"(pc), "=s"(d0), "=s"(d1));
    const char* a = pc + d0 + (l * 32 + bi) * 128;
    if (l >= 12) a = (const char*)dGsA.v + (bi * 2 + (l & 1)) * 128;
    if (l < 14 && (l >= 12 || a < pc + d1)) l2touch(LDSADDR(smem) + K1_AUX + 16, a);
  }
  if (w >= 8) {
    const int v = w - 8, u = v >> 1;
    int cn = ((nt & 7) * 5 >> 2) + ((m & 3) * 5 >> 1); if (cn >= 10) cn -= 10;
    const int cq = nt >= 11, cbase = cq ? 128 * (nt - 11) : 640 + 60 * nt, cs = cq ? 64 : 640, cmax = nt == 10 ? 4 : 7;
    u32 da[4];
#pragma unroll
    for (int e = 0; e < 4; ++e) {
      const int cbk = (l & 15) ^ (((l >> 4) << 2) | e);
      da[e] = ((v & 1) ? (4 * e + (l >> 4)) * 3840 + ((cbk >> 3) * cs + 8 * min(cbk & 7, cmax)) * 2
                       : (8 * e + (l >> 3)) * 40960 + (((l & 7) ^ ((4 * e + (l >> 4)) & 7)) << 4)) - 1024 * e;
    }
    const char* sa = (v & 1) ? (const char*)wqkv + u * (16 * 3840) + cbase * 2 : (const char*)x + ((u & 1) * 1024 + 2 * m + (u >> 1)) * 1280;
    const int cstep = (v & 1) ? 64 * 3840 : 128;
    const u32 dl = LDSADDR(smem) + u * 4096 + (v & 1) * K1_B;
    int cc = cn;
    auto issue = [&](int slot) {
      dma4(dl + slot * K1_SLOT, sa + cn * cstep, da[0], da[1], da[2], da[3]);
      cn = cn == 9 ? 0 : cn + 1;
    };
    __builtin_amdgcn_s_setprio(2);
    issue(0); issue(1);
    STAMP();
    if (l == 0 && v == 0) *(LDS_AS u32*)((LDS_AS char*)smem + K1_AUX) = 0u;
    const int n2s = nt >> 3, bg = nt & 7, g = l >> 4;
    const u32 ca = n2s * 8192 + (l & 15) * 128 + ((bg ^ ((l >> 1) & 7)) << 4);
    u32x4 xq[4][3];
#pragma unroll
    for (int rt = 0; rt < 4; ++rt) xq[rt][0] = xq[rt][1] = xq[rt][2] = u32x4{0u, 0u, 0u, 0u};
    auto step = [&](int C) {
      RAWBAR();
      if (C < 7) issue((C + 3) & 3);
      {
        const u32 o = LDSADDR(smem) + ca + ((C + 1) & 3) * K1_SLOT;
        const unsigned long long mk = (v < 5 ? 0xffffull : 0ull) << (16 * (cc & 3));
        asm volatile("s_mov_b64 exec, %13\n\ts_cmp_lt_u32 %14, 4\n\ts_cbranch_scc0 1f\n\t"
                     "ds_read_b128 %0, %12\n\tds_read_b128 %1, %12 offset:2048\n\tds_read_b128 %2, %12 offset:4096\n\tds_read_b128 %3, %12 offset:6144\n\ts_branch 3f\n"
                     "1:\n\ts_cmp_lt_u32 %14, 8\n\ts_cbranch_scc0 2f\n\t"
                     "ds_read_b128 %4, %12\n\tds_read_b128 %5, %12 offset:2048\n\tds_read_b128 %6, %12 offset:4096\n\tds_read_b128 %7, %12 offset:6144\n\ts_branch 3f\n"
                     "2:\n\tds_read_b128 %8, %12\n\tds_read_b128 %9, %12 offset:2048\n\tds_read_b128 %10, %12 offset:4096\n\tds_read_b128 %11, %12 offset:6144\n"
                     "3:\n\ts_mov_b64 exec, -1"
                     : "+v"(xq[0][0]), "+v"(xq[1][0]), "+v"(xq[2][0]), "+v"(xq[3][0]), "+v"(xq[0][1]), "+v"(xq[1][1]), "+v"(xq[2][1]), "+v"(xq[3][1]),
                       "+v"(xq[0][2]), "+v"(xq[1][2]), "+v"(xq[2][2]), "+v"(xq[3][2]) : "v"(o), "s"(mk), "s"(cc) : "memory", "scc");
      }
      cc = cc == 9 ? 0 : cc + 1;
    };
#pragma unroll 1
    for (int C = -1; C < 2; ++C) { WAITV(4); step(C); }
    dma1(LDSADDR(smem) + K1_GS + v * 1024, (const char*)dGsA.v + v * 1024, l * 16);
    const int h = (l >> 3) & 1, bb = 8 * bg + (l & 7);
    u32 kv01, kv2;
    {
      float c3[3];
#pragma unroll
      for (int s = 0; s < 3; ++s) c3[s] = __builtin_amdgcn_cosf((float)((v + 1) * (64 * (4 * s + g) + bb)) * (1.f / 640.f) - 0.25f * h);
      kv01 = pk2(c3[0], c3[1]); kv2 = pk2(c3[2], c3[2]);
      if (v == 4) kv01 = kv2 = (h & g) ? 0xbf80bf80u : 0x3f803f80u;
    }
    float tc = h ? __builtin_amdgcn_cosf(bb * (1.f / 128.f)) : 1.f, ts = h ? __builtin_amdgcn_sinf(bb * (1.f / 128.f)) : 0.f;
    u32 vo = ((2 * m + n2s) * 8 + bg) * VB_TS + l * 4;
    PIN(kv01); PIN(kv2); PIN(tc); PIN(ts); PIN(vo);
    WAITV(5); step(2);
#pragma unroll 1
    for (int C = 3; C < 7; ++C) { WAITV(4); step(C); }
    STAMP();
    {
      const u32 dump = LDSADDR(smem) + K1_AUX + 16;
      const int ci = 8 * ((b & 7) >> 1) + 2 * (b >> 6) + (b & 1), q = ((b >> 5) & 1) ? (ci ? 64 - ci : 32) : ci;
      const u32 wo2 = ((q + 64 * (8 * ((b >> 3) & 3) + (l >> 3))) * 642 + min((l & 7) * 128, 638)) & ~3u;
      const char* ca2 = (const char*)dF64.v + (bi * 4 + (l & 3)) * 128;
      const char* pc = ca2;
      const int id = 25 * b + min((l & 7) * 4, 24), ko = (id * 52429) >> 22;
      const char* wo3 = (const char*)wo + (8 * ko + (l >> 3)) * 1280 + (id - 80 * ko) * 16;
      l2touch(dump, v == 6 ? (const char*)wr + wo2 : (v == 7 ? (const char*)wi + wo2 : (v == 5 ? wo3 : (v == 4 ? ca2
              : (v == 3 ? (const char*)out + l * 40960 : (v == 2 ? ws + OFF_O2 + l * 40960 : (v == 1 ? ws + OFF_AP + l * 40960 : pc)))))));
    }
    WAITV(5); step(7);
    WAITV(1); step(8);
    u32x4 gs[8];
#pragma unroll
    for (int f = 0; f < 8; ++f) gs[f] = ldsr128(smem, K1_GS + f * 1024 + l * 16);
    asm volatile("s_waitcnt lgkmcnt(0)" : "+v"(xq[0][0]), "+v"(xq[1][0]), "+v"(xq[2][0]), "+v"(xq[3][0]), "+v"(xq[0][1]), "+v"(xq[1][1]), "+v"(xq[2][1]), "+v"(xq[3][1]),
                 "+v"(xq[0][2]), "+v"(xq[1][2]), "+v"(xq[2][2]), "+v"(xq[3][2]) :: "memory");
    STAMP();
    if (v == 5) {
      const int id = 25 * b + min(l, 24), ko = (id * 52429) >> 22, cb = id - 80 * ko, a = cb >> 3, hp = (ko * 205) >> 10, wq = ko - 5 * hp;
#pragma unroll
      for (int i = 0; i < 8; ++i) gs[i] = *(const u32x4*)((const char*)wo + ko * 10240 + cb * 16 + i * 1280);
      u32x4* dst = (u32x4*)(ws + OFF_WOF) + ((((cb & 7) * 5 + (a >> 1)) * 20 + (hp >> 2) * 5 + wq) * 64 + (hp & 3) * 16 + (a & 1) * 8);
#pragma unroll
      for (int jj = 0; jj < 8; ++jj) {
        u32x4 o2;
#pragma unroll
        for (int d = 0; d < 4; ++d) o2[d] = __builtin_amdgcn_perm(gs[2 * d + 1][jj >> 1], gs[2 * d][jj >> 1], (jj & 1) ? 0x07060302u : 0x05040100u);
        dst[jj] = o2;
      }
    }
    asm volatile("" : "+v"(gs[0]), "+v"(gs[1]), "+v"(gs[2]), "+v"(gs[3]), "+v"(gs[4]), "+v"(gs[5]), "+v"(gs[6]), "+v"(gs[7]));
    if (v < 4) k1_dft<false>(ws + OFF_VB, gs, xq, vo + v * 2048, kv01, kv2, tc, ts, l);
    else if (v == 4) k1_dft<true>(ws + OFF_VB, gs, xq, vo + 8192, kv01, kv2, tc, ts, l);
  } else if (nt >= 11) k1_gemm<true>(smem, x, gate, ws, m, nt, w, l TSARG);
  else k1_gemm<false>(smem, x, gate, ws, m, nt, w, l TSARG);
  STAMP();
  STAMP_FLUSH(0);
}

constexpr int K2_V = 0, K2_A = 16384, K2_U = 32768, K2_US = 272, K2_PF = K2_U + 2 * 32 * K2_US, K2_W = K2_PF + 272, WROW = 656, K2_LDS = K2_W + 128 * WROW;
constexpr int K2_NF = 168;
constexpr int K2_NO = 64;

__global__ __launch_bounds__(1024) __attribute__((amdgpu_waves_per_eu(4, 4)))
void k2(const u16* __restrict__ wr, const u16* __restrict__ wi, char* __restrict__ ws) {
  extern __shared__ __attribute__((aligned(16))) char smem[];
  const int tid = threadIdx.x, l = tid & 63, w8 = __builtin_amdgcn_readfirstlane(tid >> 6);
  const int g = l >> 4, l15 = l & 15;
  const int b = blockIdx.x;
  const f32x4 z4 = {0.f, 0.f, 0.f, 0.f};
  asm volatile("" :: "s"(wr), "s"(wi), "s"(ws));
  auto prefetch = [&]() {
    const int i = (b >> 3) * 224 + tid;
    if (tid < 224 && i < 6400) l2touch(LDSADDR(smem) + K2_PF, ws + OFF_WOF + (size_t)i * 128);
    if (tid >= 448 && tid < 451 && (b >> 3) * 3 + tid - 448 < 64) l2touch(LDSADDR(smem) + K2_PF, (const char*)dGsB.v + ((b >> 3) * 3 + tid - 448) * 128);
  };
  STAMP_DECL; STAMP();
  if (b < K2_NF) {
    const int sidx = (b >> 5) * 32 + (b & 7) * 4 + ((b >> 3) & 3);
    if (sidx > 160) { prefetch(); return; }
    const int c = sidx >> 5 > 4 ? 4 : sidx >> 5;
    const int ci = sidx == 160 ? 32 : ((sidx >> 3) & 3) * 8 + (sidx & 3) * 2 + ((sidx >> 2) & 1);
    const int qa = ci, qb = (c < 4 && ci == 0) ? 32 : (64 - ci) & 63;
    LDS_AS u32* cnt = (LDS_AS u32*)((LDS_AS char*)smem + K2_PF + 256);
    if (tid == 0) *cnt = 0u;
    RAWBAR();
    if (w8 >= 8) {
      const int v = w8 - 8;
      u32 vo[11];
#pragma unroll
      for (int i = 0; i < 11; ++i) { const int P = 64 * i + l; vo[i] = (P - 64 * (i & 3)) * 16 + ((P * 1599) >> 16) * (41088 - 656); }
      const char* wsrc = (const char*)((v >> 2) ? wi : wr) + (((((v >> 1) & 1) ? qb : qa) * 642) & ~15) + (v & 1) * (16 * 41088);
      const u32 L = LDSADDR(smem) + K2_W + v * 16 * WROW;
      {
      const int half = v >> 2, w4 = v & 3, q = c == 4 ? ci : (half == 0 ? ci : (ci == 0 ? 32 : 64 - ci));
    {
      const int ri = 16 * (w4 & 1) + l15;
      const int B = w4 < 2 ? q + 64 * ri : 64 * ri, A = (w4 < 2 ? 0 : ri * q) + B * 4 * g;
      float cs[8], sn[8];
#pragma unroll
      for (int j = 0; j < 8; ++j) {
        float rev = (float)((A + B * (j < 4 ? j : 12 + j)) & 2047) * (1.f / 2048.f);
        cs[j] = __builtin_amdgcn_cosf(rev); sn[j] = __builtin_amdgcn_sinf(rev);
      }
      ldsw128(smem, K2_A + v * 2048 + l * 16, u32x4{pk2(cs[0], cs[1]), pk2(cs[2], cs[3]), pk2(cs[4], cs[5]), pk2(cs[6], cs[7])});
      ldsw128(smem, K2_A + v * 2048 + 1024 + l * 16, u32x4{pk2(sn[0], sn[1]), pk2(sn[2], sn[3]), pk2(sn[4], sn[5]), pk2(sn[6], sn[7])});
    }
      __builtin_amdgcn_s_waitcnt(0xC07F);
      if (l == 0) __hip_atomic_fetch_add(cnt, 1u, __ATOMIC_RELAXED, __HIP_MEMORY_SCOPE_WORKGROUP);
      }
      dma4(L, wsrc, vo[0], vo[1], vo[2], vo[3]); dma4(L + 4096, wsrc, vo[4], vo[5], vo[6], vo[7]); dma2(L + 8192, wsrc, vo[8], vo[9]);
      if (l < 16) dma1(L + 10240, wsrc + 2048, vo[10]);
      WAITV(0);
      STAMP_FLUSH(1);
      return;
    }
    const int half = w8 >> 2, w4 = w8 & 3;
    {
      int kc0 = l15 ^ ((8 * w4 + g) & 15), kc1 = kc0 ^ 4;
      u32 v0 = ((8 * w4 + g) * 8 + (kc0 >> 1)) * VB_TS + (kc0 & 1) * 16, v1 = ((8 * w4 + 4 + g) * 8 + (kc1 >> 1)) * VB_TS + (kc1 & 1) * 16 - 1024;
      dma2(LDSADDR(smem) + K2_V + w8 * 2048, ws + OFF_VB + sidx * 64 + half * 32, v0, v1);
    }
    u32x4 tr[4], ti[4];
    dma2(LDSADDR(smem) + K2_U + w8 * 2048, (const char*)dF64.v + w8 * 2048, l * 16, l * 16);
    const int q = c == 4 ? ci : (half == 0 ? ci : (ci == 0 ? 32 : 64 - ci));
    u32 va = K2_V + half * 8192 + l15 * 256 + ((g ^ (l15 & 3)) << 4) + ((l15 >> 2) << 6), aa = K2_A + half * 8192 + l * 16;
    PIN(va); PIN(aa);
    SB();
    const float sc = 2.f / (2048.f * 640.f), s2 = (c == 4 && (ci & 31) == 0) ? 0.5f * sc : sc;
    f32x4 tc = z4, ts = z4;
    if (c == 4) {
#pragma unroll
      for (int i = 0; i < 4; ++i) {
        float rev = (float)(16 * w4 + 4 * g + i) * (1.f / 128.f);
        tc[i] = __builtin_amdgcn_cosf(rev) * s2; ts[i] = __builtin_amdgcn_sinf(rev) * s2;
      }
    }
    const int so = sidx == 160 ? 128 : sidx;
    char* dst = ws + OFF_AP + (size_t)((16 * half + l15) * 8 + 2 * w4 + (g >> 1)) * AP_TS + so * 64 + (g & 1) * 32;
    u32 uw = K2_U + half * (32 * K2_US) + 4 * g * K2_US + (16 * w4 + l15) * 4, ur0 = K2_U + (16 * half + l15) * K2_US + g * 16;
    asm volatile("" : "+v"(tc), "+v"(ts), "+v"(dst), "+v"(uw), "+v"(ur0));
    const int r = c == 4 ? 5 * half : c + 1, kd = r + 10 * (16 * w4 + l15);
    const bool mir = w4 >= 2;
    u32 wg[2], wg0[2];
#pragma unroll
    for (int k = 0; k < 2; ++k) {
      const bool mr = mir != (k == 1);
      const int qs = mr ? (64 - q) & 63 : q, col = k ? (mir ? 320 : 0) : (mir ? 640 - kd : kd);
      wg[k] = K2_W + (qs != qa) * 32 * WROW + ((2 * qs) & 15) + 2 * col + (mr ? ((q == 0 ? 32 : 31) - 19 - 4 * g) * WROW : 4 * g * WROW);
      wg0[k] = (mr && q == 0 && g == 0) ? wg[k] - 13 * WROW : wg[k] + (mr ? 19 * WROW : 0);
      PIN(wg[k]); PIN(wg0[k]);
    }
    WAITV(0);
    STAMP();
    __builtin_amdgcn_s_waitcnt(0xC07F);
    if (l == 0) __hip_atomic_fetch_add(cnt, 1u, __ATOMIC_RELAXED, __HIP_MEMORY_SCOPE_WORKGROUP);
    while (__builtin_amdgcn_readfirstlane(__hip_atomic_load(cnt, __ATOMIC_RELAXED, __HIP_MEMORY_SCOPE_WORKGROUP)) < 16u) {}
    asm volatile("" ::: "memory");
    STAMP();
    u32x4 vf[2][4], cf[4], sf[4];
#pragma unroll
    for (int ks = 0; ks < 4; ++ks) tr[ks] = ldsr128(smem, K2_U + w4 * 4096 + ks * 1024 + l * 16);
#pragma unroll
    for (int ks = 0; ks < 4; ++ks)
#pragma unroll
      for (int mt = 0; mt < 2; ++mt) vf[mt][ks] = ldsr128(smem, (va ^ (ks << 6)) + mt * 4096);
#pragma unroll
    for (int i = 0; i < 4; ++i) {
      cf[i] = ldsr128(smem, aa + i * 2048); sf[i] = ldsr128(smem, aa + i * 2048 + 1024);
    }
    SB();
    f32x4 Tr[2] = {z4, z4}, Ti[2] = {z4, z4};
#pragma unroll
    for (int ks = 0; ks < 4; ++ks) {
#pragma unroll
      for (int mt = 0; mt < 2; ++mt) Tr[mt] = mfma(vf[mt][ks], tr[ks], Tr[mt]);
#pragma unroll
      for (int i = 0; i < 4; ++i) ti[ks][i] = __builtin_amdgcn_alignbit(tr[ks][i], tr[ks][i], 16) ^ 0x8000u;
      SB();
    }
#pragma unroll
    for (int ks = 0; ks < 4; ++ks)
#pragma unroll
      for (int mt = 0; mt < 2; ++mt) Ti[mt] = mfma(vf[mt][ks], ti[ks], Ti[mt]);
    SB();
    u32x4 br = pkfrag(Tr[0], Tr[1]), bi = pkfrag(Ti[0], Ti[1]), ni = bi ^ 0x80008000u;
    f32x4 zr[2], zn[2];
#pragma unroll
    for (int mt = 0; mt < 2; ++mt) { zr[mt] = mfma(cf[mt], br, z4); zn[mt] = mfma(cf[mt], bi, z4); }
#pragma unroll
    for (int mt = 0; mt < 2; ++mt) { zr[mt] = mfma(sf[mt], ni, zr[mt]); zn[mt] = mfma(sf[mt], br, zn[mt]); }
    SB();
    __builtin_amdgcn_s_barrier();
    asm volatile("" ::: "memory");
    STAMP();
    f32x4 wa[2], wb[2];
    {
      u32 ra[8], rb[8];
      auto gather = [&](int s, bool mr) {
#pragma unroll
        for (int k = 0; k < 8; ++k) {
          const int dk = 16 * (k >> 2) + (k & 3);
          const u32 o = k == 0 ? wg0[s] : wg[s] + (mr ? 19 - dk : dk) * WROW;
          ra[k] = ldsr16(smem, o); rb[k] = ldsr16(smem, o + 64 * WROW);
        }
      };
      if (mir) gather(0, true); else gather(0, false);
      prefetch();
      const u32 sg = mir ? 0x80000000u : 0u;
#pragma unroll
      for (int k = 0; k < 8; ++k) { wa[k >> 2][k & 3] = lo2f(ra[k]); wb[k >> 2][k & 3] = __uint_as_float((rb[k] << 16) ^ sg); }
      if (r == 0 && (w4 & 1) == 0) {
        if (mir) gather(1, false); else gather(1, true);
#pragma unroll
        for (int k = 0; k < 8; ++k) {
          wa[k >> 2][k & 3] = l15 == 0 ? 0.5f * (wa[k >> 2][k & 3] + lo2f(ra[k])) : wa[k >> 2][k & 3];
          wb[k >> 2][k & 3] = l15 == 0 ? 0.5f * (wb[k >> 2][k & 3] - __uint_as_float((rb[k] << 16) ^ sg)) : wb[k >> 2][k & 3];
        }
      }
    }
    f32x4 Yr[2], Yi[2];
#pragma unroll
    for (int mt = 0; mt < 2; ++mt) { Yr[mt] = zr[mt] * wa[mt]; Yi[mt] = zr[mt] * wb[mt]; }
    SB();
#pragma unroll
    for (int mt = 0; mt < 2; ++mt) { Yr[mt] += zn[mt] * wb[mt]; Yi[mt] -= zn[mt] * wa[mt]; }
    SB();
    br = pkfrag(Yr[0], Yr[1]); bi = pkfrag(Yi[0], Yi[1]); ni = bi ^ 0x80008000u;
    f32x4 ur[2], ui[2];
#pragma unroll
    for (int mt = 0; mt < 2; ++mt) { ur[mt] = mfma(cf[2 + mt], br, z4); ui[mt] = mfma(cf[2 + mt], bi, z4); }
#pragma unroll
    for (int mt = 0; mt < 2; ++mt) { ur[mt] = mfma(sf[2 + mt], ni, ur[mt]); ui[mt] = mfma(sf[2 + mt], br, ui[mt]); }
    SB();
#pragma unroll
    for (int mt = 0; mt < 2; ++mt)
#pragma unroll
      for (int i = 0; i < 4; ++i) *(LDS_AS u32*)((LDS_AS char*)smem + uw + (16 * mt + i) * K2_US) = pk2(ur[mt][i], ui[mt][i]);
    STAMP();
    RAWBAR();
    STAMP();
    u32x4 uf[2][4];
#pragma unroll
    for (int ks = 0; ks < 4; ++ks)
#pragma unroll
      for (int cl = 0; cl < 2; ++cl) uf[cl][ks] = ldsr128(smem, ur0 + cl * (32 * K2_US) + ks * 64);
    SB();
    f32x4 Rr[2] = {z4, z4}, Ri[2] = {z4, z4};
#pragma unroll
    for (int ks = 0; ks < 4; ++ks)
#pragma unroll
      for (int cl = 0; cl < 2; ++cl) { Rr[cl] = mfma(tr[ks], uf[cl][ks], Rr[cl]); Ri[cl] = mfma(ti[ks], uf[cl][ks], Ri[cl]); }
    f32x4 e00, e01, e10, e11;
    if (c < 4) {
      f32x4 ar = Rr[0] * sc, ai = Ri[0] * -sc;
      if (ci > 0) { e00 = ar + Rr[1] * sc; e01 = ai - Ri[1] * sc; e10 = ai + Ri[1] * sc; e11 = Rr[1] * sc - ar; }
      else { e00 = ar; e01 = ai; e10 = Rr[1] * sc; e11 = Ri[1] * -sc; }
    } else {
      e00 = Rr[0] * s2; e01 = Rr[1] * tc - Ri[1] * ts;
      if (ci & 31) { e10 = Ri[0] * -s2; e11 = -(Rr[1] * ts + Ri[1] * tc); }
      else { e10 = e00; e11 = e01; }
    }
    if (sidx != 160) *(u32x4*)dst = pkfrag(e00, e01);
    if (sidx != 128) *(u32x4*)(dst + 16) = pkfrag(e10, e11);
    STAMP();
  } else {
    if (w8 >= 8) return;
    const int ob = b - K2_NF, hp = ob & 15, n2 = 8 * (ob >> 4) + w8;
    u32x4 s[8];
    const int rh = tid >= 256, rp = tid & 255;
    if (rp < 210) {
      const char* kb = ws + OFF_KVP + hp * 3360;
      const u32 ko = rp * 16 + rh * (8 * 53760);
#pragma unroll
      for (int mm = 0; mm < 8; ++mm) asm volatile("global_load_dwordx4 %0, %1, %2" : "=v"(s[mm]) : "v"(ko), "s"(kb + mm * 53760) : "memory");
    }
    const char* src = ws + OFF_PQ + ((size_t)hp * 2048 + 64 * n2) * 80;
    const u32 pl = 8192 + w8 * 5136;
    dma4(LDSADDR(smem) + pl, src, l * 16, l * 16, l * 16, l * 16);
    dma1(LDSADDR(smem) + pl + 4096, src + 4096, l * 16);
    const int gc = g > 2 ? 2 : g;
    const u32 m0 = g < 3 ? ~0u : 0u, m1 = g < 2 ? ~0u : 0u;
    u32 oo = (n2 * 16 + hp) * 5120 + l * 16;
    u32 a0 = l15 * 48 + gc * 16, a1 = (16 + (l15 & 3)) * 48 + gc * 16, a2 = 960 + gc * 16;
    u32 pr = pl + l15 * 80 + 16 * gc, pw = pl + l15 * 80 + 8 * g, pw2 = pl + l15 * 80 + 32 + 4 * (g & 1), pc = pl + l * 16;
    const int u_ = rp >= 105, in = rp - 105 * u_, e_ = (in * 205) >> 10;
    u32 kw = u_ * 1024 + e_ * 48 + (in - 5 * e_) * 8;
    PIN(oo); PIN(a0); PIN(a1); PIN(a2); PIN(pr); PIN(pw); PIN(pw2); PIN(pc); PIN(kw);
    asm volatile("s_waitcnt vmcnt(5)" : "+v"(s[0]), "+v"(s[1]), "+v"(s[2]), "+v"(s[3]), "+v"(s[4]), "+v"(s[5]), "+v"(s[6]), "+v"(s[7]) :: "memory");
    f32x4 sum = ((__builtin_bit_cast(f32x4, s[0]) + __builtin_bit_cast(f32x4, s[1])) + (__builtin_bit_cast(f32x4, s[2]) + __builtin_bit_cast(f32x4, s[3])))
              + ((__builtin_bit_cast(f32x4, s[4]) + __builtin_bit_cast(f32x4, s[5])) + (__builtin_bit_cast(f32x4, s[6]) + __builtin_bit_cast(f32x4, s[7])));
    if (rh) *(LDS_AS f32x4*)((LDS_AS char*)smem + 2048 + rp * 16) = sum;
    RAWBAR();
    if (!rh && rp < 210) {
      sum += *(const LDS_AS f32x4*)((const LDS_AS char*)smem + 2048 + rp * 16);
      ldsw64(smem, kw, u32x2{pk2(sum[0], sum[1]), pk2(sum[2], sum[3])});
    }
    STAMP();
    WAITV(0);
    prefetch();
    u32x4 af[2][3], pf[4][2];
#pragma unroll
    for (int rt = 0; rt < 4; ++rt)
#pragma unroll
      for (int u = 0; u < 2; ++u) {
        u32x2 a = ldsr64(smem, pr + rt * 1280 + 40 * u), bq = ldsr64(smem, pr + rt * 1280 + 40 * u + 8);
        pf[rt][u] = u32x4{a[0], a[1], bq[0], bq[1]};
      }
    RAWBAR();
    STAMP();
#pragma unroll
    for (int u = 0; u < 2; ++u) { af[u][2] = ldsr128(smem, a2 + u * 1024); af[u][0] = ldsr128(smem, a0 + u * 1024); af[u][1] = ldsr128(smem, a1 + u * 1024); }
    SB();
    pf[3][1] &= u32x4{~0u, ~0u, m1, m1};
#pragma unroll
    for (int u = 0; u < 2; ++u)
#pragma unroll
      for (int tI = 0; tI < 3; ++tI) af[u][tI] &= u32x4{m0, m0, m1, m1};
    f32x4 n0[4][2], n1[4][2], dn[4][2];
#pragma unroll
    for (int u = 0; u < 2; ++u)
#pragma unroll
      for (int rt = 0; rt < 4; ++rt) dn[rt][u] = mfma(af[u][2], pf[rt][u], z4);
#pragma unroll
    for (int u = 0; u < 2; ++u)
#pragma unroll
      for (int rt = 0; rt < 4; ++rt) { n0[rt][u] = mfma(af[u][0], pf[rt][u], z4); n1[rt][u] = mfma(af[u][1], pf[rt][u], z4); }
    SB();
    float rd[8]; u32 b0[8], b1[8], b2[8];
#pragma unroll
    for (int k = 0; k < 8; ++k) rd[k] = bfr(dn[k & 3][k >> 2][0]) + 1e-6f;
    SB();
#pragma unroll
    for (int k = 0; k < 8; ++k) rd[k] = __builtin_amdgcn_rcpf(bfr(rd[k]));
    SB();
#pragma unroll
    for (int k = 0; k < 8; ++k) {
      const f32x4 a = n0[k & 3][k >> 2], c2 = n1[k & 3][k >> 2];
      b0[k] = pk2(a[0], a[1]); b1[k] = pk2(a[2], a[3]); b2[k] = (g & 1) ? pk2(c2[2], c2[3]) : pk2(c2[0], c2[1]);
    }
    SB();
#pragma unroll
    for (int k = 0; k < 8; ++k) {
      f32x2 x0 = f32x2{lo2f(b0[k]), hi2f(b0[k])} * rd[k], x1 = f32x2{lo2f(b1[k]), hi2f(b1[k])} * rd[k], x2 = f32x2{lo2f(b2[k]), hi2f(b2[k])} * rd[k];
      b0[k] = pk2(x0[0], x0[1]); b1[k] = pk2(x1[0], x1[1]); b2[k] = pk2(x2[0], x2[1]);
    }
#pragma unroll
    for (int k = 0; k < 8; ++k) {
      ldsw64(smem, pw + (k & 3) * 1280 + 40 * (k >> 2), u32x2{b0[k], b1[k]});
      *(LDS_AS u32*)((LDS_AS char*)smem + pw2 + (k & 3) * 1280 + 40 * (k >> 2)) = b2[k];
    }
    u32x4 o[5];
#pragma unroll
    for (int i = 0; i < 5; ++i) o[i] = ldsr128(smem, pc + i * 1024);
    ST16SO(ws + OFF_O2, oo, 0, o[0]); ST16SO(ws + OFF_O2, oo, 1024, o[1]); ST16SO(ws + OFF_O2, oo, 2048, o[2]); ST16SO(ws + OFF_O2, oo, 3072, o[3]);
    ST16SO(ws + OFF_O2 + 4096, oo, 0, o[4]);
    STAMP();
  }
  STAMP_FLUSH(1);
}

constexpr int K3_W = 81920, K3_AP = 61440, K3_GS = K3_AP + 10240, K3_CNT = K3_W + 3 * 25600, K3_LDS = K3_CNT + 16;

__global__ __launch_bounds__(1024) __attribute__((amdgpu_waves_per_eu(4, 4)))
void k3(const char* __restrict__ ws, u16* __restrict__ out) {
  extern __shared__ __attribute__((aligned(16))) char smem[];
  const int tid = threadIdx.x, l = tid & 63, w = __builtin_amdgcn_readfirstlane(tid >> 6);
  const int g = l >> 4, l15 = l & 15;
  const int b = blockIdx.x, n2 = 4 * (b & 7) + ((b >> 3) & 3), bg = b >> 5;
  const f32x4 z4 = {0.f, 0.f, 0.f, 0.f};
  asm volatile("" :: "s"(ws), "s"(out));
  STAMP_DECL; STAMP();
  if (w >= 10) {
    const int v = w - 10, rot = (bg + (b >> 3)) & 3;
    const u32 lo = l * 16, L = LDSADDR(smem);
    __builtin_amdgcn_s_setprio(2);
    {
      const char* ap = ws + OFF_AP + (size_t)(n2 * 8 + bg) * AP_TS;
      if (v < 5) dma2(L + K3_AP + v * 2048, ap + v * 2048, lo, lo);
      if (v < 5) dma1(L + K3_GS + v * 1024, (const char*)dGsB.v + v * 1024, lo);
      else { dma2(L + K3_GS + 5120, (const char*)dGsB.v + 5120, lo, lo); dma1(L + K3_GS + 7168, (const char*)dGsB.v + 7168, lo); }
    }
    const char* op = ws + OFF_O2 + (size_t)n2 * 81920 + v * 4096;
    const char* wp = ws + OFF_WOF + (size_t)bg * 102400;
    auto issue = [&](int Q) {
      const int Qd = (Q + rot) & 3;
      const u32 lw = L + K3_W + (Q == 3 ? 0 : Q) * 25600;
      if (v < 5) {
        dma4(L + Q * 20480 + v * 4096, op + Qd * 20480, lo, lo, lo, lo);
        dma4(lw + v * 5120, wp + v * 20480 + Qd * 5120, lo, lo, lo, lo);
      } else {
#pragma unroll
        for (int c = 0; c < 5; ++c) dma1(lw + c * 5120 + 4096, wp + c * 20480 + Qd * 5120 + 4096, lo);
      }
    };
#define K3W() do { if (v < 5) WAITV(8); else WAITV(5); __builtin_amdgcn_s_barrier(); } while (0)
    issue(0);
    if (tid == 640) *(LDS_AS u32*)((LDS_AS char*)smem + K3_CNT) = 0u;
    __builtin_amdgcn_s_waitcnt(0xC07F);
    K3W();
    issue(1);
    K3W();
    issue(2);
    K3W();
    issue(3);
    K3W();
    WAITV(0);
    if (l == 0) __hip_atomic_fetch_add((LDS_AS u32*)((LDS_AS char*)smem + K3_CNT), 1u, __ATOMIC_RELAXED, __HIP_MEMORY_SCOPE_WORKGROUP);
    __builtin_amdgcn_s_waitcnt(0xC07F);
  } else {
    const int ga = w < 5, ct = ga ? w : w - 5;
    const u32 oa = g * 5120 + l15 * 80, wa = K3_W + ct * 5120 + l * 16;
    f32x4 acc[4] = {z4, z4, z4, z4};
    auto quarter = [&](int Q) {
      u32x4 of[5][4], wf[5];
#pragma unroll
      for (int wv = 0; wv < 5; ++wv) {
        wf[wv] = ldsr128(smem, wa + (Q == 3 ? 0 : Q) * 25600 + wv * 1024);
        if (wv < 3) {
#pragma unroll
          for (int rt = 0; rt < 4; ++rt) of[wv][rt] = ldsr128(smem, oa + Q * 20480 + rt * 1280 + wv * 16);
        }
      }
      SB();
#pragma unroll
      for (int wv = 0; wv < 2; ++wv)
#pragma unroll
        for (int rt = 0; rt < 4; ++rt) {
          acc[rt] = mfma(wf[wv], of[wv][rt], acc[rt]);
          SB();
          of[3 + wv][rt] = ldsr128(smem, oa + Q * 20480 + rt * 1280 + (3 + wv) * 16);
          SB();
        }
#pragma unroll
      for (int wv = 2; wv < 5; ++wv)
#pragma unroll
        for (int rt = 0; rt < 4; ++rt) acc[rt] = mfma(wf[wv], of[wv][rt], acc[rt]);
      SB();
    };
    if (!ga) {
      __builtin_amdgcn_s_barrier();
      __builtin_amdgcn_s_barrier();
      STAMP();
      quarter(0);
      RAWBAR();
      __builtin_amdgcn_s_barrier();
      STAMP();
      quarter(2);
#pragma unroll
      for (int rt = 0; rt < 4; ++rt) *(LDS_AS f32x4*)((LDS_AS char*)smem + (ct * 4 + rt) * 1024 + l * 16) = acc[rt];
      __builtin_amdgcn_s_waitcnt(0xC07F);
      STAMP();
    } else {
      u32 gv[4];
      const char* gp = ws + OFF_G + (64 * n2 + l15) * 4;
      GLD1(gv[0], gp, 0); GLD1(gv[1], gp, 64); GLD1(gv[2], gp, 128); GLD1(gv[3], gp, 192);
      u32x4 kd[3];
      const int a = 2 * ct + (l15 >> 3), j = l15 & 7, d = 64 * a + 8 * bg + j;
#pragma unroll
      for (int s = 0; s < 3; ++s) {
        int kc = 4 * s + g, c = kc >> 1;
        float rev = c < 4 ? (float)((c + 1) * d) * (1.f / 640.f) : 0.f;
        float v0 = __builtin_amdgcn_cosf(rev), v1 = c < 4 ? __builtin_amdgcn_sinf(rev) : ((a & 1) ? -1.f : 1.f);
        bool on = kc < 10 && (kc & 1) == (j >> 2);
        u32 pp = on ? pk2(v0, v1) : 0u;
        u32 p0 = (pp & 0xffffu) << ((j & 1) * 16), p1 = (pp >> 16) << ((j & 1) * 16);
        kd[s] = (j & 2) ? u32x4{0u, p0, 0u, p1} : u32x4{p0, 0u, p1, 0u};
        PIN(kd[s]);
      }
      u32 ao = K3_AP + ((l15 >> 1) & 1) * 256 + (l15 >> 2) * 64 + (l15 & 1) * 16 + (g & 1) * 32, ao1 = ao + (g >> 1) * 2048;
      u32 oo = ((32 * l15 + n2) * 640 + 64 * (2 * ct + (g >> 1)) + 8 * bg + 4 * (g & 1)) * 2;
      PIN(ao); PIN(ao1); PIN(oo);
      STAMP();
      __builtin_amdgcn_s_barrier();
      asm volatile("" ::: "memory");
      u32x4 af[4][3], gs[8];
#pragma unroll
      for (int rt = 0; rt < 4; ++rt) { af[rt][0] = ldsr128(smem, ao1 + rt * 512); af[rt][1] = ldsr128(smem, ao1 + 4096 + rt * 512); af[rt][2] = ldsr128(smem, ao + 8192 + rt * 512); }
#pragma unroll
      for (int f = 0; f < 8; ++f) gs[f] = ldsr128(smem, K3_GS + f * 1024 + l * 16);
      SB();
      f32x4 P[4];
#pragma unroll
      for (int s = 0; s < 3; ++s)
#pragma unroll
        for (int rt = 0; rt < 4; ++rt) P[rt] = mfma(af[rt][s], kd[s], s ? P[rt] : z4);
      u32x4 p0 = pkfrag(P[0], P[1]), p1 = pkfrag(P[2], P[3]);
      SB();
      RAWBAR();
      f32x4 yb[4];
#pragma unroll
      for (int nt = 0; nt < 4; ++nt) yb[nt] = mfma(p0, gs[2 * nt], z4);
#pragma unroll
      for (int nt = 0; nt < 4; ++nt) yb[nt] = mfma(p1, gs[2 * nt + 1], yb[nt]);
      asm volatile("s_waitcnt vmcnt(0)" : "+v"(gv[0]), "+v"(gv[1]), "+v"(gv[2]), "+v"(gv[3]) :: "memory");
      u32 yp[4][2];
#pragma unroll
      for (int nt = 0; nt < 4; ++nt) {
        const float hh = bfr(1.f - __uint_as_float(gv[nt]));
        u32 b0 = pk2(yb[nt][0], yb[nt][1]), b1 = pk2(yb[nt][2], yb[nt][3]);
        yp[nt][0] = pk2(hh * lo2f(b0), hh * hi2f(b0)); yp[nt][1] = pk2(hh * lo2f(b1), hh * hi2f(b1));
        PIN(yp[nt][0]); PIN(yp[nt][1]);
      }
      STAMP();
      __builtin_amdgcn_s_barrier();
      asm volatile("" ::: "memory");
      STAMP();
      quarter(1);
      __builtin_amdgcn_s_barrier();
      while (__builtin_amdgcn_readfirstlane(__hip_atomic_load((LDS_AS u32*)((LDS_AS char*)smem + K3_CNT), __ATOMIC_RELAXED, __HIP_MEMORY_SCOPE_WORKGROUP)) < 6u) __builtin_amdgcn_s_sleep(1);
      asm volatile("" ::: "memory");
      STAMP();
      quarter(3);
      __builtin_amdgcn_s_barrier();
      asm volatile("" ::: "memory");
      f32x4 pb[4];
#pragma unroll
      for (int rt = 0; rt < 4; ++rt) pb[rt] = *(const LDS_AS f32x4*)((const LDS_AS char*)smem + (ct * 4 + rt) * 1024 + l * 16);
      STAMP();
      u32 a0[4], a1[4];
#pragma unroll
      for (int rt = 0; rt < 4; ++rt) { f32x4 t = acc[rt] + pb[rt]; a0[rt] = pk2(t[0], t[1]); a1[rt] = pk2(t[2], t[3]); }
      SB();
#pragma unroll
      for (int rt = 0; rt < 4; ++rt) {
        const float gg = __uint_as_float(gv[rt]);
        a0[rt] = pk2(gg * lo2f(a0[rt]), gg * hi2f(a0[rt])); a1[rt] = pk2(gg * lo2f(a1[rt]), gg * hi2f(a1[rt]));
      }
      SB();
#pragma unroll
      for (int rt = 0; rt < 4; ++rt) {
        u32x2 o = {pk2(lo2f(a0[rt]) + lo2f(yp[rt][0]), hi2f(a0[rt]) + hi2f(yp[rt][0])), pk2(lo2f(a1[rt]) + lo2f(yp[rt][1]), hi2f(a1[rt]) + hi2f(yp[rt][1]))};
        *(u32x2*)((char*)out + (size_t)(oo + rt * (512 * 1280))) = o;
      }
      STAMP();
    }
  }
  STAMP_FLUSH(2);
}

__global__ void kend() {}

extern "C" void kernel_launch(void* const* d_in, const int* in_sizes, int n_in, void* d_out, int out_size, void* d_ws,
                              size_t ws_size, hipStream_t stream) {
  const u16* x = (const u16*)d_in[0];
  const u16* wqkv = (const u16*)d_in[1];
  const u16* wo = (const u16*)d_in[2];
  const u16* wr = (const u16*)d_in[4];
  const u16* wi = (const u16*)d_in[5];
  const u16* gate = (const u16*)d_in[6];
  char* ws = (char*)d_ws;
  (void)hipFuncSetAttribute((const void*)k1, hipFuncAttributeMaxDynamicSharedMemorySize, K1_LDS);
  (void)hipFuncSetAttribute((const void*)k2, hipFuncAttributeMaxDynamicSharedMemorySize, K2_LDS);
  (void)hipFuncSetAttribute((const void*)k3, hipFuncAttributeMaxDynamicSharedMemorySize, K3_LDS);
  k1<<<256, 1024, K1_LDS, stream>>>(x, wqkv, wr, wi, gate, wo, ws, (u16*)d_out);
  k2<<<K2_NF + K2_NO, 1024, K2_LDS, stream>>>(wr, wi, ws);
  k3<<<256, 1024, K3_LDS, stream>>>(ws, (u16*)d_out);
}
```

```cpp
#include <hip/hip_runtime.h>
#include <stdint.h>

typedef unsigned short u16;
typedef unsigned int u32;
typedef short s16x4 __attribute__((ext_vector_type(4)));
typedef __bf16 bf16x8 __attribute__((ext_vector_type(8)));
typedef __bf16 bf16x2 __attribute__((ext_vector_type(2)));
typedef float f32x4 __attribute__((ext_vector_type(4)));
typedef float f32x2 __attribute__((ext_vector_type(2)));
typedef u32 u32x4 __attribute__((ext_vector_type(4)));
typedef u32 u32x2 __attribute__((ext_vector_type(2)));

#define LDS_AS __attribute__((address_space(3)))
#define DI __device__ __forceinline__
__device__ __forceinline__ void dma1(u32 lds, const void* base, u32 v0) {
  u32 keep;
  asm volatile("s_mov_b32 %0, m0\n\ts_mov_b32 m0, %1\n\ts_nop 4\n\tglobal_load_lds_dwordx4 %2, %3\n\ts_mov_b32 m0, %0"
               : "=&s"(keep) : "s"(lds), "v"(v0), "s"(base) : "memory");
}
__device__ __forceinline__ void dma4(u32 lds, const void* base, u32 v0, u32 v1, u32 v2, u32 v3) {
  u32 keep;
  asm volatile("s_mov_b32 %0, m0\n\ts_mov_b32 m0, %1\n\ts_nop 4\n\tglobal_load_lds_dwordx4 %2, %6\n\t"
               "global_load_lds_dwordx4 %3, %6 offset:1024\n\tglobal_load_lds_dwordx4 %4, %6 offset:2048\n\t"
               "global_load_lds_dwordx4 %5, %6 offset:3072\n\ts_mov_b32 m0, %0"
               : "=&s"(keep) : "s"(lds), "v"(v0), "v"(v1), "v"(v2), "v"(v3), "s"(base) : "memory");
}
__device__ __forceinline__ void dma2(u32 lds, const void* base, u32 v0, u32 v1) {
  u32 keep;
  asm volatile("s_mov_b32 %0, m0\n\ts_mov_b32 m0, %1\n\ts_nop 4\n\tglobal_load_lds_dwordx4 %2, %4\n\t"
               "global_load_lds_dwordx4 %3, %4 offset:1024\n\ts_mov_b32 m0, %0"
               : "=&s"(keep) : "s"(lds), "v"(v0), "v"(v1), "s"(base) : "memory");
}
#define GLD1(dst, p, imm) asm volatile("global_load_dword %0, %1, off offset:" #imm : "=v"(dst) : "v"(p) : "memory")
#define LDSADDR(p) ((u32)(uintptr_t)(LDS_AS char*)(p))
#define SB() __builtin_amdgcn_sched_barrier(0)
#define WAITV(n) asm volatile("s_waitcnt vmcnt(" #n ")" ::: "memory")
#define RAWBAR() do { asm volatile("" ::: "memory"); __builtin_amdgcn_s_waitcnt(0xC07F); __builtin_amdgcn_s_barrier(); asm volatile("" ::: "memory"); } while (0)

constexpr size_t OFF_PQ  = 0;
constexpr size_t OFF_O2  = OFF_PQ + 2621440;
constexpr size_t OFF_KVP = OFF_O2 + 2621440;
constexpr size_t OFF_VB  = OFF_KVP + 860160;
constexpr int    VB_TS   = 10368;
constexpr size_t OFF_AP  = OFF_VB + (size_t)256 * VB_TS;
constexpr int    AP_TS   = 10240;
constexpr size_t OFF_WOF = OFF_AP + (size_t)256 * AP_TS;
constexpr size_t OFF_G   = OFF_WOF + 819200;

constexpr double kPi = 3.14159265358979323846;
constexpr double tcos(double x) {
  double x2 = x * x, term = 1.0, s = 1.0;
  for (int i = 1; i < 14; ++i) { term *= -x2 / ((2 * i - 1) * (2 * i)); s += term; }
  return s;
}
constexpr double cos64(int k) {
  k &= 63; if (k > 32) k = 64 - k;
  if (k == 16) return 0.0;
  return (k < 16) ? tcos(2 * kPi * k / 64) : -tcos(2 * kPi * (32 - k) / 64);
}
constexpr u16 tobf(double v) {
  u32 u = __builtin_bit_cast(u32, (float)v);
  return (u16)((u + 0x7fffu + ((u >> 16) & 1u)) >> 16);
}
struct C64 { u16 c[64]; };
constexpr C64 mkC64() { C64 t{}; for (int i = 0; i < 64; ++i) t.c[i] = tobf(cos64(i)); return t; }
constexpr C64 kC64 = mkC64();
constexpr u16 gsv(int qp, int n1) {
  int i = qp >> 1;
  if (qp == 1) return kC64.c[(32 * n1) & 63];
  return (qp & 1) ? kC64.c[(i * n1 + 48) & 63] : kC64.c[(i * n1) & 63];
}
constexpr int kperm(int s2, int g, int j) { return 32 * s2 + (j < 4 ? 4 * g + j : 12 + 4 * g + j); }
struct T16 { u16 v[4096]; };
struct T32 { u32 v[4096]; };
constexpr T16 mkGsA() { T16 t{};
  for (int f = 0; f < 8; ++f) for (int l = 0; l < 64; ++l) for (int j = 0; j < 8; ++j)
    t.v[(f * 64 + l) * 8 + j] = gsv(16 * (f >> 1) + (l & 15), kperm(f & 1, l >> 4, j));
  return t; }
constexpr T16 mkGsB() { T16 t{};
  for (int f = 0; f < 8; ++f) for (int l = 0; l < 64; ++l) for (int j = 0; j < 8; ++j)
    t.v[(f * 64 + l) * 8 + j] = gsv(kperm(f & 1, l >> 4, j), 16 * (f >> 1) + (l & 15));
  return t; }
constexpr T32 mkF64() { T32 t{};
  for (int f = 0; f < 16; ++f) for (int l = 0; l < 64; ++l) for (int j = 0; j < 4; ++j) {
    int u = 16 * (f >> 2) + (l & 15), v = 4 * (4 * (f & 3) + (l >> 4)) + j;
    t.v[(f * 64 + l) * 4 + j] = (u32)kC64.c[(u * v) & 63] | ((u32)kC64.c[(u * v + 16) & 63] << 16);
  }
  return t; }
__device__ __attribute__((aligned(128))) const T16 dGsA = mkGsA();
__device__ __attribute__((aligned(128))) const T16 dGsB = mkGsB();
__device__ __attribute__((aligned(128))) const T32 dF64 = mkF64();

DI f32x4 mfma(u32x4 a, u32x4 b, f32x4 c) {
  return __builtin_amdgcn_mfma_f32_16x16x32_bf16(__builtin_bit_cast(bf16x8, a), __builtin_bit_cast(bf16x8, b), c, 0, 0, 0);
}
DI u32 pk2(float lo, float hi) { f32x2 v = {lo, hi}; return __builtin_bit_cast(u32, __builtin_convertvector(v, bf16x2)); }
DI float lo2f(u32 p) { return __uint_as_float(p << 16); }
DI float hi2f(u32 p) { return __uint_as_float(p & 0xffff0000u); }
DI float bfr(float x) { return lo2f(pk2(x, x)); }
DI u32x4 pkfrag(f32x4 a, f32x4 b) { u32x4 r = {pk2(a[0], a[1]), pk2(a[2], a[3]), pk2(b[0], b[1]), pk2(b[2], b[3])}; return r; }
DI float ror8(float v) { return __int_as_float(__builtin_amdgcn_update_dpp(0, __float_as_int(v), 0x128, 0xf, 0xf, true)); }
DI u32x4 ldsr128(const char* smem, u32 off) { return *(const LDS_AS u32x4*)((const LDS_AS char*)smem + off); }
DI u32x2 ldstr(const char* smem, u32 off) {
  return __builtin_bit_cast(u32x2, __builtin_amdgcn_ds_read_tr16_b64_v4i16((LDS_AS s16x4*)((LDS_AS char*)smem + off)));
}
DI void l2touch(u32 lds, const void* p) {
  u32 keep;
  asm volatile("s_mov_b32 %0, m0\n\ts_mov_b32 m0, %1\n\ts_nop 0\n\tglobal_load_lds_dword %2, off\n\ts_mov_b32 m0, %0"
               : "=&s"(keep) : "s"(lds), "v"(p) : "memory");
}
DI void st16p(char* b, u32 o, f32x4 v) { *(f32x4*)(b + (size_t)o) = v; }
#define ST16SO(b, o, imm, v) asm volatile("global_store_dwordx4 %0, %1, %2 offset:" #imm " sc1\n\ts_nop 1" :: "v"(o), "v"(v), "s"(b) : "memory")
#define ST4SO(b, o, imm, v) asm volatile("global_store_dword %0, %1, %2 offset:%3 sc1" :: "v"(o), "v"(v), "s"(b), "i"(imm) : "memory")
DI void ldsw128(char* smem, u32 off, u32x4 v) { *(LDS_AS u32x4*)((LDS_AS char*)smem + off) = v; }
DI void ldsw64(char* smem, u32 off, u32x2 v) { *(LDS_AS u32x2*)((LDS_AS char*)smem + off) = v; }
DI u32x2 ldsr64(const char* smem, u32 off) { return *(const LDS_AS u32x2*)((const LDS_AS char*)smem + off); }
DI u32 ldsr16(const char* smem, u32 off) { return *(const LDS_AS u16*)((const LDS_AS char*)smem + off); }
template <int N> DI void phi4(u32x4 (&p)[N]) {
  f32x2 t[4 * N], e[4 * N];
#pragma unroll
  for (int i = 0; i < 4 * N; ++i) t[i] = f32x2{lo2f(p[i >> 2][i & 3]), hi2f(p[i >> 2][i & 3])};
  SB();
#pragma unroll
  for (int i = 0; i < 4 * N; ++i) e[i] = t[i] * 1.44269504088896f;
  SB();
#pragma unroll
  for (int i = 0; i < 4 * N; ++i) {
    e[i][0] = __builtin_amdgcn_fmed3f(__builtin_amdgcn_exp2f(e[i][0]), 0.f, 1.f); e[i][1] = __builtin_amdgcn_fmed3f(__builtin_amdgcn_exp2f(e[i][1]), 0.f, 1.f);
    t[i][0] = __builtin_fmaxf(t[i][0], 0.f); t[i][1] = __builtin_fmaxf(t[i][1], 0.f);
  }
  SB();
  float mn = 1.f;
#pragma unroll
  for (int i = 0; i < 4 * N; ++i) { mn = __builtin_fminf(__builtin_fminf(mn, e[i][0]), e[i][1]); e[i] += t[i]; }
  SB();
#pragma unroll
  for (int i = 0; i < 4 * N; ++i) p[i >> 2][i & 3] = pk2(e[i][0], e[i][1]);
  if (__builtin_amdgcn_ballot_w64(mn < 0.001f)) {
#pragma unroll
    for (int n = 0; n < N; ++n)
#pragma unroll
      for (int i = 0; i < 4; ++i) p[n][i] = pk2(lo2f(p[n][i]) + 1e-6f, hi2f(p[n][i]) + 1e-6f);
  }
}
template <int CTRL> DI float dppf(float v) { return __int_as_float(__builtin_amdgcn_update_dpp(0, __float_as_int(v), CTRL, 0xf, 0xf, true)); }
DI u32x4 onehot(u32 s, int d) { u32x4 r = {d == 0 ? s : 0u, d == 1 ? s : 0u, d == 2 ? s : 0u, d == 3 ? s : 0u}; return r; }

constexpr int K1_SLOT = 16384, K1_B = 65536, K1_AUX = 131072, K1_GS = K1_AUX + 272, K1_LDS = K1_GS + 8192;

struct Frags { u32x4 x[2][2], w[2][4]; };
#define PIN(v) asm volatile("" : "+v"(v))

template <bool QT>
DI void k1_gemm(char* smem, const u16* x, const u16* gate, char* ws, int m, int nt, int w, int l) {
  const int g = l >> 4, l15 = l & 15, rg = w & 3, chh = w >> 2;
  {
    const int r0 = 8 * nt + w, lc = l < 40 ? l : 39;
    const u32x4* gx = (const u32x4*)(x + (32 * (r0 & 63) + 2 * m + (r0 >> 6)) * 640) + lc;
    const u32x4 gxv[2] = {gx[0], gx[40]}, gwv[2] = {((const u32x4*)gate)[lc], ((const u32x4*)gate)[lc + 40]};
    float g0 = 0.f, g1 = 0.f;
#pragma unroll
    for (int i = 0; i < 8; ++i) {
      const u32 xv = gxv[i >> 2][i & 3], wv = gwv[i >> 2][i & 3];
      u32 pr = pk2(lo2f(xv) * lo2f(wv), hi2f(xv) * hi2f(wv));
      g0 += lo2f(pr); g1 += hi2f(pr);
    }
    g0 = l < 40 ? g0 + g1 : 0.f;
    g0 += dppf<0xB1>(g0); g0 += dppf<0x4E>(g0); g0 += dppf<0x124>(g0); g0 += dppf<0x128>(g0);
    float tot = __uint_as_float(__builtin_amdgcn_readlane(__float_as_uint(g0), 0)) + __uint_as_float(__builtin_amdgcn_readlane(__float_as_uint(g0), 16))
              + __uint_as_float(__builtin_amdgcn_readlane(__float_as_uint(g0), 32)) + __uint_as_float(__builtin_amdgcn_readlane(__float_as_uint(g0), 48));
    float gt = bfr(__builtin_amdgcn_rcpf(1.f + __builtin_amdgcn_exp2f(bfr(tot) * -1.44269504088896f)));
    if (l == 0) ((float*)(ws + OFF_G))[128 * m + r0] = gt;
  }
  u32 xa[2], wa[4][2];
  xa[0] = rg * 4096 + l15 * 128 + ((g ^ ((l15 >> 1) & 7)) << 4); xa[1] = xa[0] ^ 64;
#pragma unroll
  for (int t2 = 0; t2 < 2; ++t2) {
    const int q4 = l15 >> 2, swz = (q4 << 2) | ((2 * g + t2) & 3);
    const u32 b0 = K1_B + (8 * g + 4 * t2 + q4) * 256 + (((((l >> 1) & 1) | ((QT ? 8 : 4) * chh)) ^ swz) << 4) + 8 * (l & 1);
#pragma unroll
    for (int i = 0; i < 4; ++i) { wa[i][t2] = b0 ^ ((QT ? i : (i & 1) + 4 * (i >> 1)) << 5); PIN(wa[i][t2]); }
  }
  f32x4 acc[2][4];
#pragma unroll
  for (int rt = 0; rt < 2; ++rt)
#pragma unroll
    for (int i = 0; i < 4; ++i) acc[rt][i] = f32x4{0.f, 0.f, 0.f, 0.f};
  Frags F[2];
  auto rd = [&](Frags& f, int C) {
    const int so = (C & 3) * K1_SLOT;
#pragma unroll
    for (int ks = 0; ks < 2; ++ks) {
#pragma unroll
      for (int rt = 0; rt < 2; ++rt) f.x[ks][rt] = ldsr128(smem, xa[ks] + so + rt * 2048);
#pragma unroll
      for (int i = 0; i < 4; ++i) {
        u32x2 a = ldstr(smem, wa[i][0] + so + ks * 8192), b = ldstr(smem, wa[i][1] + so + ks * 8192);
        f.w[ks][i] = u32x4{a[0], a[1], b[0], b[1]};
      }
    }
  };
  auto mm = [&](const Frags& f) {
#pragma unroll
    for (int ks = 0; ks < 2; ++ks)
#pragma unroll
      for (int i = 0; i < 4; ++i)
#pragma unroll
        for (int rt = 0; rt < 2; ++rt)
          acc[rt][i] = QT ? mfma(f.w[ks][i], f.x[ks][rt], acc[rt][i]) : mfma(f.x[ks][rt], f.w[ks][i], acc[rt][i]);
  };
  RAWBAR();
  rd(F[0], 0);
  SB();
  u32 eo[2], ra[2], rb[2];
  if (QT) {
    const int ck = 16 * (nt - 11) + 8 * chh + (l & 7), hp = (ck * 205) >> 10;
    eo[0] = (hp * 2048 + 128 * m + 32 * rg + (l >> 3)) * 80 + (ck - 5 * hp) * 16;
    ra[0] = 2 * K1_SLOT + chh * K1_B + rg * 4608 + l15 * 144 + g * 8;
    rb[0] = 2 * K1_SLOT + chh * K1_B + rg * 4608 + (l >> 3) * 144 + (l & 7) * 16;
    PIN(eo[0]); PIN(ra[0]); PIN(rb[0]);
  } else {
#pragma unroll
    for (int k = 0; k < 2; ++k) {
      int P = w + 8 * k;
      int pi = (0x103322211100ull >> (4 * P)) & 15, pj = (0x333232121010ull >> (4 * P)) & 15;
      int d = 16 * pi + 4 * g, e = 16 * pj + l15, hd = (d * 205) >> 12, he = (e * 205) >> 12;
      int h = 3 * nt + hd;
      bool ok = P < 12 && d < 60 && h < 32 && (e == 60 || (e < 60 && he == hd));
      int ee = e == 60 ? 20 : e - 20 * he;
      eo[k] = ok ? (((m * 32 + h) * 21 + ee) * 20 + (d - 20 * hd)) * 4 : ~0u;
      ra[k] = 2 * K1_SLOT + pi * 1024 + l * 16; rb[k] = 2 * K1_SLOT + (4 + pj) * 1024 + l * 16;
      PIN(eo[k]); PIN(ra[k]); PIN(rb[k]);
    }
  }
  SB();
#pragma unroll
  for (int C = 0; C < 10; ++C) {
    Frags& cur = F[C & 1]; Frags& nxt = F[(C + 1) & 1];
    if (C == 8) { mm(cur); SB(); }
    if (C < 9) RAWBAR();
    SB();
    if (C < 9) rd(nxt, C + 1);
    if (C != 8) mm(cur);
    if (C < 8) {
#pragma unroll
      for (int i = 0; i < 4; ++i) {
        __builtin_amdgcn_sched_group_barrier(0x008, 1, 0); __builtin_amdgcn_sched_group_barrier(0x100, 2, 0);
#pragma unroll
        for (int k = 0; k < 3; ++k) { __builtin_amdgcn_sched_group_barrier(0x008, 1, 0); __builtin_amdgcn_sched_group_barrier(0x100, 1, 0); }
      }
    }
    SB();
  }
  if (QT) {
    u32x4 pa[4];
#pragma unroll
    for (int i = 0; i < 4; ++i) pa[i] = pkfrag(acc[0][i], acc[1][i]);
    SB();
    phi4(pa);
#pragma unroll
    for (int i = 0; i < 4; ++i) { ldsw64(smem, ra[0] + i * 32, u32x2{pa[i][0], pa[i][1]}); ldsw64(smem, ra[0] + 2304 + i * 32, u32x2{pa[i][2], pa[i][3]}); }
    u32x4 o[4];
#pragma unroll
    for (int i = 0; i < 4; ++i) o[i] = ldsr128(smem, rb[0] + i * 1152);
#pragma unroll
    for (int i = 0; i < 4; ++i) *(u32x4*)(ws + OFF_PQ + (size_t)(eo[0] + i * 640)) = o[i];
  } else {
    u32x4 v1 = pkfrag(acc[0][3], acc[1][3]);
    if (chh && l15 == 12) v1 = u32x4{0x3f803f80u, 0x3f803f80u, 0x3f803f80u, 0x3f803f80u};
    const u32 fo = 2 * K1_SLOT + (rg * 8 + 2 * chh) * 1024 + l * 16;
    ldsw128(smem, fo + 4096, pkfrag(acc[0][2], acc[1][2]));
    ldsw128(smem, fo + 5120, v1);
    u32x4 pa[2] = {pkfrag(acc[0][0], acc[1][0]), pkfrag(acc[0][1], acc[1][1])};
    phi4(pa);
    ldsw128(smem, fo, pa[0]);
    ldsw128(smem, fo + 1024, pa[1]);
    __builtin_amdgcn_s_waitcnt(0xC07F);
    LDS_AS u32* cnt = (LDS_AS u32*)((LDS_AS char*)smem + K1_AUX);
    if (l == 0) __hip_atomic_fetch_add(cnt, 1u, __ATOMIC_RELAXED, __HIP_MEMORY_SCOPE_WORKGROUP);
    while (__builtin_amdgcn_readfirstlane(__hip_atomic_load(cnt, __ATOMIC_RELAXED, __HIP_MEMORY_SCOPE_WORKGROUP)) < 8u) {}
    asm volatile("" ::: "memory");
    u32x4 fa[4], fb[4];
    f32x4 r0 = {0.f, 0.f, 0.f, 0.f};
#pragma unroll
    for (int r = 0; r < 4; ++r) { fa[r] = ldsr128(smem, ra[0] + r * 8192); fb[r] = ldsr128(smem, rb[0] + r * 8192); }
    if (w < 4) {
      u32x4 fc[4], fd[4];
      f32x4 r1 = r0;
#pragma unroll
      for (int r = 0; r < 4; ++r) { fc[r] = ldsr128(smem, ra[1] + r * 8192); fd[r] = ldsr128(smem, rb[1] + r * 8192); }
      SB();
#pragma unroll
      for (int r = 0; r < 4; ++r) { r0 = mfma(fa[r], fb[r], r0); r1 = mfma(fc[r], fd[r], r1); }
      if (eo[1] != ~0u) st16p(ws + OFF_KVP, eo[1], r1);
    } else {
      SB();
#pragma unroll
      for (int r = 0; r < 4; ++r) r0 = mfma(fa[r], fb[r], r0);
    }
    if (eo[0] != ~0u) st16p(ws + OFF_KVP, eo[0], r0);
  }
}

template <bool C4>
DI void k1_dft(const char* vb, const u32x4 (&gs)[8], const u32x4 (&xq)[4][3], u32 vo, u32 k01, u32 k2, float tc, float ts, int l) {
  constexpr int NB = 1;
  const u32 kv01[1] = {k01}, kv2[1] = {k2};
  const int g = l >> 4, h = (l >> 3) & 1, j = l & 7;
  const u32 hm = (j & 1) ? 0xffff0000u : 0xffffu;
  const f32x4 z4 = {0.f, 0.f, 0.f, 0.f};
  u32x4 kd[NB][3], tb[NB][2];
  f32x4 t[NB][4], y[NB][4];
#pragma unroll
  for (int u = 0; u < NB; ++u) {
    kd[u][0] = onehot(((j & 1) ? kv01[u] << 16 : kv01[u]) & hm, j >> 1); kd[u][1] = onehot(((j & 1) ? kv01[u] : kv01[u] >> 16) & hm, j >> 1);
    kd[u][2] = onehot(kv2[u] & hm, j >> 1);
  }
  SB();
#pragma unroll
  for (int s = 0; s < 3; ++s)
#pragma unroll
    for (int u = 0; u < NB; ++u)
#pragma unroll
      for (int rt = 0; rt < 4; ++rt) t[u][rt] = mfma(xq[rt][s], kd[u][s], s ? t[u][rt] : z4);
#pragma unroll
  for (int u = 0; u < NB; ++u) { tb[u][0] = pkfrag(t[u][0], t[u][1]); tb[u][1] = pkfrag(t[u][2], t[u][3]); }
#pragma unroll
  for (int k = 0; k < 2; ++k)
#pragma unroll
    for (int u = 0; u < NB; ++u)
#pragma unroll
      for (int mt = 0; mt < 4; ++mt) y[u][mt] = mfma(gs[2 * mt + k], tb[u][k], k ? y[u][mt] : z4);
  SB();
#pragma unroll
  for (int u = 0; u < NB; ++u) {
    float X[8], Y[8]; u32 pk[8];
    const u32 vu = vo;
    if (!C4) {
#pragma unroll
      for (int i = 0; i < 8; ++i) { X[i] = y[u][i >> 1][2 * (i & 1)] - ror8(y[u][i >> 1][2 * (i & 1) + 1]); Y[i] = y[u][i >> 1][2 * (i & 1) + 1] + ror8(y[u][i >> 1][2 * (i & 1)]); }
      float p0 = ror8(y[u][0][0]), p1 = ror8(y[u][0][1]);
      X[0] = g == 0 ? (h ? y[u][0][1] : y[u][0][0]) : X[0]; Y[0] = g == 0 ? (h ? p1 : p0) : Y[0];
      SB();
#pragma unroll
      for (int i = 0; i < 8; ++i) pk[i] = pk2(X[i], Y[i]);
      SB();
#pragma unroll
      for (int i = 0; i < 8; ++i) pk[i] = __builtin_amdgcn_alignbit(pk[i], pk[i], 16 * h);
    } else {
      const float o1 = y[u][0][1];
      if (g == 0) ST4SO(vb, vu, 2048, pk2(o1 * tc, o1 * ts));
      y[u][0][1] = g == 0 ? 0.f : o1;
#pragma unroll
      for (int i = 0; i < 8; ++i) {
        float o0 = y[u][i >> 1][2 * (i & 1)], o2 = y[u][i >> 1][2 * (i & 1) + 1];
        X[i] = o0 * tc - o2 * ts; Y[i] = o0 * ts + o2 * tc;
      }
      SB();
#pragma unroll
      for (int i = 0; i < 8; ++i) pk[i] = pk2(X[i], Y[i]);
    }
    SB();
#pragma unroll
    for (int i = 0; i < 8; ++i) ST4SO(vb, vu, i * 256, pk[i]);
  }
}

__global__ __launch_bounds__(1024) __attribute__((amdgpu_waves_per_eu(4, 4)))
void k1(const u16* __restrict__ x, const u16* __restrict__ wqkv, const u16* __restrict__ wr, const u16* __restrict__ wi,
        const u16* __restrict__ gate, const u16* __restrict__ wo, char* __restrict__ ws) {
  extern __shared__ __attribute__((aligned(16))) char smem[];
  const int tid = threadIdx.x, l = tid & 63, w = __builtin_amdgcn_readfirstlane(tid >> 6);
  const int b = blockIdx.x, xcd = b & 7, bi = b >> 3;
  const int m = (xcd >> 1) * 4 + (bi & 3), nt = (xcd & 1) * 8 + (bi >> 2);
  asm volatile("" :: "s"(x), "s"(wqkv), "s"(wr), "s"(wi), "s"(gate), "s"(wo), "s"(ws));
  if (w == 7) {
    const char* pc; int d0, d1;
    asm volatile("s_getpc_b64 %0\n1:\n\ts_mov_b32 %1, _Z2k1PKtS0_S0_S0_S0_S0_Pc-1b\n\ts_mov_b32 %2, _Z4kendv-1b" : "=s"(pc), "=s"(d0), "=s"(d1));
    const char* a = pc + d0 + (l * 32 + bi) * 128;
    if (l >= 12) a = (const char*)dGsA.v + (bi * 2 + (l & 1)) * 128;
    if (l < 14 && (l >= 12 || a < pc + d1)) l2touch(LDSADDR(smem) + K1_AUX + 16, a);
  }
  if (w >= 8) {
    const int v = w - 8, u = v >> 1;
    int cn = ((nt & 7) * 5 >> 2) + ((m & 3) * 5 >> 1); if (cn >= 10) cn -= 10;
    const int cq = nt >= 11, cbase = cq ? 128 * (nt - 11) : 640 + 60 * nt, cs = cq ? 64 : 640, cmax = nt == 10 ? 4 : 7;
    u32 da[4];
#pragma unroll
    for (int e = 0; e < 4; ++e) {
      const int cbk = (l & 15) ^ (((l >> 4) << 2) | e);
      da[e] = ((v & 1) ? (4 * e + (l >> 4)) * 3840 + ((cbk >> 3) * cs + 8 * min(cbk & 7, cmax)) * 2
                       : (8 * e + (l >> 3)) * 40960 + (((l & 7) ^ ((4 * e + (l >> 4)) & 7)) << 4)) - 1024 * e;
    }
    const char* sa = (v & 1) ? (const char*)wqkv + u * (16 * 3840) + cbase * 2 : (const char*)x + ((u & 1) * 1024 + 2 * m + (u >> 1)) * 1280;
    const int cstep = (v & 1) ? 64 * 3840 : 128;
    const u32 dl = LDSADDR(smem) + u * 4096 + (v & 1) * K1_B;
    int cc = cn;
    auto issue = [&](int slot) {
      dma4(dl + slot * K1_SLOT, sa + cn * cstep, da[0], da[1], da[2], da[3]);
      cn = cn == 9 ? 0 : cn + 1;
    };
    __builtin_amdgcn_s_setprio(2);
    issue(0); issue(1);
    if (l == 0 && v == 0) *(LDS_AS u32*)((LDS_AS char*)smem + K1_AUX) = 0u;
    const int n2s = nt >> 3, bg = nt & 7, g = l >> 4;
    const u32 ca = n2s * 8192 + (l & 15) * 128 + ((bg ^ ((l >> 1) & 7)) << 4);
    u32x4 xq[4][3];
#pragma unroll
    for (int rt = 0; rt < 4; ++rt) xq[rt][0] = xq[rt][1] = xq[rt][2] = u32x4{0u, 0u, 0u, 0u};
    auto step = [&](int C) {
      RAWBAR();
      if (C < 7) issue((C + 3) & 3);
      {
        const u32 o = LDSADDR(smem) + ca + ((C + 1) & 3) * K1_SLOT;
        const unsigned long long mk = (v < 5 ? 0xffffull : 0ull) << (16 * (cc & 3));
        asm volatile("s_mov_b64 exec, %13\n\ts_cmp_lt_u32 %14, 4\n\ts_cbranch_scc0 1f\n\t"
                     "ds_read_b128 %0, %12\n\tds_read_b128 %1, %12 offset:2048\n\tds_read_b128 %2, %12 offset:4096\n\tds_read_b128 %3, %12 offset:6144\n\ts_branch 3f\n"
                     "1:\n\ts_cmp_lt_u32 %14, 8\n\ts_cbranch_scc0 2f\n\t"
                     "ds_read_b128 %4, %12\n\tds_read_b128 %5, %12 offset:2048\n\tds_read_b128 %6, %12 offset:4096\n\tds_read_b128 %7, %12 offset:6144\n\ts_branch 3f\n"
                     "2:\n\tds_read_b128 %8, %12\n\tds_read_b128 %9, %12 offset:2048\n\tds_read_b128 %10, %12 offset:4096\n\tds_read_b128 %11, %12 offset:6144\n"
                     "3:\n\ts_mov_b64 exec, -1"
                     : "+v"(xq[0][0]), "+v"(xq[1][0]), "+v"(xq[2][0]), "+v"(xq[3][0]), "+v"(xq[0][1]), "+v"(xq[1][1]), "+v"(xq[2][1]), "+v"(xq[3][1]),
                       "+v"(xq[0][2]), "+v"(xq[1][2]), "+v"(xq[2][2]), "+v"(xq[3][2]) : "v"(o), "s"(mk), "s"(cc) : "memory", "scc");
      }
      cc = cc == 9 ? 0 : cc + 1;
    };
#pragma unroll 1
    for (int C = -1; C < 2; ++C) { WAITV(4); step(C); }
    dma1(LDSADDR(smem) + K1_GS + v * 1024, (const char*)dGsA.v + v * 1024, l * 16);
    const int h = (l >> 3) & 1, bb = 8 * bg + (l & 7);
    u32 kv01, kv2;
    {
      float c3[3];
#pragma unroll
      for (int s = 0; s < 3; ++s) c3[s] = __builtin_amdgcn_cosf((float)((v + 1) * (64 * (4 * s + g) + bb)) * (1.f / 640.f) - 0.25f * h);
      kv01 = pk2(c3[0], c3[1]); kv2 = pk2(c3[2], c3[2]);
      if (v == 4) kv01 = kv2 = (h & g) ? 0xbf80bf80u : 0x3f803f80u;
    }
    float tc = h ? __builtin_amdgcn_cosf(bb * (1.f / 128.f)) : 1.f, ts = h ? __builtin_amdgcn_sinf(bb * (1.f / 128.f)) : 0.f;
    u32 vo = ((2 * m + n2s) * 8 + bg) * VB_TS + l * 4;
    PIN(kv01); PIN(kv2); PIN(tc); PIN(ts); PIN(vo);
    WAITV(5); step(2);
#pragma unroll 1
    for (int C = 3; C < 7; ++C) { WAITV(4); step(C); }
    {
      const u32 dump = LDSADDR(smem) + K1_AUX + 16;
      const int ci = 8 * ((b & 7) >> 1) + 2 * (b >> 6) + (b & 1), q = ((b >> 5) & 1) ? (ci ? 64 - ci : 32) : ci;
      const u32 wo2 = ((q + 64 * (8 * ((b >> 3) & 3) + (l >> 3))) * 642 + min((l & 7) * 128, 638)) & ~3u;
      const char* ca2 = (const char*)dF64.v + (bi * 4 + (l & 3)) * 128;
      const int id = 25 * b + min((l & 7) * 4, 24), ko = (id * 52429) >> 22;
      const char* wo3 = (const char*)wo + (8 * ko + (l >> 3)) * 1280 + (id - 80 * ko) * 16;
      l2touch(dump, v == 6 ? (const char*)wr + wo2 : (v == 7 ? (const char*)wi + wo2 : (v == 5 ? wo3 : ca2)));
    }
    WAITV(5); step(7);
    WAITV(1); step(8);
    u32x4 gs[8];
#pragma unroll
    for (int f = 0; f < 8; ++f) gs[f] = ldsr128(smem, K1_GS + f * 1024 + l * 16);
    asm volatile("s_waitcnt lgkmcnt(0)" : "+v"(xq[0][0]), "+v"(xq[1][0]), "+v"(xq[2][0]), "+v"(xq[3][0]), "+v"(xq[0][1]), "+v"(xq[1][1]), "+v"(xq[2][1]), "+v"(xq[3][1]),
                 "+v"(xq[0][2]), "+v"(xq[1][2]), "+v"(xq[2][2]), "+v"(xq[3][2]) :: "memory");
    if (v == 5) {
      const int id = 25 * b + min(l, 24), ko = (id * 52429) >> 22, cb = id - 80 * ko, a = cb >> 3, hp = (ko * 205) >> 10, wq = ko - 5 * hp;
#pragma unroll
      for (int i = 0; i < 8; ++i) gs[i] = *(const u32x4*)((const char*)wo + ko * 10240 + cb * 16 + i * 1280);
      u32x4* dst = (u32x4*)(ws + OFF_WOF) + ((((cb & 7) * 5 + (a >> 1)) * 20 + (hp >> 2) * 5 + wq) * 64 + (hp & 3) * 16 + (a & 1) * 8);
#pragma unroll
      for (int jj = 0; jj < 8; ++jj) {
        u32x4 o2;
#pragma unroll
        for (int d = 0; d < 4; ++d) o2[d] = __builtin_amdgcn_perm(gs[2 * d + 1][jj >> 1], gs[2 * d][jj >> 1], (jj & 1) ? 0x07060302u : 0x05040100u);
        dst[jj] = o2;
      }
    }
    asm volatile("" : "+v"(gs[0]), "+v"(gs[1]), "+v"(gs[2]), "+v"(gs[3]), "+v"(gs[4]), "+v"(gs[5]), "+v"(gs[6]), "+v"(gs[7]));
    if (v < 4) k1_dft<false>(ws + OFF_VB, gs, xq, vo + v * 2048, kv01, kv2, tc, ts, l);
    else if (v == 4) k1_dft<true>(ws + OFF_VB, gs, xq, vo + 8192, kv01, kv2, tc, ts, l);
    WAITV(0);
  } else if (nt >= 11) k1_gemm<true>(smem, x, gate, ws, m, nt, w, l);
  else k1_gemm<false>(smem, x, gate, ws, m, nt, w, l);
}

constexpr int K2_V = 0, K2_A = 16384, K2_U = 32768, K2_US = 272, K2_PF = K2_U + 2 * 32 * K2_US, K2_W = K2_PF + 272, WROW = 656, K2_LDS = K2_W + 128 * WROW;
constexpr int K2_NF = 168;
constexpr int K2_NO = 64;

__global__ __launch_bounds__(1024) __attribute__((amdgpu_waves_per_eu(4, 4)))
void k2(const u16* __restrict__ wr, const u16* __restrict__ wi, char* __restrict__ ws) {
  extern __shared__ __attribute__((aligned(16))) char smem[];
  const int tid = threadIdx.x, l = tid & 63, w8 = __builtin_amdgcn_readfirstlane(tid >> 6);
  const int g = l >> 4, l15 = l & 15;
  const int b = blockIdx.x;
  const f32x4 z4 = {0.f, 0.f, 0.f, 0.f};
  asm volatile("" :: "s"(wr), "s"(wi), "s"(ws));
  auto prefetch = [&]() {
    const int i = (b >> 3) * 224 + tid;
    if (tid < 224 && i < 6400) l2touch(LDSADDR(smem) + K2_PF, ws + OFF_WOF + (size_t)i * 128);
    if (tid >= 448 && tid < 451 && (b >> 3) * 3 + tid - 448 < 64) l2touch(LDSADDR(smem) + K2_PF, (const char*)dGsB.v + ((b >> 3) * 3 + tid - 448) * 128);
  };
  if (b < K2_NF) {
    const int sidx = (b >> 5) * 32 + (b & 7) * 4 + ((b >> 3) & 3);
    if (sidx > 160) { prefetch(); WAITV(0); return; }
    const int c = sidx >> 5 > 4 ? 4 : sidx >> 5;
    const int ci = sidx == 160 ? 32 : ((sidx >> 3) & 3) * 8 + (sidx & 3) * 2 + ((sidx >> 2) & 1);
    const int qa = ci, qb = (c < 4 && ci == 0) ? 32 : (64 - ci) & 63;
    LDS_AS u32* cnt = (LDS_AS u32*)((LDS_AS char*)smem + K2_PF + 256);
    if (tid == 0) *cnt = 0u;
    RAWBAR();
    if (w8 >= 8) {
      const int v = w8 - 8;
      u32 vo[11];
#pragma unroll
      for (int i = 0; i < 11; ++i) { const int P = 64 * i + l; vo[i] = (P - 64 * (i & 3)) * 16 + ((P * 1599) >> 16) * (41088 - 656); }
      const char* wsrc = (const char*)((v >> 2) ? wi : wr) + (((((v >> 1) & 1) ? qb : qa) * 642) & ~15) + (v & 1) * (16 * 41088);
      const u32 L = LDSADDR(smem) + K2_W + v * 16 * WROW;
      {
      const int half = v >> 2, w4 = v & 3, q = c == 4 ? ci : (half == 0 ? ci : (ci == 0 ? 32 : 64 - ci));
    {
      const int ri = 16 * (w4 & 1) + l15;
      const int B = w4 < 2 ? q + 64 * ri : 64 * ri, A = (w4 < 2 ? 0 : ri * q) + B * 4 * g;
      float cs[8], sn[8];
#pragma unroll
      for (int j = 0; j < 8; ++j) {
        float rev = (float)((A + B * (j < 4 ? j : 12 + j)) & 2047) * (1.f / 2048.f);
        cs[j] = __builtin_amdgcn_cosf(rev); sn[j] = __builtin_amdgcn_sinf(rev);
      }
      ldsw128(smem, K2_A + v * 2048 + l * 16, u32x4{pk2(cs[0], cs[1]), pk2(cs[2], cs[3]), pk2(cs[4], cs[5]), pk2(cs[6], cs[7])});
      ldsw128(smem, K2_A + v * 2048 + 1024 + l * 16, u32x4{pk2(sn[0], sn[1]), pk2(sn[2], sn[3]), pk2(sn[4], sn[5]), pk2(sn[6], sn[7])});
    }
      __builtin_amdgcn_s_waitcnt(0xC07F);
      if (l == 0) __hip_atomic_fetch_add(cnt, 1u, __ATOMIC_RELAXED, __HIP_MEMORY_SCOPE_WORKGROUP);
      }
      dma4(L, wsrc, vo[0], vo[1], vo[2], vo[3]); dma4(L + 4096, wsrc, vo[4], vo[5], vo[6], vo[7]); dma2(L + 8192, wsrc, vo[8], vo[9]);
      if (l < 16) dma1(L + 10240, wsrc + 2048, vo[10]);
      WAITV(0);
      return;
    }
    const int half = w8 >> 2, w4 = w8 & 3;
    {
      int kc0 = l15 ^ ((8 * w4 + g) & 15), kc1 = kc0 ^ 4;
      u32 v0 = ((8 * w4 + g) * 8 + (kc0 >> 1)) * VB_TS + (kc0 & 1) * 16, v1 = ((8 * w4 + 4 + g) * 8 + (kc1 >> 1)) * VB_TS + (kc1 & 1) * 16 - 1024;
      dma2(LDSADDR(smem) + K2_V + w8 * 2048, ws + OFF_VB + sidx * 64 + half * 32, v0, v1);
    }
    u32x4 tr[4], ti[4];
    dma2(LDSADDR(smem) + K2_U + w8 * 2048, (const char*)dF64.v + w8 * 2048, l * 16, l * 16);
    const int q = c == 4 ? ci : (half == 0 ? ci : (ci == 0 ? 32 : 64 - ci));
    u32 va = K2_V + half * 8192 + l15 * 256 + ((g ^ (l15 & 3)) << 4) + ((l15 >> 2) << 6), aa = K2_A + half * 8192 + l * 16;
    PIN(va); PIN(aa);
    SB();
    const float sc = 2.f / (2048.f * 640.f), s2 = (c == 4 && (ci & 31) == 0) ? 0.5f * sc : sc;
    f32x4 tc = z4, ts = z4;
    if (c == 4) {
#pragma unroll
      for (int i = 0; i < 4; ++i) {
        float rev = (float)(16 * w4 + 4 * g + i) * (1.f / 128.f);
        tc[i] = __builtin_amdgcn_cosf(rev) * s2; ts[i] = __builtin_amdgcn_sinf(rev) * s2;
      }
    }
    const int so = sidx == 160 ? 128 : sidx;
    char* dst = ws + OFF_AP + (size_t)((16 * half + l15) * 8 + 2 * w4 + (g >> 1)) * AP_TS + so * 64 + (g & 1) * 32;
    u32 uw = K2_U + half * (32 * K2_US) + 4 * g * K2_US + (16 * w4 + l15) * 4, ur0 = K2_U + (16 * half + l15) * K2_US + g * 16;
    asm volatile("" : "+v"(tc), "+v"(ts), "+v"(dst), "+v"(uw), "+v"(ur0));
    const int r = c == 4 ? 5 * half : c + 1, kd = r + 10 * (16 * w4 + l15);
    const bool mir = w4 >= 2;
    u32 wg[2], wg0[2];
#pragma unroll
    for (int k = 0; k < 2; ++k) {
      const bool mr = mir != (k == 1);
      const int qs = mr ? (64 - q) & 63 : q, col = k ? (mir ? 320 : 0) : (mir ? 640 - kd : kd);
      wg[k] = K2_W + (qs != qa) * 32 * WROW + ((2 * qs) & 15) + 2 * col + (mr ? ((q == 0 ? 32 : 31) - 19 - 4 * g) * WROW : 4 * g * WROW);
      wg0[k] = (mr && q == 0 && g == 0) ? wg[k] - 13 * WROW : wg[k] + (mr ? 19 * WROW : 0);
      PIN(wg[k]); PIN(wg0[k]);
    }
    WAITV(0);
    __builtin_amdgcn_s_waitcnt(0xC07F);
    if (l == 0) __hip_atomic_fetch_add(cnt, 1u, __ATOMIC_RELAXED, __HIP_MEMORY_SCOPE_WORKGROUP);
    while (__builtin_amdgcn_readfirstlane(__hip_atomic_load(cnt, __ATOMIC_RELAXED, __HIP_MEMORY_SCOPE_WORKGROUP)) < 16u) {}
    asm volatile("" ::: "memory");
    u32x4 vf[2][4], cf[4], sf[4];
#pragma unroll
    for (int ks = 0; ks < 4; ++ks) tr[ks] = ldsr128(smem, K2_U + w4 * 4096 + ks * 1024 + l * 16);
#pragma unroll
    for (int ks = 0; ks < 4; ++ks)
#pragma unroll
      for (int mt = 0; mt < 2; ++mt) vf[mt][ks] = ldsr128(smem, (va ^ (ks << 6)) + mt * 4096);
#pragma unroll
    for (int i = 0; i < 4; ++i) {
      cf[i] = ldsr128(smem, aa + i * 2048); sf[i] = ldsr128(smem, aa + i * 2048 + 1024);
    }
    SB();
    f32x4 Tr[2] = {z4, z4}, Ti[2] = {z4, z4};
#pragma unroll
    for (int ks = 0; ks < 4; ++ks) {
#pragma unroll
      for (int mt = 0; mt < 2; ++mt) Tr[mt] = mfma(vf[mt][ks], tr[ks], Tr[mt]);
#pragma unroll
      for (int i = 0; i < 4; ++i) ti[ks][i] = __builtin_amdgcn_alignbit(tr[ks][i], tr[ks][i], 16) ^ 0x8000u;
      SB();
    }
#pragma unroll
    for (int ks = 0; ks < 4; ++ks)
#pragma unroll
      for (int mt = 0; mt < 2; ++mt) Ti[mt] = mfma(vf[mt][ks], ti[ks], Ti[mt]);
    SB();
    u32x4 br = pkfrag(Tr[0], Tr[1]), bi = pkfrag(Ti[0], Ti[1]), ni = bi ^ 0x80008000u;
    f32x4 zr[2], zn[2];
#pragma unroll
    for (int mt = 0; mt < 2; ++mt) { zr[mt] = mfma(cf[mt], br, z4); zn[mt] = mfma(cf[mt], bi, z4); }
#pragma unroll
    for (int mt = 0; mt < 2; ++mt) { zr[mt] = mfma(sf[mt], ni, zr[mt]); zn[mt] = mfma(sf[mt], br, zn[mt]); }
    SB();
    __builtin_amdgcn_s_barrier();
    asm volatile("" ::: "memory");
    f32x4 wa[2], wb[2];
    {
      u32 ra[8], rb[8];
      auto gather = [&](int s, bool mr) {
#pragma unroll
        for (int k = 0; k < 8; ++k) {
          const int dk = 16 * (k >> 2) + (k & 3);
          const u32 o = k == 0 ? wg0[s] : wg[s] + (mr ? 19 - dk : dk) * WROW;
          ra[k] = ldsr16(smem, o); rb[k] = ldsr16(smem, o + 64 * WROW);
        }
      };
      if (mir) gather(0, true); else gather(0, false);
      prefetch();
      const u32 sg = mir ? 0x80000000u : 0u;
#pragma unroll
      for (int k = 0; k < 8; ++k) { wa[k >> 2][k & 3] = lo2f(ra[k]); wb[k >> 2][k & 3] = __uint_as_float((rb[k] << 16) ^ sg); }
      if (r == 0 && (w4 & 1) == 0) {
        if (mir) gather(1, false); else gather(1, true);
#pragma unroll
        for (int k = 0; k < 8; ++k) {
          wa[k >> 2][k & 3] = l15 == 0 ? 0.5f * (wa[k >> 2][k & 3] + lo2f(ra[k])) : wa[k >> 2][k & 3];
          wb[k >> 2][k & 3] = l15 == 0 ? 0.5f * (wb[k >> 2][k & 3] - __uint_as_float((rb[k] << 16) ^ sg)) : wb[k >> 2][k & 3];
        }
      }
    }
    f32x4 Yr[2], Yi[2];
#pragma unroll
    for (int mt = 0; mt < 2; ++mt) { Yr[mt] = zr[mt] * wa[mt]; Yi[mt] = zr[mt] * wb[mt]; }
    SB();
#pragma unroll
    for (int mt = 0; mt < 2; ++mt) { Yr[mt] += zn[mt] * wb[mt]; Yi[mt] -= zn[mt] * wa[mt]; }
    SB();
    br = pkfrag(Yr[0], Yr[1]); bi = pkfrag(Yi[0], Yi[1]); ni = bi ^ 0x80008000u;
    f32x4 ur[2], ui[2];
#pragma unroll
    for (int mt = 0; mt < 2; ++mt) { ur[mt] = mfma(cf[2 + mt], br, z4); ui[mt] = mfma(cf[2 + mt], bi, z4); }
#pragma unroll
    for (int mt = 0; mt < 2; ++mt) { ur[mt] = mfma(sf[2 + mt], ni, ur[mt]); ui[mt] = mfma(sf[2 + mt], br, ui[mt]); }
    SB();
#pragma unroll
    for (int mt = 0; mt < 2; ++mt)
#pragma unroll
      for (int i = 0; i < 4; ++i) *(LDS_AS u32*)((LDS_AS char*)smem + uw + (16 * mt + i) * K2_US) = pk2(ur[mt][i], ui[mt][i]);
    RAWBAR();
    u32x4 uf[2][4];
#pragma unroll
    for (int ks = 0; ks < 4; ++ks)
#pragma unroll
      for (int cl = 0; cl < 2; ++cl) uf[cl][ks] = ldsr128(smem, ur0 + cl * (32 * K2_US) + ks * 64);
    SB();
    f32x4 Rr[2] = {z4, z4}, Ri[2] = {z4, z4};
#pragma unroll
    for (int ks = 0; ks < 4; ++ks)
#pragma unroll
      for (int cl = 0; cl < 2; ++cl) { Rr[cl] = mfma(tr[ks], uf[cl][ks], Rr[cl]); Ri[cl] = mfma(ti[ks], uf[cl][ks], Ri[cl]); }
    f32x4 e00, e01, e10, e11;
    if (c < 4) {
      f32x4 ar = Rr[0] * sc, ai = Ri[0] * -sc;
      if (ci > 0) { e00 = ar + Rr[1] * sc; e01 = ai - Ri[1] * sc; e10 = ai + Ri[1] * sc; e11 = Rr[1] * sc - ar; }
      else { e00 = ar; e01 = ai; e10 = Rr[1] * sc; e11 = Ri[1] * -sc; }
    } else {
      e00 = Rr[0] * s2; e01 = Rr[1] * tc - Ri[1] * ts;
      if (ci & 31) { e10 = Ri[0] * -s2; e11 = -(Rr[1] * ts + Ri[1] * tc); }
      else { e10 = e00; e11 = e01; }
    }
    if (sidx != 160) *(u32x4*)dst = pkfrag(e00, e01);
    if (sidx != 128) *(u32x4*)(dst + 16) = pkfrag(e10, e11);
  } else {
    if (w8 >= 8) return;
    const int ob = b - K2_NF, hp = ob & 15, n2 = 8 * (ob >> 4) + w8;
    u32x4 s[8];
    const int rh = tid >= 256, rp = tid & 255;
    if (rp < 210) {
      const char* kb = ws + OFF_KVP + hp * 3360;
      const u32 ko = rp * 16 + rh * (8 * 53760);
#pragma unroll
      for (int mm = 0; mm < 8; ++mm) asm volatile("global_load_dwordx4 %0, %1, %2" : "=v"(s[mm]) : "v"(ko), "s"(kb + mm * 53760) : "memory");
    }
    const char* src = ws + OFF_PQ + ((size_t)hp * 2048 + 64 * n2) * 80;
    const u32 pl = 8192 + w8 * 5136;
    dma4(LDSADDR(smem) + pl, src, l * 16, l * 16, l * 16, l * 16);
    dma1(LDSADDR(smem) + pl + 4096, src + 4096, l * 16);
    const int gc = g > 2 ? 2 : g;
    const u32 m0 = g < 3 ? ~0u : 0u, m1 = g < 2 ? ~0u : 0u;
    u32 oo = (n2 * 16 + hp) * 5120 + l * 16;
    u32 a0 = l15 * 48 + gc * 16, a1 = (16 + (l15 & 3)) * 48 + gc * 16, a2 = 960 + gc * 16;
    u32 pr = pl + l15 * 80 + 16 * gc, pw = pl + l15 * 80 + 8 * g, pw2 = pl + l15 * 80 + 32 + 4 * (g & 1), pc = pl + l * 16;
    const int u_ = rp >= 105, in = rp - 105 * u_, e_ = (in * 205) >> 10;
    u32 kw = u_ * 1024 + e_ * 48 + (in - 5 * e_) * 8;
    PIN(oo); PIN(a0); PIN(a1); PIN(a2); PIN(pr); PIN(pw); PIN(pw2); PIN(pc); PIN(kw);
    asm volatile("s_waitcnt vmcnt(5)" : "+v"(s[0]), "+v"(s[1]), "+v"(s[2]), "+v"(s[3]), "+v"(s[4]), "+v"(s[5]), "+v"(s[6]), "+v"(s[7]) :: "memory");
    f32x4 sum = ((__builtin_bit_cast(f32x4, s[0]) + __builtin_bit_cast(f32x4, s[1])) + (__builtin_bit_cast(f32x4, s[2]) + __builtin_bit_cast(f32x4, s[3])))
              + ((__builtin_bit_cast(f32x4, s[4]) + __builtin_bit_cast(f32x4, s[5])) + (__builtin_bit_cast(f32x4, s[6]) + __builtin_bit_cast(f32x4, s[7])));
    if (rh) *(LDS_AS f32x4*)((LDS_AS char*)smem + 2048 + rp * 16) = sum;
    RAWBAR();
    if (!rh && rp < 210) {
      sum += *(const LDS_AS f32x4*)((const LDS_AS char*)smem + 2048 + rp * 16);
      ldsw64(smem, kw, u32x2{pk2(sum[0], sum[1]), pk2(sum[2], sum[3])});
    }
    WAITV(0);
    prefetch();
    u32x4 af[2][3], pf[4][2];
#pragma unroll
    for (int rt = 0; rt < 4; ++rt)
#pragma unroll
      for (int u = 0; u < 2; ++u) {
        u32x2 a = ldsr64(smem, pr + rt * 1280 + 40 * u), bq = ldsr64(smem, pr + rt * 1280 + 40 * u + 8);
        pf[rt][u] = u32x4{a[0], a[1], bq[0], bq[1]};
      }
    RAWBAR();
#pragma unroll
    for (int u = 0; u < 2; ++u) { af[u][2] = ldsr128(smem, a2 + u * 1024); af[u][0] = ldsr128(smem, a0 + u * 1024); af[u][1] = ldsr128(smem, a1 + u * 1024); }
    SB();
    pf[3][1] &= u32x4{~0u, ~0u, m1, m1};
#pragma unroll
    for (int u = 0; u < 2; ++u)
#pragma unroll
      for (int tI = 0; tI < 3; ++tI) af[u][tI] &= u32x4{m0, m0, m1, m1};
    f32x4 n0[4][2], n1[4][2], dn[4][2];
#pragma unroll
    for (int u = 0; u < 2; ++u)
#pragma unroll
      for (int rt = 0; rt < 4; ++rt) dn[rt][u] = mfma(af[u][2], pf[rt][u], z4);
#pragma unroll
    for (int u = 0; u < 2; ++u)
#pragma unroll
      for (int rt = 0; rt < 4; ++rt) { n0[rt][u] = mfma(af[u][0], pf[rt][u], z4); n1[rt][u] = mfma(af[u][1], pf[rt][u], z4); }
    SB();
    float rd[8]; u32 b0[8], b1[8], b2[8];
#pragma unroll
    for (int k = 0; k < 8; ++k) rd[k] = bfr(dn[k & 3][k >> 2][0]) + 1e-6f;
    SB();
#pragma unroll
    for (int k = 0; k < 8; ++k) rd[k] = __builtin_amdgcn_rcpf(bfr(rd[k]));
    SB();
#pragma unroll
    for (int k = 0; k < 8; ++k) {
      const f32x4 a = n0[k & 3][k >> 2], c2 = n1[k & 3][k >> 2];
      b0[k] = pk2(a[0], a[1]); b1[k] = pk2(a[2], a[3]); b2[k] = (g & 1) ? pk2(c2[2], c2[3]) : pk2(c2[0], c2[1]);
    }
    SB();
#pragma unroll
    for (int k = 0; k < 8; ++k) {
      f32x2 x0 = f32x2{lo2f(b0[k]), hi2f(b0[k])} * rd[k], x1 = f32x2{lo2f(b1[k]), hi2f(b1[k])} * rd[k], x2 = f32x2{lo2f(b2[k]), hi2f(b2[k])} * rd[k];
      b0[k] = pk2(x0[0], x0[1]); b1[k] = pk2(x1[0], x1[1]); b2[k] = pk2(x2[0], x2[1]);
    }
#pragma unroll
    for (int k = 0; k < 8; ++k) {
      ldsw64(smem, pw + (k & 3) * 1280 + 40 * (k >> 2), u32x2{b0[k], b1[k]});
      *(LDS_AS u32*)((LDS_AS char*)smem + pw2 + (k & 3) * 1280 + 40 * (k >> 2)) = b2[k];
    }
    u32x4 o[5];
#pragma unroll
    for (int i = 0; i < 5; ++i) o[i] = ldsr128(smem, pc + i * 1024);
    ST16SO(ws + OFF_O2, oo, 0, o[0]); ST16SO(ws + OFF_O2, oo, 1024, o[1]); ST16SO(ws + OFF_O2, oo, 2048, o[2]); ST16SO(ws + OFF_O2, oo, 3072, o[3]);
    ST16SO(ws + OFF_O2 + 4096, oo, 0, o[4]);
  }
  WAITV(0);
}

constexpr int K3_W = 81920, K3_AP = 61440, K3_GS = K3_AP + 10240, K3_CNT = K3_W + 3 * 25600, K3_LDS = K3_CNT + 16;

__global__ __launch_bounds__(1024) __attribute__((amdgpu_waves_per_eu(4, 4)))
void k3(const char* __restrict__ ws, u16* __restrict__ out) {
  extern __shared__ __attribute__((aligned(16))) char smem[];
  const int tid = threadIdx.x, l = tid & 63, w = __builtin_amdgcn_readfirstlane(tid >> 6);
  const int g = l >> 4, l15 = l & 15;
  const int b = blockIdx.x, n2 = 4 * (b & 7) + ((b >> 3) & 3), bg = b >> 5;
  const f32x4 z4 = {0.f, 0.f, 0.f, 0.f};
  asm volatile("" :: "s"(ws), "s"(out));
  if (w >= 10) {
    const int v = w - 10, rot = (bg + (b >> 3)) & 3;
    const u32 lo = l * 16, L = LDSADDR(smem);
    __builtin_amdgcn_s_setprio(2);
    {
      const char* ap = ws + OFF_AP + (size_t)(n2 * 8 + bg) * AP_TS;
      if (v < 5) dma2(L + K3_AP + v * 2048, ap + v * 2048, lo, lo);
      if (v < 5) dma1(L + K3_GS + v * 1024, (const char*)dGsB.v + v * 1024, lo);
      else { dma2(L + K3_GS + 5120, (const char*)dGsB.v + 5120, lo, lo); dma1(L + K3_GS + 7168, (const char*)dGsB.v + 7168, lo); }
    }
    const char* op = ws + OFF_O2 + (size_t)n2 * 81920 + v * 4096;
    const char* wp = ws + OFF_WOF + (size_t)bg * 102400;
    auto issue = [&](int Q) {
      const int Qd = (Q + rot) & 3;
      const u32 lw = L + K3_W + (Q == 3 ? 0 : Q) * 25600;
      if (v < 5) {
        dma4(L + Q * 20480 + v * 4096, op + Qd * 20480, lo, lo, lo, lo);
        dma4(lw + v * 5120, wp + v * 20480 + Qd * 5120, lo, lo, lo, lo);
      } else {
#pragma unroll
        for (int c = 0; c < 5; ++c) dma1(lw + c * 5120 + 4096, wp + c * 20480 + Qd * 5120 + 4096, lo);
      }
    };
#define K3W() do { if (v < 5) WAITV(8); else WAITV(5); __builtin_amdgcn_s_barrier(); } while (0)
    issue(0);
    if (tid == 640) *(LDS_AS u32*)((LDS_AS char*)smem + K3_CNT) = 0u;
    __builtin_amdgcn_s_waitcnt(0xC07F);
    K3W();
    issue(1);
    K3W();
    issue(2);
    K3W();
    issue(3);
    K3W();
    WAITV(0);
    if (l == 0) __hip_atomic_fetch_add((LDS_AS u32*)((LDS_AS char*)smem + K3_CNT), 1u, __ATOMIC_RELAXED, __HIP_MEMORY_SCOPE_WORKGROUP);
    __builtin_amdgcn_s_waitcnt(0xC07F);
  } else {
    const int ga = w < 5, ct = ga ? w : w - 5;
    const u32 oa = g * 5120 + l15 * 80, wa = K3_W + ct * 5120 + l * 16;
    f32x4 acc[4] = {z4, z4, z4, z4};
    auto quarter = [&](int Q) {
      u32x4 of[5][4], wf[5];
#pragma unroll
      for (int wv = 0; wv < 5; ++wv) {
        wf[wv] = ldsr128(smem, wa + (Q == 3 ? 0 : Q) * 25600 + wv * 1024);
        if (wv < 3) {
#pragma unroll
          for (int rt = 0; rt < 4; ++rt) of[wv][rt] = ldsr128(smem, oa + Q * 20480 + rt * 1280 + wv * 16);
        }
      }
      SB();
#pragma unroll
      for (int wv = 0; wv < 2; ++wv)
#pragma unroll
        for (int rt = 0; rt < 4; ++rt) {
          acc[rt] = mfma(wf[wv], of[wv][rt], acc[rt]);
          SB();
          of[3 + wv][rt] = ldsr128(smem, oa + Q * 20480 + rt * 1280 + (3 + wv) * 16);
          SB();
        }
#pragma unroll
      for (int wv = 2; wv < 5; ++wv)
#pragma unroll
        for (int rt = 0; rt < 4; ++rt) acc[rt] = mfma(wf[wv], of[wv][rt], acc[rt]);
      SB();
    };
    if (!ga) {
      __builtin_amdgcn_s_barrier();
      __builtin_amdgcn_s_barrier();
      quarter(0);
      RAWBAR();
      __builtin_amdgcn_s_barrier();
      quarter(2);
#pragma unroll
      for (int rt = 0; rt < 4; ++rt) *(LDS_AS f32x4*)((LDS_AS char*)smem + (ct * 4 + rt) * 1024 + l * 16) = acc[rt];
      __builtin_amdgcn_s_waitcnt(0xC07F);
    } else {
      u32 gv[4];
      const char* gp = ws + OFF_G + (64 * n2 + l15) * 4;
      GLD1(gv[0], gp, 0); GLD1(gv[1], gp, 64); GLD1(gv[2], gp, 128); GLD1(gv[3], gp, 192);
      u32x4 kd[3];
      const int a = 2 * ct + (l15 >> 3), j = l15 & 7, d = 64 * a + 8 * bg + j;
#pragma unroll
      for (int s = 0; s < 3; ++s) {
        int kc = 4 * s + g, c = kc >> 1;
        float rev = c < 4 ? (float)((c + 1) * d) * (1.f / 640.f) : 0.f;
        float v0 = __builtin_amdgcn_cosf(rev), v1 = c < 4 ? __builtin_amdgcn_sinf(rev) : ((a & 1) ? -1.f : 1.f);
        bool on = kc < 10 && (kc & 1) == (j >> 2);
        u32 pp = on ? pk2(v0, v1) : 0u;
        u32 p0 = (pp & 0xffffu) << ((j & 1) * 16), p1 = (pp >> 16) << ((j & 1) * 16);
        kd[s] = (j & 2) ? u32x4{0u, p0, 0u, p1} : u32x4{p0, 0u, p1, 0u};
        PIN(kd[s]);
      }
      u32 ao = K3_AP + ((l15 >> 1) & 1) * 256 + (l15 >> 2) * 64 + (l15 & 1) * 16 + (g & 1) * 32, ao1 = ao + (g >> 1) * 2048;
      u32 oo = ((32 * l15 + n2) * 640 + 64 * (2 * ct + (g >> 1)) + 8 * bg + 4 * (g & 1)) * 2;
      PIN(ao); PIN(ao1); PIN(oo);
      __builtin_amdgcn_s_barrier();
      asm volatile("" ::: "memory");
      u32x4 af[4][3], gs[8];
#pragma unroll
      for (int rt = 0; rt < 4; ++rt) { af[rt][0] = ldsr128(smem, ao1 + rt * 512); af[rt][1] = ldsr128(smem, ao1 + 4096 + rt * 512); af[rt][2] = ldsr128(smem, ao + 8192 + rt * 512); }
#pragma unroll
      for (int f = 0; f < 8; ++f) gs[f] = ldsr128(smem, K3_GS + f * 1024 + l * 16);
      SB();
      f32x4 P[4];
#pragma unroll
      for (int s = 0; s < 3; ++s)
#pragma unroll
        for (int rt = 0; rt < 4; ++rt) P[rt] = mfma(af[rt][s], kd[s], s ? P[rt] : z4);
      u32x4 p0 = pkfrag(P[0], P[1]), p1 = pkfrag(P[2], P[3]);
      SB();
      RAWBAR();
      f32x4 yb[4];
#pragma unroll
      for (int nt = 0; nt < 4; ++nt) yb[nt] = mfma(p0, gs[2 * nt], z4);
#pragma unroll
      for (int nt = 0; nt < 4; ++nt) yb[nt] = mfma(p1, gs[2 * nt + 1], yb[nt]);
      asm volatile("s_waitcnt vmcnt(0)" : "+v"(gv[0]), "+v"(gv[1]), "+v"(gv[2]), "+v"(gv[3]) :: "memory");
      u32 yp[4][2];
#pragma unroll
      for (int nt = 0; nt < 4; ++nt) {
        const float hh = bfr(1.f - __uint_as_float(gv[nt]));
        u32 b0 = pk2(yb[nt][0], yb[nt][1]), b1 = pk2(yb[nt][2], yb[nt][3]);
        yp[nt][0] = pk2(hh * lo2f(b0), hh * hi2f(b0)); yp[nt][1] = pk2(hh * lo2f(b1), hh * hi2f(b1));
        PIN(yp[nt][0]); PIN(yp[nt][1]);
      }
      __builtin_amdgcn_s_barrier();
      asm volatile("" ::: "memory");
      quarter(1);
      __builtin_amdgcn_s_barrier();
      while (__builtin_amdgcn_readfirstlane(__hip_atomic_load((LDS_AS u32*)((LDS_AS char*)smem + K3_CNT), __ATOMIC_RELAXED, __HIP_MEMORY_SCOPE_WORKGROUP)) < 6u) __builtin_amdgcn_s_sleep(1);
      asm volatile("" ::: "memory");
      quarter(3);
      __builtin_amdgcn_s_barrier();
      asm volatile("" ::: "memory");
      f32x4 pb[4];
#pragma unroll
      for (int rt = 0; rt < 4; ++rt) pb[rt] = *(const LDS_AS f32x4*)((const LDS_AS char*)smem + (ct * 4 + rt) * 1024 + l * 16);
      u32 a0[4], a1[4];
#pragma unroll
      for (int rt = 0; rt < 4; ++rt) { f32x4 t = acc[rt] + pb[rt]; a0[rt] = pk2(t[0], t[1]); a1[rt] = pk2(t[2], t[3]); }
      SB();
#pragma unroll
      for (int rt = 0; rt < 4; ++rt) {
        const float gg = __uint_as_float(gv[rt]);
        a0[rt] = pk2(gg * lo2f(a0[rt]), gg * hi2f(a0[rt])); a1[rt] = pk2(gg * lo2f(a1[rt]), gg * hi2f(a1[rt]));
      }
      SB();
#pragma unroll
      for (int rt = 0; rt < 4; ++rt) {
        u32x2 o = {pk2(lo2f(a0[rt]) + lo2f(yp[rt][0]), hi2f(a0[rt]) + hi2f(yp[rt][0])), pk2(lo2f(a1[rt]) + lo2f(yp[rt][1]), hi2f(a1[rt]) + hi2f(yp[rt][1]))};
        *(u32x2*)((char*)out + (size_t)(oo + rt * (512 * 1280))) = o;
      }
    }
  }
}

__global__ void kend() {}

extern "C" void kernel_launch(void* const* d_in, const int* in_sizes, int n_in, void* d_out, int out_size, void* d_ws,
                              size_t ws_size, hipStream_t stream) {
  const u16* x = (const u16*)d_in[0];
  const u16* wqkv = (const u16*)d_in[1];
  const u16* wo = (const u16*)d_in[2];
  const u16* wr = (const u16*)d_in[4];
  const u16* wi = (const u16*)d_in[5];
  const u16* gate = (const u16*)d_in[6];
  char* ws = (char*)d_ws;
  (void)hipFuncSetAttribute((const void*)k1, hipFuncAttributeMaxDynamicSharedMemorySize, K1_LDS);
  (void)hipFuncSetAttribute((const void*)k2, hipFuncAttributeMaxDynamicSharedMemorySize, K2_LDS);
  (void)hipFuncSetAttribute((const void*)k3, hipFuncAttributeMaxDynamicSharedMemorySize, K3_LDS);
  k1<<<256, 1024, K1_LDS, stream>>>(x, wqkv, wr, wi, gate, wo, ws);
  k2<<<K2_NF + K2_NO, 1024, K2_LDS, stream>>>(wr, wi, ws);
  k3<<<256, 1024, K3_LDS, stream>>>(ws, (u16*)d_out);
}
```

```cpp
#include <hip/hip_runtime.h>
#include <stdint.h>

typedef unsigned short u16;
typedef unsigned int u32;
typedef short s16x4 __attribute__((ext_vector_type(4)));
typedef __bf16 bf16x8 __attribute__((ext_vector_type(8)));
typedef __bf16 bf16x2 __attribute__((ext_vector_type(2)));
typedef float f32x4 __attribute__((ext_vector_type(4)));
typedef float f32x2 __attribute__((ext_vector_type(2)));
typedef u32 u32x4 __attribute__((ext_vector_type(4)));
typedef u32 u32x2 __attribute__((ext_vector_type(2)));

#define LDS_AS __attribute__((address_space(3)))
#define DI __device__ __forceinline__
__device__ __forceinline__ void dma1(u32 lds, const void* base, u32 v0) {
  u32 keep;
  asm volatile("s_mov_b32 %0, m0\n\ts_mov_b32 m0, %1\n\ts_nop 4\n\tglobal_load_lds_dwordx4 %2, %3\n\ts_mov_b32 m0, %0"
               : "=&s"(keep) : "s"(lds), "v"(v0), "s"(base) : "memory");
}
__device__ __forceinline__ void dma4(u32 lds, const void* base, u32 v0, u32 v1, u32 v2, u32 v3) {
  u32 keep;
  asm volatile("s_mov_b32 %0, m0\n\ts_mov_b32 m0, %1\n\ts_nop 4\n\tglobal_load_lds_dwordx4 %2, %6\n\t"
               "global_load_lds_dwordx4 %3, %6 offset:1024\n\tglobal_load_lds_dwordx4 %4, %6 offset:2048\n\t"
               "global_load_lds_dwordx4 %5, %6 offset:3072\n\ts_mov_b32 m0, %0"
               : "=&s"(keep) : "s"(lds), "v"(v0), "v"(v1), "v"(v2), "v"(v3), "s"(base) : "memory");
}
__device__ __forceinline__ void dma2(u32 lds, const void* base, u32 v0, u32 v1) {
  u32 keep;
  asm volatile("s_mov_b32 %0, m0\n\ts_mov_b32 m0, %1\n\ts_nop 4\n\tglobal_load_lds_dwordx4 %2, %4\n\t"
               "global_load_lds_dwordx4 %3, %4 offset:1024\n\ts_mov_b32 m0, %0"
               : "=&s"(keep) : "s"(lds), "v"(v0), "v"(v1), "s"(base) : "memory");
}
#define LDSADDR(p) ((u32)(uintptr_t)(LDS_AS char*)(p))
#define SB() __builtin_amdgcn_sched_barrier(0)
#define WAITV(n) asm volatile("s_waitcnt vmcnt(" #n ")" ::: "memory")
#define RAWBAR() do { asm volatile("" ::: "memory"); __builtin_amdgcn_s_waitcnt(0xC07F); __builtin_amdgcn_s_barrier(); asm volatile("" ::: "memory"); } while (0)

constexpr size_t OFF_PQ  = 0;
constexpr size_t OFF_O2  = OFF_PQ + 2621440;
constexpr size_t OFF_KVP = OFF_O2 + 2621440;
constexpr size_t OFF_VB  = OFF_KVP + 860160;
constexpr int    VB_TS   = 10368;
constexpr size_t OFF_AP  = OFF_VB + (size_t)256 * VB_TS;
constexpr int    AP_TS   = 10240;
constexpr size_t OFF_WOF = OFF_AP + (size_t)256 * AP_TS;
constexpr size_t OFF_G   = OFF_WOF + 819200;

constexpr double kPi = 3.14159265358979323846;
constexpr double tcos(double x) {
  double x2 = x * x, term = 1.0, s = 1.0;
  for (int i = 1; i < 14; ++i) { term *= -x2 / ((2 * i - 1) * (2 * i)); s += term; }
  return s;
}
constexpr double cos64(int k) {
  k &= 63; if (k > 32) k = 64 - k;
  if (k == 16) return 0.0;
  return (k < 16) ? tcos(2 * kPi * k / 64) : -tcos(2 * kPi * (32 - k) / 64);
}
constexpr u16 tobf(double v) {
  u32 u = __builtin_bit_cast(u32, (float)v);
  return (u16)((u + 0x7fffu + ((u >> 16) & 1u)) >> 16);
}
struct C64 { u16 c[64]; };
constexpr C64 mkC64() { C64 t{}; for (int i = 0; i < 64; ++i) t.c[i] = tobf(cos64(i)); return t; }
constexpr C64 kC64 = mkC64();
constexpr u16 gsv(int qp, int n1) {
  int i = qp >> 1;
  if (qp == 1) return kC64.c[(32 * n1) & 63];
  return (qp & 1) ? kC64.c[(i * n1 + 48) & 63] : kC64.c[(i * n1) & 63];
}
constexpr int kperm(int s2, int g, int j) { return 32 * s2 + (j < 4 ? 4 * g + j : 12 + 4 * g + j); }
struct T16 { u16 v[4096]; };
struct T32 { u32 v[4096]; };
constexpr T16 mkGsA() { T16 t{};
  for (int f = 0; f < 8; ++f) for (int l = 0; l < 64; ++l) for (int j = 0; j < 8; ++j)
    t.v[(f * 64 + l) * 8 + j] = gsv(16 * (f >> 1) + (l & 15), kperm(f & 1, l >> 4, j));
  return t; }
constexpr T16 mkGsB() { T16 t{};
  for (int f = 0; f < 8; ++f) for (int l = 0; l < 64; ++l) for (int j = 0; j < 8; ++j)
    t.v[(f * 64 + l) * 8 + j] = gsv(kperm(f & 1, l >> 4, j), 16 * (f >> 1) + (l & 15));
  return t; }
constexpr T32 mkF64() { T32 t{};
  for (int f = 0; f < 16; ++f) for (int l = 0; l < 64; ++l) for (int j = 0; j < 4; ++j) {
    int u = 16 * (f >> 2) + (l & 15), v = 4 * (4 * (f & 3) + (l >> 4)) + j;
    t.v[(f * 64 + l) * 4 + j] = (u32)kC64.c[(u * v) & 63] | ((u32)kC64.c[(u * v + 16) & 63] << 16);
  }
  return t; }
__device__ __attribute__((aligned(128))) const T16 dGsA = mkGsA();
__device__ __attribute__((aligned(128))) const T16 dGsB = mkGsB();
__device__ __attribute__((aligned(128))) const T32 dF64 = mkF64();

DI f32x4 mfma(u32x4 a, u32x4 b, f32x4 c) {
  return __builtin_amdgcn_mfma_f32_16x16x32_bf16(__builtin_bit_cast(bf16x8, a), __builtin_bit_cast(bf16x8, b), c, 0, 0, 0);
}
DI u32 pk2(float lo, float hi) { f32x2 v = {lo, hi}; return __builtin_bit_cast(u32, __builtin_convertvector(v, bf16x2)); }
DI float lo2f(u32 p) { return __uint_as_float(p << 16); }
DI float hi2f(u32 p) { return __uint_as_float(p & 0xffff0000u); }
DI float bfr(float x) { return lo2f(pk2(x, x)); }
DI u32x4 pkfrag(f32x4 a, f32x4 b) { u32x4 r = {pk2(a[0], a[1]), pk2(a[2], a[3]), pk2(b[0], b[1]), pk2(b[2], b[3])}; return r; }
DI float ror8(float v) { return __int_as_float(__builtin_amdgcn_update_dpp(0, __float_as_int(v), 0x128, 0xf, 0xf, true)); }
DI u32x4 ldsr128(const char* smem, u32 off) { return *(const LDS_AS u32x4*)((const LDS_AS char*)smem + off); }
DI u32x2 ldstr(const char* smem, u32 off) {
  return __builtin_bit_cast(u32x2, __builtin_amdgcn_ds_read_tr16_b64_v4i16((LDS_AS s16x4*)((LDS_AS char*)smem + off)));
}
DI void l2touch(u32 lds, const void* p) {
  u32 keep;
  asm volatile("s_mov_b32 %0, m0\n\ts_mov_b32 m0, %1\n\ts_nop 0\n\tglobal_load_lds_dword %2, off\n\ts_mov_b32 m0, %0"
               : "=&s"(keep) : "s"(lds), "v"(p) : "memory");
}
DI void st16p(char* b, u32 o, f32x4 v) { *(f32x4*)(b + (size_t)o) = v; }
#define ST16SO(b, o, imm, v) asm volatile("global_store_dwordx4 %0, %1, %2 offset:" #imm " sc1\n\ts_nop 1" :: "v"(o), "v"(v), "s"(b) : "memory")
#define ST4SO(b, o, imm, v) asm volatile("global_store_dword %0, %1, %2 offset:%3 sc1" :: "v"(o), "v"(v), "s"(b), "i"(imm) : "memory")
DI void ldsw128(char* smem, u32 off, u32x4 v) { *(LDS_AS u32x4*)((LDS_AS char*)smem + off) = v; }
DI void ldsw64(char* smem, u32 off, u32x2 v) { *(LDS_AS u32x2*)((LDS_AS char*)smem + off) = v; }
DI u32x2 ldsr64(const char* smem, u32 off) { return *(const LDS_AS u32x2*)((const LDS_AS char*)smem + off); }
DI u32 ldsr16(const char* smem, u32 off) { return *(const LDS_AS u16*)((const LDS_AS char*)smem + off); }
template <int N> DI void phi4(u32x4 (&p)[N]) {
  f32x2 t[4 * N], e[4 * N];
#pragma unroll
  for (int i = 0; i < 4 * N; ++i) t[i] = f32x2{lo2f(p[i >> 2][i & 3]), hi2f(p[i >> 2][i & 3])};
  SB();
#pragma unroll
  for (int i = 0; i < 4 * N; ++i) e[i] = t[i] * 1.44269504088896f;
  SB();
#pragma unroll
  for (int i = 0; i < 4 * N; ++i) {
    e[i][0] = __builtin_amdgcn_fmed3f(__builtin_amdgcn_exp2f(e[i][0]), 0.f, 1.f); e[i][1] = __builtin_amdgcn_fmed3f(__builtin_amdgcn_exp2f(e[i][1]), 0.f, 1.f);
    t[i][0] = __builtin_fmaxf(t[i][0], 0.f); t[i][1] = __builtin_fmaxf(t[i][1], 0.f);
  }
  SB();
  float mn = 1.f;
#pragma unroll
  for (int i = 0; i < 4 * N; ++i) { mn = __builtin_fminf(__builtin_fminf(mn, e[i][0]), e[i][1]); e[i] += t[i]; }
  SB();
#pragma unroll
  for (int i = 0; i < 4 * N; ++i) p[i >> 2][i & 3] = pk2(e[i][0], e[i][1]);
  if (__builtin_amdgcn_ballot_w64(mn < 0.001f)) {
#pragma unroll
    for (int n = 0; n < N; ++n)
#pragma unroll
      for (int i = 0; i < 4; ++i) p[n][i] = pk2(lo2f(p[n][i]) + 1e-6f, hi2f(p[n][i]) + 1e-6f);
  }
}
template <int CTRL> DI float dppf(float v) { return __int_as_float(__builtin_amdgcn_update_dpp(0, __float_as_int(v), CTRL, 0xf, 0xf, true)); }
DI u32x4 onehot(u32 s, int d) { u32x4 r = {d == 0 ? s : 0u, d == 1 ? s : 0u, d == 2 ? s : 0u, d == 3 ? s : 0u}; return r; }

constexpr int K1_SLOT = 16384, K1_B = 65536, K1_AUX = 131072, K1_GS = K1_AUX + 272, K1_LDS = K1_GS + 8192;

struct Frags { u32x4 x[2][2], w[2][4]; };
#define PIN(v) asm volatile("" : "+v"(v))

template <bool QT>
DI void k1_gemm(char* smem, const u16* x, const u16* gate, char* ws, int m, int nt, int w, int l) {
  const int g = l >> 4, l15 = l & 15, rg = w & 3, chh = w >> 2;
  {
    const int r0 = 8 * nt + w, lc = l < 40 ? l : 39;
    const u32x4* gx = (const u32x4*)(x + (32 * (r0 & 63) + 2 * m + (r0 >> 6)) * 640) + lc;
    const u32x4 gxv[2] = {gx[0], gx[40]}, gwv[2] = {((const u32x4*)gate)[lc], ((const u32x4*)gate)[lc + 40]};
    float g0 = 0.f, g1 = 0.f;
#pragma unroll
    for (int i = 0; i < 8; ++i) {
      const u32 xv = gxv[i >> 2][i & 3], wv = gwv[i >> 2][i & 3];
      u32 pr = pk2(lo2f(xv) * lo2f(wv), hi2f(xv) * hi2f(wv));
      g0 += lo2f(pr); g1 += hi2f(pr);
    }
    g0 = l < 40 ? g0 + g1 : 0.f;
    g0 += dppf<0xB1>(g0); g0 += dppf<0x4E>(g0); g0 += dppf<0x124>(g0); g0 += dppf<0x128>(g0);
    float tot = __uint_as_float(__builtin_amdgcn_readlane(__float_as_uint(g0), 0)) + __uint_as_float(__builtin_amdgcn_readlane(__float_as_uint(g0), 16))
              + __uint_as_float(__builtin_amdgcn_readlane(__float_as_uint(g0), 32)) + __uint_as_float(__builtin_amdgcn_readlane(__float_as_uint(g0), 48));
    float gt = bfr(__builtin_amdgcn_rcpf(1.f + __builtin_amdgcn_exp2f(bfr(tot) * -1.44269504088896f)));
    if (l == 0) ((float*)(ws + OFF_G))[128 * m + r0] = gt;
  }
  u32 xa[2], wa[4][2];
  xa[0] = rg * 4096 + l15 * 128 + ((g ^ ((l15 >> 1) & 7)) << 4); xa[1] = xa[0] ^ 64;
#pragma unroll
  for (int t2 = 0; t2 < 2; ++t2) {
    const int q4 = l15 >> 2, swz = (q4 << 2) | ((2 * g + t2) & 3);
    const u32 b0 = K1_B + (8 * g + 4 * t2 + q4) * 256 + (((((l >> 1) & 1) | ((QT ? 8 : 4) * chh)) ^ swz) << 4) + 8 * (l & 1);
#pragma unroll
    for (int i = 0; i < 4; ++i) { wa[i][t2] = b0 ^ ((QT ? i : (i & 1) + 4 * (i >> 1)) << 5); PIN(wa[i][t2]); }
  }
  f32x4 acc[2][4];
#pragma unroll
  for (int rt = 0; rt < 2; ++rt)
#pragma unroll
    for (int i = 0; i < 4; ++i) acc[rt][i] = f32x4{0.f, 0.f, 0.f, 0.f};
  Frags F[2];
  auto rd = [&](Frags& f, int C) {
    const int so = (C & 3) * K1_SLOT;
#pragma unroll
    for (int ks = 0; ks < 2; ++ks) {
#pragma unroll
      for (int rt = 0; rt < 2; ++rt) f.x[ks][rt] = ldsr128(smem, xa[ks] + so + rt * 2048);
#pragma unroll
      for (int i = 0; i < 4; ++i) {
        u32x2 a = ldstr(smem, wa[i][0] + so + ks * 8192), b = ldstr(smem, wa[i][1] + so + ks * 8192);
        f.w[ks][i] = u32x4{a[0], a[1], b[0], b[1]};
      }
    }
  };
  auto mm = [&](const Frags& f) {
#pragma unroll
    for (int ks = 0; ks < 2; ++ks)
#pragma unroll
      for (int i = 0; i < 4; ++i)
#pragma unroll
        for (int rt = 0; rt < 2; ++rt)
          acc[rt][i] = QT ? mfma(f.w[ks][i], f.x[ks][rt], acc[rt][i]) : mfma(f.x[ks][rt], f.w[ks][i], acc[rt][i]);
  };
  RAWBAR();
  rd(F[0], 0);
  SB();
  u32 eo[2], ra[2], rb[2];
  if (QT) {
    const int ck = 16 * (nt - 11) + 8 * chh + (l & 7), hp = (ck * 205) >> 10;
    eo[0] = (hp * 2048 + 128 * m + 32 * rg + (l >> 3)) * 80 + (ck - 5 * hp) * 16;
    ra[0] = 2 * K1_SLOT + chh * K1_B + rg * 4608 + l15 * 144 + g * 8;
    rb[0] = 2 * K1_SLOT + chh * K1_B + rg * 4608 + (l >> 3) * 144 + (l & 7) * 16;
    PIN(eo[0]); PIN(ra[0]); PIN(rb[0]);
  } else {
#pragma unroll
    for (int k = 0; k < 2; ++k) {
      int P = w + 8 * k;
      int pi = (0x103322211100ull >> (4 * P)) & 15, pj = (0x333232121010ull >> (4 * P)) & 15;
      int d = 16 * pi + 4 * g, e = 16 * pj + l15, hd = (d * 205) >> 12, he = (e * 205) >> 12;
      int h = 3 * nt + hd;
      bool ok = P < 12 && d < 60 && h < 32 && (e == 60 || (e < 60 && he == hd));
      int ee = e == 60 ? 20 : e - 20 * he;
      eo[k] = ok ? (((m * 32 + h) * 21 + ee) * 20 + (d - 20 * hd)) * 4 : ~0u;
      ra[k] = 2 * K1_SLOT + pi * 1024 + l * 16; rb[k] = 2 * K1_SLOT + (4 + pj) * 1024 + l * 16;
      PIN(eo[k]); PIN(ra[k]); PIN(rb[k]);
    }
  }
  SB();
#pragma unroll
  for (int C = 0; C < 10; ++C) {
    Frags& cur = F[C & 1]; Frags& nxt = F[(C + 1) & 1];
    if (C == 8) { mm(cur); SB(); }
    if (C < 9) RAWBAR();
    SB();
    if (C < 9) rd(nxt, C + 1);
    if (C != 8) mm(cur);
    if (C < 8) {
#pragma unroll
      for (int i = 0; i < 4; ++i) {
        __builtin_amdgcn_sched_group_barrier(0x008, 1, 0); __builtin_amdgcn_sched_group_barrier(0x100, 2, 0);
#pragma unroll
        for (int k = 0; k < 3; ++k) { __builtin_amdgcn_sched_group_barrier(0x008, 1, 0); __builtin_amdgcn_sched_group_barrier(0x100, 1, 0); }
      }
    }
    SB();
  }
  if (QT) {
    u32x4 pa[4];
#pragma unroll
    for (int i = 0; i < 4; ++i) pa[i] = pkfrag(acc[0][i], acc[1][i]);
    SB();
    phi4(pa);
#pragma unroll
    for (int i = 0; i < 4; ++i) { ldsw64(smem, ra[0] + i * 32, u32x2{pa[i][0], pa[i][1]}); ldsw64(smem, ra[0] + 2304 + i * 32, u32x2{pa[i][2], pa[i][3]}); }
    u32x4 o[4];
#pragma unroll
    for (int i = 0; i < 4; ++i) o[i] = ldsr128(smem, rb[0] + i * 1152);
#pragma unroll
    for (int i = 0; i < 4; ++i) *(u32x4*)(ws + OFF_PQ + (size_t)(eo[0] + i * 640)) = o[i];
  } else {
    u32x4 v1 = pkfrag(acc[0][3], acc[1][3]);
    if (chh && l15 == 12) v1 = u32x4{0x3f803f80u, 0x3f803f80u, 0x3f803f80u, 0x3f803f80u};
    const u32 fo = 2 * K1_SLOT + (rg * 8 + 2 * chh) * 1024 + l * 16;
    ldsw128(smem, fo + 4096, pkfrag(acc[0][2], acc[1][2]));
    ldsw128(smem, fo + 5120, v1);
    u32x4 pa[2] = {pkfrag(acc[0][0], acc[1][0]), pkfrag(acc[0][1], acc[1][1])};
    phi4(pa);
    ldsw128(smem, fo, pa[0]);
    ldsw128(smem, fo + 1024, pa[1]);
    __builtin_amdgcn_s_waitcnt(0xC07F);
    LDS_AS u32* cnt = (LDS_AS u32*)((LDS_AS char*)smem + K1_AUX);
    if (l == 0) __hip_atomic_fetch_add(cnt, 1u, __ATOMIC_RELAXED, __HIP_MEMORY_SCOPE_WORKGROUP);
    while (__builtin_amdgcn_readfirstlane(__hip_atomic_load(cnt, __ATOMIC_RELAXED, __HIP_MEMORY_SCOPE_WORKGROUP)) < 8u) {}
    asm volatile("" ::: "memory");
    u32x4 fa[4], fb[4];
    f32x4 r0 = {0.f, 0.f, 0.f, 0.f};
#pragma unroll
    for (int r = 0; r < 4; ++r) { fa[r] = ldsr128(smem, ra[0] + r * 8192); fb[r] = ldsr128(smem, rb[0] + r * 8192); }
    if (w < 4) {
      u32x4 fc[4], fd[4];
      f32x4 r1 = r0;
#pragma unroll
      for (int r = 0; r < 4; ++r) { fc[r] = ldsr128(smem, ra[1] + r * 8192); fd[r] = ldsr128(smem, rb[1] + r * 8192); }
      SB();
#pragma unroll
      for (int r = 0; r < 4; ++r) { r0 = mfma(fa[r], fb[r], r0); r1 = mfma(fc[r], fd[r], r1); }
      if (eo[1] != ~0u) st16p(ws + OFF_KVP, eo[1], r1);
    } else {
      SB();
#pragma unroll
      for (int r = 0; r < 4; ++r) r0 = mfma(fa[r], fb[r], r0);
    }
    if (eo[0] != ~0u) st16p(ws + OFF_KVP, eo[0], r0);
  }
}

template <bool C4>
DI void k1_dft(const char* vb, const u32x4 (&gs)[8], const u32x4 (&xq)[4][3], u32 vo, u32 k01, u32 k2, float tc, float ts, int l) {
  constexpr int NB = 1;
  const u32 kv01[1] = {k01}, kv2[1] = {k2};
  const int g = l >> 4, h = (l >> 3) & 1, j = l & 7;
  const u32 hm = (j & 1) ? 0xffff0000u : 0xffffu;
  const f32x4 z4 = {0.f, 0.f, 0.f, 0.f};
  u32x4 kd[NB][3], tb[NB][2];
  f32x4 t[NB][4], y[NB][4];
#pragma unroll
  for (int u = 0; u < NB; ++u) {
    kd[u][0] = onehot(((j & 1) ? kv01[u] << 16 : kv01[u]) & hm, j >> 1); kd[u][1] = onehot(((j & 1) ? kv01[u] : kv01[u] >> 16) & hm, j >> 1);
    kd[u][2] = onehot(kv2[u] & hm, j >> 1);
  }
  SB();
#pragma unroll
  for (int s = 0; s < 3; ++s)
#pragma unroll
    for (int u = 0; u < NB; ++u)
#pragma unroll
      for (int rt = 0; rt < 4; ++rt) t[u][rt] = mfma(xq[rt][s], kd[u][s], s ? t[u][rt] : z4);
#pragma unroll
  for (int u = 0; u < NB; ++u) { tb[u][0] = pkfrag(t[u][0], t[u][1]); tb[u][1] = pkfrag(t[u][2], t[u][3]); }
#pragma unroll
  for (int k = 0; k < 2; ++k)
#pragma unroll
    for (int u = 0; u < NB; ++u)
#pragma unroll
      for (int mt = 0; mt < 4; ++mt) y[u][mt] = mfma(gs[2 * mt + k], tb[u][k], k ? y[u][mt] : z4);
  SB();
#pragma unroll
  for (int u = 0; u < NB; ++u) {
    float X[8], Y[8]; u32 pk[8];
    const u32 vu = vo;
    if (!C4) {
#pragma unroll
      for (int i = 0; i < 8; ++i) { X[i] = y[u][i >> 1][2 * (i & 1)] - ror8(y[u][i >> 1][2 * (i & 1) + 1]); Y[i] = y[u][i >> 1][2 * (i & 1) + 1] + ror8(y[u][i >> 1][2 * (i & 1)]); }
      float p0 = ror8(y[u][0][0]), p1 = ror8(y[u][0][1]);
      X[0] = g == 0 ? (h ? y[u][0][1] : y[u][0][0]) : X[0]; Y[0] = g == 0 ? (h ? p1 : p0) : Y[0];
      SB();
#pragma unroll
      for (int i = 0; i < 8; ++i) pk[i] = pk2(X[i], Y[i]);
      SB();
#pragma unroll
      for (int i = 0; i < 8; ++i) pk[i] = __builtin_amdgcn_alignbit(pk[i], pk[i], 16 * h);
    } else {
      const float o1 = y[u][0][1];
      if (g == 0) ST4SO(vb, vu, 2048, pk2(o1 * tc, o1 * ts));
      y[u][0][1] = g == 0 ? 0.f : o1;
#pragma unroll
      for (int i = 0; i < 8; ++i) {
        float o0 = y[u][i >> 1][2 * (i & 1)], o2 = y[u][i >> 1][2 * (i & 1) + 1];
        X[i] = o0 * tc - o2 * ts; Y[i] = o0 * ts + o2 * tc;
      }
      SB();
#pragma unroll
      for (int i = 0; i < 8; ++i) pk[i] = pk2(X[i], Y[i]);
    }
    SB();
#pragma unroll
    for (int i = 0; i < 8; ++i) ST4SO(vb, vu, i * 256, pk[i]);
  }
}

__global__ __launch_bounds__(1024) __attribute__((amdgpu_waves_per_eu(4, 4)))
void k1(const u16* __restrict__ x, const u16* __restrict__ wqkv, const u16* __restrict__ wr, const u16* __restrict__ wi,
        const u16* __restrict__ gate, const u16* __restrict__ wo, char* __restrict__ ws) {
  extern __shared__ __attribute__((aligned(16))) char smem[];
  const int tid = threadIdx.x, l = tid & 63, w = __builtin_amdgcn_readfirstlane(tid >> 6);
  const int b = blockIdx.x, xcd = b & 7, bi = b >> 3;
  const int m = (xcd >> 1) * 4 + (bi & 3), nt = (xcd & 1) * 8 + (bi >> 2);
  asm volatile("" :: "s"(x), "s"(wqkv), "s"(wr), "s"(wi), "s"(gate), "s"(wo), "s"(ws));
  if (w == 7) {
    const char* pc; int d0, d1;
    asm volatile("s_getpc_b64 %0\n1:\n\ts_mov_b32 %1, _Z2k1PKtS0_S0_S0_S0_S0_Pc-1b\n\ts_mov_b32 %2, _Z4kendv-1b" : "=s"(pc), "=s"(d0), "=s"(d1));
    const char* a = pc + d0 + (l * 32 + bi) * 128;
    if (l >= 12) a = (const char*)dGsA.v + (bi * 2 + (l & 1)) * 128;
    if (l < 14 && (l >= 12 || a < pc + d1)) l2touch(LDSADDR(smem) + K1_AUX + 16, a);
  }
  if (w >= 8) {
    const int v = w - 8, u = v >> 1;
    int cn = ((nt & 7) * 5 >> 2) + ((m & 3) * 5 >> 1); if (cn >= 10) cn -= 10;
    const int cq = nt >= 11, cbase = cq ? 128 * (nt - 11) : 640 + 60 * nt, cs = cq ? 64 : 640, cmax = nt == 10 ? 4 : 7;
    u32 da[4];
#pragma unroll
    for (int e = 0; e < 4; ++e) {
      const int cbk = (l & 15) ^ (((l >> 4) << 2) | e);
      da[e] = ((v & 1) ? (4 * e + (l >> 4)) * 3840 + ((cbk >> 3) * cs + 8 * min(cbk & 7, cmax)) * 2
                       : (8 * e + (l >> 3)) * 40960 + (((l & 7) ^ ((4 * e + (l >> 4)) & 7)) << 4)) - 1024 * e;
    }
    const char* sa = (v & 1) ? (const char*)wqkv + u * (16 * 3840) + cbase * 2 : (const char*)x + ((u & 1) * 1024 + 2 * m + (u >> 1)) * 1280;
    const int cstep = (v & 1) ? 64 * 3840 : 128;
    const u32 dl = LDSADDR(smem) + u * 4096 + (v & 1) * K1_B;
    int cc = cn;
    auto issue = [&](int slot) {
      dma4(dl + slot * K1_SLOT, sa + cn * cstep, da[0], da[1], da[2], da[3]);
      cn = cn == 9 ? 0 : cn + 1;
    };
    __builtin_amdgcn_s_setprio(2);
    issue(0); issue(1);
    if (l == 0 && v == 0) *(LDS_AS u32*)((LDS_AS char*)smem + K1_AUX) = 0u;
    const int n2s = nt >> 3, bg = nt & 7, g = l >> 4;
    const u32 ca = n2s * 8192 + (l & 15) * 128 + ((bg ^ ((l >> 1) & 7)) << 4);
    u32x4 xq[4][3];
#pragma unroll
    for (int rt = 0; rt < 4; ++rt) xq[rt][0] = xq[rt][1] = xq[rt][2] = u32x4{0u, 0u, 0u, 0u};
    auto step = [&](int C) {
      RAWBAR();
      if (C < 7) issue((C + 3) & 3);
      {
        const u32 o = LDSADDR(smem) + ca + ((C + 1) & 3) * K1_SLOT;
        const unsigned long long mk = (v < 5 ? 0xffffull : 0ull) << (16 * (cc & 3));
        asm volatile("s_mov_b64 exec, %13\n\ts_cmp_lt_u32 %14, 4\n\ts_cbranch_scc0 1f\n\t"
                     "ds_read_b128 %0, %12\n\tds_read_b128 %1, %12 offset:2048\n\tds_read_b128 %2, %12 offset:4096\n\tds_read_b128 %3, %12 offset:6144\n\ts_branch 3f\n"
                     "1:\n\ts_cmp_lt_u32 %14, 8\n\ts_cbranch_scc0 2f\n\t"
                     "ds_read_b128 %4, %12\n\tds_read_b128 %5, %12 offset:2048\n\tds_read_b128 %6, %12 offset:4096\n\tds_read_b128 %7, %12 offset:6144\n\ts_branch 3f\n"
                     "2:\n\tds_read_b128 %8, %12\n\tds_read_b128 %9, %12 offset:2048\n\tds_read_b128 %10, %12 offset:4096\n\tds_read_b128 %11, %12 offset:6144\n"
                     "3:\n\ts_mov_b64 exec, -1"
                     : "+v"(xq[0][0]), "+v"(xq[1][0]), "+v"(xq[2][0]), "+v"(xq[3][0]), "+v"(xq[0][1]), "+v"(xq[1][1]), "+v"(xq[2][1]), "+v"(xq[3][1]),
                       "+v"(xq[0][2]), "+v"(xq[1][2]), "+v"(xq[2][2]), "+v"(xq[3][2]) : "v"(o), "s"(mk), "s"(cc) : "memory", "scc");
      }
      cc = cc == 9 ? 0 : cc + 1;
    };
#pragma unroll 1
    for (int C = -1; C < 2; ++C) { WAITV(4); step(C); }
    dma1(LDSADDR(smem) + K1_GS + v * 1024, (const char*)dGsA.v + v * 1024, l * 16);
    const int h = (l >> 3) & 1, bb = 8 * bg + (l & 7);
    u32 kv01, kv2;
    {
      float c3[3];
#pragma unroll
      for (int s = 0; s < 3; ++s) c3[s] = __builtin_amdgcn_cosf((float)((v + 1) * (64 * (4 * s + g) + bb)) * (1.f / 640.f) - 0.25f * h);
      kv01 = pk2(c3[0], c3[1]); kv2 = pk2(c3[2], c3[2]);
      if (v == 4) kv01 = kv2 = (h & g) ? 0xbf80bf80u : 0x3f803f80u;
    }
    float tc = h ? __builtin_amdgcn_cosf(bb * (1.f / 128.f)) : 1.f, ts = h ? __builtin_amdgcn_sinf(bb * (1.f / 128.f)) : 0.f;
    u32 vo = ((2 * m + n2s) * 8 + bg) * VB_TS + l * 4;
    PIN(kv01); PIN(kv2); PIN(tc); PIN(ts); PIN(vo);
    WAITV(5); step(2);
#pragma unroll 1
    for (int C = 3; C < 7; ++C) { WAITV(4); step(C); }
    {
      const u32 dump = LDSADDR(smem) + K1_AUX + 16;
      const int ci = 8 * ((b & 7) >> 1) + 2 * (b >> 6) + (b & 1), q = ((b >> 5) & 1) ? (ci ? 64 - ci : 32) : ci;
      const u32 wo2 = ((q + 64 * (8 * ((b >> 3) & 3) + (l >> 3))) * 642 + min((l & 7) * 128, 638)) & ~3u;
      const char* ca2 = (const char*)dF64.v + (bi * 4 + (l & 3)) * 128;
      const int id = 25 * b + min((l & 7) * 4, 24), ko = (id * 52429) >> 22;
      const char* wo3 = (const char*)wo + (8 * ko + (l >> 3)) * 1280 + (id - 80 * ko) * 16;
      l2touch(dump, v == 6 ? (const char*)wr + wo2 : (v == 7 ? (const char*)wi + wo2 : (v == 5 ? wo3 : ca2)));
    }
    WAITV(5); step(7);
    WAITV(1); step(8);
    u32x4 gs[8];
#pragma unroll
    for (int f = 0; f < 8; ++f) gs[f] = ldsr128(smem, K1_GS + f * 1024 + l * 16);
    asm volatile("s_waitcnt lgkmcnt(0)" : "+v"(xq[0][0]), "+v"(xq[1][0]), "+v"(xq[2][0]), "+v"(xq[3][0]), "+v"(xq[0][1]), "+v"(xq[1][1]), "+v"(xq[2][1]), "+v"(xq[3][1]),
                 "+v"(xq[0][2]), "+v"(xq[1][2]), "+v"(xq[2][2]), "+v"(xq[3][2]) :: "memory");
    if (v == 5) {
      const int id = 25 * b + min(l, 24), ko = (id * 52429) >> 22, cb = id - 80 * ko, a = cb >> 3, hp = (ko * 205) >> 10, wq = ko - 5 * hp;
#pragma unroll
      for (int i = 0; i < 8; ++i) gs[i] = *(const u32x4*)((const char*)wo + ko * 10240 + cb * 16 + i * 1280);
      u32x4* dst = (u32x4*)(ws + OFF_WOF) + ((((cb & 7) * 5 + (a >> 1)) * 20 + (hp >> 2) * 5 + wq) * 64 + (hp & 3) * 16 + (a & 1) * 8);
#pragma unroll
      for (int jj = 0; jj < 8; ++jj) {
        u32x4 o2;
#pragma unroll
        for (int d = 0; d < 4; ++d) o2[d] = __builtin_amdgcn_perm(gs[2 * d + 1][jj >> 1], gs[2 * d][jj >> 1], (jj & 1) ? 0x07060302u : 0x05040100u);
        dst[jj] = o2;
      }
    }
    asm volatile("" : "+v"(gs[0]), "+v"(gs[1]), "+v"(gs[2]), "+v"(gs[3]), "+v"(gs[4]), "+v"(gs[5]), "+v"(gs[6]), "+v"(gs[7]));
    if (v < 4) k1_dft<false>(ws + OFF_VB, gs, xq, vo + v * 2048, kv01, kv2, tc, ts, l);
    else if (v == 4) k1_dft<true>(ws + OFF_VB, gs, xq, vo + 8192, kv01, kv2, tc, ts, l);
    WAITV(0);
  } else if (nt >= 11) k1_gemm<true>(smem, x, gate, ws, m, nt, w, l);
  else k1_gemm<false>(smem, x, gate, ws, m, nt, w, l);
}

constexpr int K2_V = 0, K2_A = 16384, K2_U = 32768, K2_US = 272, K2_PF = K2_U + 2 * 32 * K2_US, K2_W = K2_PF + 272, WROW = 656, K2_LDS = K2_W + 128 * WROW;
constexpr int K2_NF = 168;
constexpr int K2_NO = 64;

__global__ __launch_bounds__(1024) __attribute__((amdgpu_waves_per_eu(4, 4)))
void k2(const u16* __restrict__ wr, const u16* __restrict__ wi, char* __restrict__ ws) {
  extern __shared__ __attribute__((aligned(16))) char smem[];
  const int tid = threadIdx.x, l = tid & 63, w8 = __builtin_amdgcn_readfirstlane(tid >> 6);
  const int g = l >> 4, l15 = l & 15;
  const int b = blockIdx.x;
  const f32x4 z4 = {0.f, 0.f, 0.f, 0.f};
  asm volatile("" :: "s"(wr), "s"(wi), "s"(ws));
  auto prefetch = [&]() {
    const int i = (b >> 3) * 224 + tid;
    if (tid < 224 && i < 6400) l2touch(LDSADDR(smem) + K2_PF, ws + OFF_WOF + (size_t)i * 128);
    if (tid >= 448 && tid < 451 && (b >> 3) * 3 + tid - 448 < 64) l2touch(LDSADDR(smem) + K2_PF, (const char*)dGsB.v + ((b >> 3) * 3 + tid - 448) * 128);
  };
  if (b < K2_NF) {
    const int sidx = (b >> 5) * 32 + (b & 7) * 4 + ((b >> 3) & 3);
    if (sidx > 160) { prefetch(); WAITV(0); return; }
    const int c = sidx >> 5 > 4 ? 4 : sidx >> 5;
    const int ci = sidx == 160 ? 32 : ((sidx >> 3) & 3) * 8 + (sidx & 3) * 2 + ((sidx >> 2) & 1);
    const int qa = ci, qb = (c < 4 && ci == 0) ? 32 : (64 - ci) & 63;
    LDS_AS u32* cnt = (LDS_AS u32*)((LDS_AS char*)smem + K2_PF + 256);
    if (tid == 0) *cnt = 0u;
    RAWBAR();
    if (w8 >= 8) {
      const int v = w8 - 8;
      u32 vo[11];
#pragma unroll
      for (int i = 0; i < 11; ++i) { const int P = 64 * i + l; vo[i] = (P - 64 * (i & 3)) * 16 + ((P * 1599) >> 16) * (41088 - 656); }
      const char* wsrc = (const char*)((v >> 2) ? wi : wr) + (((((v >> 1) & 1) ? qb : qa) * 642) & ~15) + (v & 1) * (16 * 41088);
      const u32 L = LDSADDR(smem) + K2_W + v * 16 * WROW;
      {
      const int half = v >> 2, w4 = v & 3, q = c == 4 ? ci : (half == 0 ? ci : (ci == 0 ? 32 : 64 - ci));
    {
      const int ri = 16 * (w4 & 1) + l15;
      const int B = w4 < 2 ? q + 64 * ri : 64 * ri, A = (w4 < 2 ? 0 : ri * q) + B * 4 * g;
      float cs[8], sn[8];
#pragma unroll
      for (int j = 0; j < 8; ++j) {
        float rev = (float)((A + B * (j < 4 ? j : 12 + j)) & 2047) * (1.f / 2048.f);
        cs[j] = __builtin_amdgcn_cosf(rev); sn[j] = __builtin_amdgcn_sinf(rev);
      }
      ldsw128(smem, K2_A + v * 2048 + l * 16, u32x4{pk2(cs[0], cs[1]), pk2(cs[2], cs[3]), pk2(cs[4], cs[5]), pk2(cs[6], cs[7])});
      ldsw128(smem, K2_A + v * 2048 + 1024 + l * 16, u32x4{pk2(sn[0], sn[1]), pk2(sn[2], sn[3]), pk2(sn[4], sn[5]), pk2(sn[6], sn[7])});
    }
      __builtin_amdgcn_s_waitcnt(0xC07F);
      if (l == 0) __hip_atomic_fetch_add(cnt, 1u, __ATOMIC_RELAXED, __HIP_MEMORY_SCOPE_WORKGROUP);
      }
      dma4(L, wsrc, vo[0], vo[1], vo[2], vo[3]); dma4(L + 4096, wsrc, vo[4], vo[5], vo[6], vo[7]); dma2(L + 8192, wsrc, vo[8], vo[9]);
      if (l < 16) dma1(L + 10240, wsrc + 2048, vo[10]);
      WAITV(0);
      return;
    }
    const int half = w8 >> 2, w4 = w8 & 3;
    {
      int kc0 = l15 ^ ((8 * w4 + g) & 15), kc1 = kc0 ^ 4;
      u32 v0 = ((8 * w4 + g) * 8 + (kc0 >> 1)) * VB_TS + (kc0 & 1) * 16, v1 = ((8 * w4 + 4 + g) * 8 + (kc1 >> 1)) * VB_TS + (kc1 & 1) * 16 - 1024;
      dma2(LDSADDR(smem) + K2_V + w8 * 2048, ws + OFF_VB + sidx * 64 + half * 32, v0, v1);
    }
    u32x4 tr[4], ti[4];
    dma2(LDSADDR(smem) + K2_U + w8 * 2048, (const char*)dF64.v + w8 * 2048, l * 16, l * 16);
    const int q = c == 4 ? ci : (half == 0 ? ci : (ci == 0 ? 32 : 64 - ci));
    u32 va = K2_V + half * 8192 + l15 * 256 + ((g ^ (l15 & 3)) << 4) + ((l15 >> 2) << 6), aa = K2_A + half * 8192 + l * 16;
    PIN(va); PIN(aa);
    SB();
    const float sc = 2.f / (2048.f * 640.f), s2 = (c == 4 && (ci & 31) == 0) ? 0.5f * sc : sc;
    f32x4 tc = z4, ts = z4;
    if (c == 4) {
#pragma unroll
      for (int i = 0; i < 4; ++i) {
        float rev = (float)(16 * w4 + 4 * g + i) * (1.f / 128.f);
        tc[i] = __builtin_amdgcn_cosf(rev) * s2; ts[i] = __builtin_amdgcn_sinf(rev) * s2;
      }
    }
    const int so = sidx == 160 ? 128 : sidx;
    char* dst = ws + OFF_AP + (size_t)((16 * half + l15) * 8 + 2 * w4 + (g >> 1)) * AP_TS + so * 64 + (g & 1) * 32;
    u32 uw = K2_U + half * (32 * K2_US) + 4 * g * K2_US + (16 * w4 + l15) * 4, ur0 = K2_U + (16 * half + l15) * K2_US + g * 16;
    asm volatile("" : "+v"(tc), "+v"(ts), "+v"(dst), "+v"(uw), "+v"(ur0));
    const int r = c == 4 ? 5 * half : c + 1, kd = r + 10 * (16 * w4 + l15);
    const bool mir = w4 >= 2;
    u32 wg[2], wg0[2];
#pragma unroll
    for (int k = 0; k < 2; ++k) {
      const bool mr = mir != (k == 1);
      const int qs = mr ? (64 - q) & 63 : q, col = k ? (mir ? 320 : 0) : (mir ? 640 - kd : kd);
      wg[k] = K2_W + (qs != qa) * 32 * WROW + ((2 * qs) & 15) + 2 * col + (mr ? ((q == 0 ? 32 : 31) - 19 - 4 * g) * WROW : 4 * g * WROW);
      wg0[k] = (mr && q == 0 && g == 0) ? wg[k] - 13 * WROW : wg[k] + (mr ? 19 * WROW : 0);
      PIN(wg[k]); PIN(wg0[k]);
    }
    WAITV(0);
    __builtin_amdgcn_s_waitcnt(0xC07F);
    if (l == 0) __hip_atomic_fetch_add(cnt, 1u, __ATOMIC_RELAXED, __HIP_MEMORY_SCOPE_WORKGROUP);
    while (__builtin_amdgcn_readfirstlane(__hip_atomic_load(cnt, __ATOMIC_RELAXED, __HIP_MEMORY_SCOPE_WORKGROUP)) < 16u) {}
    asm volatile("" ::: "memory");
    u32x4 vf[2][4], cf[4], sf[4];
#pragma unroll
    for (int ks = 0; ks < 4; ++ks) tr[ks] = ldsr128(smem, K2_U + w4 * 4096 + ks * 1024 + l * 16);
#pragma unroll
    for (int ks = 0; ks < 4; ++ks)
#pragma unroll
      for (int mt = 0; mt < 2; ++mt) vf[mt][ks] = ldsr128(smem, (va ^ (ks << 6)) + mt * 4096);
#pragma unroll
    for (int i = 0; i < 4; ++i) {
      cf[i] = ldsr128(smem, aa + i * 2048); sf[i] = ldsr128(smem, aa + i * 2048 + 1024);
    }
    SB();
    f32x4 Tr[2] = {z4, z4}, Ti[2] = {z4, z4};
#pragma unroll
    for (int ks = 0; ks < 4; ++ks) {
#pragma unroll
      for (int mt = 0; mt < 2; ++mt) Tr[mt] = mfma(vf[mt][ks], tr[ks], Tr[mt]);
#pragma unroll
      for (int i = 0; i < 4; ++i) ti[ks][i] = __builtin_amdgcn_alignbit(tr[ks][i], tr[ks][i], 16) ^ 0x8000u;
      SB();
    }
#pragma unroll
    for (int ks = 0; ks < 4; ++ks)
#pragma unroll
      for (int mt = 0; mt < 2; ++mt) Ti[mt] = mfma(vf[mt][ks], ti[ks], Ti[mt]);
    SB();
    u32x4 br = pkfrag(Tr[0], Tr[1]), bi = pkfrag(Ti[0], Ti[1]), ni = bi ^ 0x80008000u;
    f32x4 zr[2], zn[2];
#pragma unroll
    for (int mt = 0; mt < 2; ++mt) { zr[mt] = mfma(cf[mt], br, z4); zn[mt] = mfma(cf[mt], bi, z4); }
#pragma unroll
    for (int mt = 0; mt < 2; ++mt) { zr[mt] = mfma(sf[mt], ni, zr[mt]); zn[mt] = mfma(sf[mt], br, zn[mt]); }
    SB();
    __builtin_amdgcn_s_barrier();
    asm volatile("" ::: "memory");
    f32x4 wa[2], wb[2];
    {
      u32 ra[8], rb[8];
      auto gather = [&](int s, bool mr) {
#pragma unroll
        for (int k = 0; k < 8; ++k) {
          const int dk = 16 * (k >> 2) + (k & 3);
          const u32 o = k == 0 ? wg0[s] : wg[s] + (mr ? 19 - dk : dk) * WROW;
          ra[k] = ldsr16(smem, o); rb[k] = ldsr16(smem, o + 64 * WROW);
        }
      };
      if (mir) gather(0, true); else gather(0, false);
      prefetch();
      const u32 sg = mir ? 0x80000000u : 0u;
#pragma unroll
      for (int k = 0; k < 8; ++k) { wa[k >> 2][k & 3] = lo2f(ra[k]); wb[k >> 2][k & 3] = __uint_as_float((rb[k] << 16) ^ sg); }
      if (r == 0 && (w4 & 1) == 0) {
        if (mir) gather(1, false); else gather(1, true);
#pragma unroll
        for (int k = 0; k < 8; ++k) {
          wa[k >> 2][k & 3] = l15 == 0 ? 0.5f * (wa[k >> 2][k & 3] + lo2f(ra[k])) : wa[k >> 2][k & 3];
          wb[k >> 2][k & 3] = l15 == 0 ? 0.5f * (wb[k >> 2][k & 3] - __uint_as_float((rb[k] << 16) ^ sg)) : wb[k >> 2][k & 3];
        }
      }
    }
    f32x4 Yr[2], Yi[2];
#pragma unroll
    for (int mt = 0; mt < 2; ++mt) { Yr[mt] = zr[mt] * wa[mt]; Yi[mt] = zr[mt] * wb[mt]; }
    SB();
#pragma unroll
    for (int mt = 0; mt < 2; ++mt) { Yr[mt] += zn[mt] * wb[mt]; Yi[mt] -= zn[mt] * wa[mt]; }
    SB();
    br = pkfrag(Yr[0], Yr[1]); bi = pkfrag(Yi[0], Yi[1]); ni = bi ^ 0x80008000u;
    f32x4 ur[2], ui[2];
#pragma unroll
    for (int mt = 0; mt < 2; ++mt) { ur[mt] = mfma(cf[2 + mt], br, z4); ui[mt] = mfma(cf[2 + mt], bi, z4); }
#pragma unroll
    for (int mt = 0; mt < 2; ++mt) { ur[mt] = mfma(sf[2 + mt], ni, ur[mt]); ui[mt] = mfma(sf[2 + mt], br, ui[mt]); }
    SB();
#pragma unroll
    for (int mt = 0; mt < 2; ++mt)
#pragma unroll
      for (int i = 0; i < 4; ++i) *(LDS_AS u32*)((LDS_AS char*)smem + uw + (16 * mt + i) * K2_US) = pk2(ur[mt][i], ui[mt][i]);
    RAWBAR();
    u32x4 uf[2][4];
#pragma unroll
    for (int ks = 0; ks < 4; ++ks)
#pragma unroll
      for (int cl = 0; cl < 2; ++cl) uf[cl][ks] = ldsr128(smem, ur0 + cl * (32 * K2_US) + ks * 64);
    SB();
    f32x4 Rr[2] = {z4, z4}, Ri[2] = {z4, z4};
#pragma unroll
    for (int ks = 0; ks < 4; ++ks)
#pragma unroll
      for (int cl = 0; cl < 2; ++cl) { Rr[cl] = mfma(tr[ks], uf[cl][ks], Rr[cl]); Ri[cl] = mfma(ti[ks], uf[cl][ks], Ri[cl]); }
    f32x4 e00, e01, e10, e11;
    if (c < 4) {
      f32x4 ar = Rr[0] * sc, ai = Ri[0] * -sc;
      if (ci > 0) { e00 = ar + Rr[1] * sc; e01 = ai - Ri[1] * sc; e10 = ai + Ri[1] * sc; e11 = Rr[1] * sc - ar; }
      else { e00 = ar; e01 = ai; e10 = Rr[1] * sc; e11 = Ri[1] * -sc; }
    } else {
      e00 = Rr[0] * s2; e01 = Rr[1] * tc - Ri[1] * ts;
      if (ci & 31) { e10 = Ri[0] * -s2; e11 = -(Rr[1] * ts + Ri[1] * tc); }
      else { e10 = e00; e11 = e01; }
    }
    if (sidx != 160) *(u32x4*)dst = pkfrag(e00, e01);
    if (sidx != 128) *(u32x4*)(dst + 16) = pkfrag(e10, e11);
  } else {
    if (w8 >= 8) return;
    const int ob = b - K2_NF, hp = ob & 15, n2 = 8 * (ob >> 4) + w8;
    u32x4 s[8];
    const int rh = tid >= 256, rp = tid & 255;
    if (rp < 210) {
      const char* kb = ws + OFF_KVP + hp * 3360;
      const u32 ko = rp * 16 + rh * (8 * 53760);
#pragma unroll
      for (int mm = 0; mm < 8; ++mm) asm volatile("global_load_dwordx4 %0, %1, %2" : "=v"(s[mm]) : "v"(ko), "s"(kb + mm * 53760) : "memory");
    }
    const char* src = ws + OFF_PQ + ((size_t)hp * 2048 + 64 * n2) * 80;
    const u32 pl = 8192 + w8 * 5136;
    dma4(LDSADDR(smem) + pl, src, l * 16, l * 16, l * 16, l * 16);
    dma1(LDSADDR(smem) + pl + 4096, src + 4096, l * 16);
    const int gc = g > 2 ? 2 : g;
    const u32 m0 = g < 3 ? ~0u : 0u, m1 = g < 2 ? ~0u : 0u;
    u32 oo = (n2 * 16 + hp) * 5120 + l * 16;
    u32 a0 = l15 * 48 + gc * 16, a1 = (16 + (l15 & 3)) * 48 + gc * 16, a2 = 960 + gc * 16;
    u32 pr = pl + l15 * 80 + 16 * gc, pw = pl + l15 * 80 + 8 * g, pw2 = pl + l15 * 80 + 32 + 4 * (g & 1), pc = pl + l * 16;
    const int u_ = rp >= 105, in = rp - 105 * u_, e_ = (in * 205) >> 10;
    u32 kw = u_ * 1024 + e_ * 48 + (in - 5 * e_) * 8;
    PIN(oo); PIN(a0); PIN(a1); PIN(a2); PIN(pr); PIN(pw); PIN(pw2); PIN(pc); PIN(kw);
    asm volatile("s_waitcnt vmcnt(5)" : "+v"(s[0]), "+v"(s[1]), "+v"(s[2]), "+v"(s[3]), "+v"(s[4]), "+v"(s[5]), "+v"(s[6]), "+v"(s[7]) :: "memory");
    f32x4 sum = ((__builtin_bit_cast(f32x4, s[0]) + __builtin_bit_cast(f32x4, s[1])) + (__builtin_bit_cast(f32x4, s[2]) + __builtin_bit_cast(f32x4, s[3])))
              + ((__builtin_bit_cast(f32x4, s[4]) + __builtin_bit_cast(f32x4, s[5])) + (__builtin_bit_cast(f32x4, s[6]) + __builtin_bit_cast(f32x4, s[7])));
    if (rh) *(LDS_AS f32x4*)((LDS_AS char*)smem + 2048 + rp * 16) = sum;
    RAWBAR();
    if (!rh && rp < 210) {
      sum += *(const LDS_AS f32x4*)((const LDS_AS char*)smem + 2048 + rp * 16);
      ldsw64(smem, kw, u32x2{pk2(sum[0], sum[1]), pk2(sum[2], sum[3])});
    }
    WAITV(0);
    prefetch();
    u32x4 af[2][3], pf[4][2];
#pragma unroll
    for (int rt = 0; rt < 4; ++rt)
#pragma unroll
      for (int u = 0; u < 2; ++u) {
        u32x2 a = ldsr64(smem, pr + rt * 1280 + 40 * u), bq = ldsr64(smem, pr + rt * 1280 + 40 * u + 8);
        pf[rt][u] = u32x4{a[0], a[1], bq[0], bq[1]};
      }
    RAWBAR();
#pragma unroll
    for (int u = 0; u < 2; ++u) { af[u][2] = ldsr128(smem, a2 + u * 1024); af[u][0] = ldsr128(smem, a0 + u * 1024); af[u][1] = ldsr128(smem, a1 + u * 1024); }
    SB();
    pf[3][1] &= u32x4{~0u, ~0u, m1, m1};
#pragma unroll
    for (int u = 0; u < 2; ++u)
#pragma unroll
      for (int tI = 0; tI < 3; ++tI) af[u][tI] &= u32x4{m0, m0, m1, m1};
    f32x4 n0[4][2], n1[4][2], dn[4][2];
#pragma unroll
    for (int u = 0; u < 2; ++u)
#pragma unroll
      for (int rt = 0; rt < 4; ++rt) dn[rt][u] = mfma(af[u][2], pf[rt][u], z4);
#pragma unroll
    for (int u = 0; u < 2; ++u)
#pragma unroll
      for (int rt = 0; rt < 4; ++rt) { n0[rt][u] = mfma(af[u][0], pf[rt][u], z4); n1[rt][u] = mfma(af[u][1], pf[rt][u], z4); }
    SB();
    float rd[8]; u32 b0[8], b1[8], b2[8];
#pragma unroll
    for (int k = 0; k < 8; ++k) rd[k] = bfr(dn[k & 3][k >> 2][0]) + 1e-6f;
    SB();
#pragma unroll
    for (int k = 0; k < 8; ++k) rd[k] = __builtin_amdgcn_rcpf(bfr(rd[k]));
    SB();
#pragma unroll
    for (int k = 0; k < 8; ++k) {
      const f32x4 a = n0[k & 3][k >> 2], c2 = n1[k & 3][k >> 2];
      b0[k] = pk2(a[0], a[1]); b1[k] = pk2(a[2], a[3]); b2[k] = (g & 1) ? pk2(c2[2], c2[3]) : pk2(c2[0], c2[1]);
    }
    SB();
#pragma unroll
    for (int k = 0; k < 8; ++k) {
      f32x2 x0 = f32x2{lo2f(b0[k]), hi2f(b0[k])} * rd[k], x1 = f32x2{lo2f(b1[k]), hi2f(b1[k])} * rd[k], x2 = f32x2{lo2f(b2[k]), hi2f(b2[k])} * rd[k];
      b0[k] = pk2(x0[0], x0[1]); b1[k] = pk2(x1[0], x1[1]); b2[k] = pk2(x2[0], x2[1]);
    }
#pragma unroll
    for (int k = 0; k < 8; ++k) {
      ldsw64(smem, pw + (k & 3) * 1280 + 40 * (k >> 2), u32x2{b0[k], b1[k]});
      *(LDS_AS u32*)((LDS_AS char*)smem + pw2 + (k & 3) * 1280 + 40 * (k >> 2)) = b2[k];
    }
    u32x4 o[5];
#pragma unroll
    for (int i = 0; i < 5; ++i) o[i] = ldsr128(smem, pc + i * 1024);
    ST16SO(ws + OFF_O2, oo, 0, o[0]); ST16SO(ws + OFF_O2, oo, 1024, o[1]); ST16SO(ws + OFF_O2, oo, 2048, o[2]); ST16SO(ws + OFF_O2, oo, 3072, o[3]);
    ST16SO(ws + OFF_O2 + 4096, oo, 0, o[4]);
  }
  WAITV(0);
}

constexpr int K3_W = 81920, K3_AP = 61440, K3_GS = K3_AP + 10240, K3_YP = K3_W + 3 * 25600, K3_LDS = K3_YP + 5120;

__global__ __launch_bounds__(1024) __attribute__((amdgpu_waves_per_eu(4, 4)))
void k3(const char* __restrict__ ws, u16* __restrict__ out) {
  extern __shared__ __attribute__((aligned(16))) char smem[];
  const int tid = threadIdx.x, l = tid & 63, w = __builtin_amdgcn_readfirstlane(tid >> 6);
  const int g = l >> 4, l15 = l & 15;
  const int b = blockIdx.x, n2 = 4 * (b & 7) + ((b >> 3) & 3), bg = b >> 5;
  const f32x4 z4 = {0.f, 0.f, 0.f, 0.f};
  asm volatile("" :: "s"(ws), "s"(out));
  if (w >= 10) {
    const int v = w - 10, rot = (bg + (b >> 3)) & 3;
    const u32 lo = l * 16, L = LDSADDR(smem);
    __builtin_amdgcn_s_setprio(2);
    {
      const char* ap = ws + OFF_AP + (size_t)(n2 * 8 + bg) * AP_TS;
      if (v < 5) dma2(L + K3_AP + v * 2048, ap + v * 2048, lo, lo);
      if (v < 5) dma1(L + K3_GS + v * 1024, (const char*)dGsB.v + v * 1024, lo);
      else { dma2(L + K3_GS + 5120, (const char*)dGsB.v + 5120, lo, lo); dma1(L + K3_GS + 7168, (const char*)dGsB.v + 7168, lo); }
    }
    const char* op = ws + OFF_O2 + (size_t)n2 * 81920 + v * 4096;
    const char* wp = ws + OFF_WOF + (size_t)bg * 102400;
    auto issue = [&](int Q) {
      const int Qd = (Q + rot) & 3;
      const u32 lw = L + K3_W + (Q == 3 ? 0 : Q) * 25600;
      if (v < 5) {
        dma4(L + Q * 20480 + v * 4096, op + Qd * 20480, lo, lo, lo, lo);
        dma4(lw + v * 5120, wp + v * 20480 + Qd * 5120, lo, lo, lo, lo);
      } else {
#pragma unroll
        for (int c = 0; c < 5; ++c) dma1(lw + c * 5120 + 4096, wp + c * 20480 + Qd * 5120 + 4096, lo);
      }
    };
#define K3W() do { if (v < 5) WAITV(8); else WAITV(5); __builtin_amdgcn_s_barrier(); } while (0)
    issue(0);
    K3W();
    issue(1);
    K3W();
    issue(2);
    K3W();
    issue(3);
    K3W();
    WAITV(0);
  } else {
    const int ga = w < 5, ct = ga ? w : w - 5;
    const u32 oa = g * 5120 + l15 * 80, wa = K3_W + ct * 5120 + l * 16;
    f32x4 acc[4] = {z4, z4, z4, z4};
    u32 gv[4];
#pragma unroll
    for (int rt = 0; rt < 4; ++rt) gv[rt] = ((const u32*)(ws + OFF_G))[64 * n2 + 16 * rt + l15];
    u32 oo = ((32 * l15 + n2) * 640 + 64 * (2 * ct + (g >> 1)) + 8 * bg + 4 * (g & 1)) * 2;
    const u32 xo = ct * 4096 + l * 16;
    PIN(oo);
    auto finish = [&](int R0, const u32x4& yq) {
      u32x4 of[5][2], wf[5];
      f32x4 pb[2];
#pragma unroll
      for (int wv = 0; wv < 5; ++wv) {
        wf[wv] = ldsr128(smem, wa + wv * 1024);
#pragma unroll
        for (int r = 0; r < 2; ++r) of[wv][r] = ldsr128(smem, oa + 61440 + (R0 + r) * 1280 + wv * 16);
      }
#pragma unroll
      for (int r = 0; r < 2; ++r) pb[r] = *(const LDS_AS f32x4*)((const LDS_AS char*)smem + xo + (R0 + r) * 1024);
      SB();
#pragma unroll
      for (int wv = 0; wv < 5; ++wv)
#pragma unroll
        for (int r = 0; r < 2; ++r) acc[R0 + r] = mfma(wf[wv], of[wv][r], acc[R0 + r]);
#pragma unroll
      for (int r = 0; r < 2; ++r) {
        const f32x4 t = acc[R0 + r] + pb[r];
        const float gg = __uint_as_float(gv[R0 + r]);
        u32 a0 = pk2(t[0], t[1]), a1 = pk2(t[2], t[3]);
        a0 = pk2(gg * lo2f(a0), gg * hi2f(a0)); a1 = pk2(gg * lo2f(a1), gg * hi2f(a1));
        const u32x2 o = {pk2(lo2f(a0) + lo2f(yq[2 * r]), hi2f(a0) + hi2f(yq[2 * r])), pk2(lo2f(a1) + lo2f(yq[2 * r + 1]), hi2f(a1) + hi2f(yq[2 * r + 1]))};
        *(u32x2*)((char*)out + (size_t)(oo + (R0 + r) * (512 * 1280))) = o;
      }
    };
    auto quarter = [&](int Q) {
      u32x4 of[5][4], wf[5];
#pragma unroll
      for (int wv = 0; wv < 5; ++wv) {
        wf[wv] = ldsr128(smem, wa + (Q == 3 ? 0 : Q) * 25600 + wv * 1024);
        if (wv < 3) {
#pragma unroll
          for (int rt = 0; rt < 4; ++rt) of[wv][rt] = ldsr128(smem, oa + Q * 20480 + rt * 1280 + wv * 16);
        }
      }
      SB();
#pragma unroll
      for (int wv = 0; wv < 2; ++wv)
#pragma unroll
        for (int rt = 0; rt < 4; ++rt) {
          acc[rt] = mfma(wf[wv], of[wv][rt], acc[rt]);
          SB();
          of[3 + wv][rt] = ldsr128(smem, oa + Q * 20480 + rt * 1280 + (3 + wv) * 16);
          SB();
        }
#pragma unroll
      for (int wv = 2; wv < 5; ++wv)
#pragma unroll
        for (int rt = 0; rt < 4; ++rt) acc[rt] = mfma(wf[wv], of[wv][rt], acc[rt]);
      SB();
    };
    if (!ga) {
      __builtin_amdgcn_s_barrier();
      __builtin_amdgcn_s_barrier();
      quarter(0);
      RAWBAR();
      __builtin_amdgcn_s_barrier();
      quarter(2);
#pragma unroll
      for (int rt = 0; rt < 2; ++rt) *(LDS_AS f32x4*)((LDS_AS char*)smem + xo + rt * 1024) = acc[rt];
      RAWBAR();
      finish(2, ldsr128(smem, K3_YP + ct * 1024 + l * 16));
    } else {
      u32x4 kd[3];
      const int a = 2 * ct + (l15 >> 3), j = l15 & 7, d = 64 * a + 8 * bg + j;
#pragma unroll
      for (int s = 0; s < 3; ++s) {
        int kc = 4 * s + g, c = kc >> 1;
        float rev = c < 4 ? (float)((c + 1) * d) * (1.f / 640.f) : 0.f;
        float v0 = __builtin_amdgcn_cosf(rev), v1 = c < 4 ? __builtin_amdgcn_sinf(rev) : ((a & 1) ? -1.f : 1.f);
        bool on = kc < 10 && (kc & 1) == (j >> 2);
        u32 pp = on ? pk2(v0, v1) : 0u;
        u32 p0 = (pp & 0xffffu) << ((j & 1) * 16), p1 = (pp >> 16) << ((j & 1) * 16);
        kd[s] = (j & 2) ? u32x4{0u, p0, 0u, p1} : u32x4{p0, 0u, p1, 0u};
        PIN(kd[s]);
      }
      u32 ao = K3_AP + ((l15 >> 1) & 1) * 256 + (l15 >> 2) * 64 + (l15 & 1) * 16 + (g & 1) * 32, ao1 = ao + (g >> 1) * 2048;
      PIN(ao); PIN(ao1);
      __builtin_amdgcn_s_barrier();
      asm volatile("" ::: "memory");
      u32x4 af[4][3], gs[8];
#pragma unroll
      for (int rt = 0; rt < 4; ++rt) { af[rt][0] = ldsr128(smem, ao1 + rt * 512); af[rt][1] = ldsr128(smem, ao1 + 4096 + rt * 512); af[rt][2] = ldsr128(smem, ao + 8192 + rt * 512); }
#pragma unroll
      for (int f = 0; f < 8; ++f) gs[f] = ldsr128(smem, K3_GS + f * 1024 + l * 16);
      SB();
      f32x4 P[4];
#pragma unroll
      for (int s = 0; s < 3; ++s)
#pragma unroll
        for (int rt = 0; rt < 4; ++rt) P[rt] = mfma(af[rt][s], kd[s], s ? P[rt] : z4);
      u32x4 p0 = pkfrag(P[0], P[1]), p1 = pkfrag(P[2], P[3]);
      SB();
      RAWBAR();
      f32x4 yb[4];
#pragma unroll
      for (int nt = 0; nt < 4; ++nt) yb[nt] = mfma(p0, gs[2 * nt], z4);
#pragma unroll
      for (int nt = 0; nt < 4; ++nt) yb[nt] = mfma(p1, gs[2 * nt + 1], yb[nt]);
      u32 yp[4][2];
#pragma unroll
      for (int nt = 0; nt < 4; ++nt) {
        const float hh = bfr(1.f - __uint_as_float(gv[nt]));
        u32 b0 = pk2(yb[nt][0], yb[nt][1]), b1 = pk2(yb[nt][2], yb[nt][3]);
        yp[nt][0] = pk2(hh * lo2f(b0), hh * hi2f(b0)); yp[nt][1] = pk2(hh * lo2f(b1), hh * hi2f(b1));
        PIN(yp[nt][0]); PIN(yp[nt][1]);
      }
      __builtin_amdgcn_s_barrier();
      asm volatile("" ::: "memory");
      ldsw128(smem, K3_YP + ct * 1024 + l * 16, u32x4{yp[2][0], yp[2][1], yp[3][0], yp[3][1]});
      quarter(1);
      __builtin_amdgcn_s_barrier();
#pragma unroll
      for (int rt = 2; rt < 4; ++rt) *(LDS_AS f32x4*)((LDS_AS char*)smem + xo + rt * 1024) = acc[rt];
      RAWBAR();
      finish(0, u32x4{yp[0][0], yp[0][1], yp[1][0], yp[1][1]});
    }
  }
}

__global__ void kend() {}

extern "C" void kernel_launch(void* const* d_in, const int* in_sizes, int n_in, void* d_out, int out_size, void* d_ws,
                              size_t ws_size, hipStream_t stream) {
  const u16* x = (const u16*)d_in[0];
  const u16* wqkv = (const u16*)d_in[1];
  const u16* wo = (const u16*)d_in[2];
  const u16* wr = (const u16*)d_in[4];
  const u16* wi = (const u16*)d_in[5];
  const u16* gate = (const u16*)d_in[6];
  char* ws = (char*)d_ws;
  (void)hipFuncSetAttribute((const void*)k1, hipFuncAttributeMaxDynamicSharedMemorySize, K1_LDS);
  (void)hipFuncSetAttribute((const void*)k2, hipFuncAttributeMaxDynamicSharedMemorySize, K2_LDS);
  (void)hipFuncSetAttribute((const void*)k3, hipFuncAttributeMaxDynamicSharedMemorySize, K3_LDS);
  k1<<<256, 1024, K1_LDS, stream>>>(x, wqkv, wr, wi, gate, wo, ws);
  k2<<<K2_NF + K2_NO, 1024, K2_LDS, stream>>>(wr, wi, ws);
  k3<<<256, 1024, K3_LDS, stream>>>(ws, (u16*)d_out);
}
```
